```python
import torch

def _softmax(x, axis=-1):
    return torch.softmax(x, dim=axis)
def _gelu(x):
    return torch.nn.functional.gelu(x, approximate="tanh")
def _silu(x):
    return torch.nn.functional.silu(x)
def _sigmoid(x):
    return torch.sigmoid(x)
def _layernorm(x, axis=-1, eps=1e-5):
    m = x.mean(dim=axis, keepdim=True)
    v = x.var(dim=axis, keepdim=True, unbiased=False)
    return (x - m) / torch.sqrt(v + eps)
def _scatter_add(tgt, idx, src, axis=0):
    return tgt.clone().index_add_(axis, idx, src)
def _segment_sum(src, seg, n):
    out = torch.zeros((n,) + src.shape[1:], dtype=src.dtype)
    return out.index_add_(0, seg, src)
def _pad(x, pw):
    flat = tuple(v for pair in reversed(pw) for v in pair)
    return torch.nn.functional.pad(x, flat)

kernel_name = 'sequential_preLN__spectral_fnet__glu_gated_glu__prompt'

def setup_inputs():

    _w = (lambda a: (lambda t: t.to(torch.bfloat16) if t.is_floating_point() else t)(torch.as_tensor(a)))
    def _A_setup():
        B, S, D = 1, 2048, 576
        ns = torch.arange(S)[:, None] * torch.arange(S)[None, :]
        nd = torch.arange(D)[:, None] * torch.arange(D)[None, :]
        Ws_re = torch.cos(-2.0 * torch.pi * ns / S).to(float)
        Ws_im = torch.sin(-2.0 * torch.pi * ns / S).to(float)
        Wd_re = torch.cos(-2.0 * torch.pi * nd / D).to(float)
        Wd_im = torch.sin(-2.0 * torch.pi * nd / D).to(float)
        return {
            "x": _w((lambda *s: torch.randn(*s, dtype=torch.bfloat16) * 0.05)(B, S, D)),
            "dft_seq_re": _w(Ws_re),
            "dft_seq_im": _w(Ws_im),
            "dft_hid_re": _w(Wd_re),
            "dft_hid_im": _w(Wd_im),
        }
    _A_in = _A_setup()

    def _B_setup():
        B, S, D, F = 1, 2048, 576, 4608
        return {
            "x": _w((lambda *s: torch.randn(*s, dtype=torch.bfloat16) * 0.05)(B, S, D)),
            "w_gate": _w((lambda *s: torch.randn(*s, dtype=torch.bfloat16) * 1.6)(D, F)),
            "w_up": _w((lambda *s: torch.randn(*s, dtype=torch.bfloat16) * 0.4)(D, F)),
            "w_down": _w((lambda *s: torch.randn(*s, dtype=torch.bfloat16) * 0.4)(F, D)),
        }
    _B_in = _B_setup()

    return {
        "A_x": _A_in["x"],
        "A_dft_seq_re": _A_in["dft_seq_re"],
        "A_dft_seq_im": _A_in["dft_seq_im"],
        "A_dft_hid_re": _A_in["dft_hid_re"],
        "A_dft_hid_im": _A_in["dft_hid_im"],
        "B_x": _B_in["x"],
        "B_w_gate": _B_in["w_gate"],
        "B_w_up": _B_in["w_up"],
        "B_w_down": _B_in["w_down"],
    }

def reference(A_x, A_dft_seq_re, A_dft_seq_im, A_dft_hid_re, A_dft_hid_im, B_x, B_w_gate, B_w_up, B_w_down):
    def _A_ref(x, dft_seq_re, dft_seq_im, dft_hid_re, dft_hid_im):
        a = torch.einsum("bsd,ed->bse", x, dft_hid_re)
        b = torch.einsum("bsd,ed->bse", x, dft_hid_im)
        y = torch.einsum("ts,bse->bte", dft_seq_re, a) - torch.einsum("ts,bse->bte", dft_seq_im, b)
        return y

    def _B_ref(x, w_gate, w_up, w_down):
        g = torch.einsum("bsd,df->bsf", x, w_gate)
        u = torch.einsum("bsd,df->bsf", x, w_up)
        h = _sigmoid(g) * u
        return torch.einsum("bsf,fd->bsd", h, w_down)

    x = A_x
    h = x + _A_ref(_layernorm(x, axis=-1), A_dft_seq_re, A_dft_seq_im, A_dft_hid_re, A_dft_hid_im)
    h2 = h + _B_ref(_layernorm(h, axis=-1), B_w_gate, B_w_up, B_w_down)
    return h2
```

```cpp
#include <hip/hip_runtime.h>
#include <hip/hip_bf16.h>
typedef unsigned short u16;
using bf16x8 = __attribute__((ext_vector_type(8))) short;
using f32x4 = __attribute__((ext_vector_type(4))) float;

__device__ __forceinline__ float bf2f(u16 v){ return __uint_as_float(((unsigned)v)<<16); }
__device__ __forceinline__ u16 f2bf(float f){ __hip_bfloat16 b = __float2bfloat16(f); return *reinterpret_cast<u16*>(&b); }

constexpr int S=2048, D=576, F=4608;

__global__ void ln_kernel(const u16* __restrict__ x, u16* __restrict__ o){
  int row = blockIdx.x; int lane = threadIdx.x;
  float v[9]; float s=0;
  for(int i=0;i<9;i++){ v[i]=bf2f(x[row*D+lane+64*i]); s+=v[i]; }
  for(int m=32;m>=1;m>>=1) s+=__shfl_xor(s,m);
  float mean=s/D; float q=0;
  for(int i=0;i<9;i++){ float d=v[i]-mean; q+=d*d; }
  for(int m=32;m>=1;m>>=1) q+=__shfl_xor(q,m);
  float r=rsqrtf(q/D+1e-5f);
  for(int i=0;i<9;i++) o[row*D+lane+64*i]=f2bf((v[i]-mean)*r);
}

__device__ __forceinline__ void nt_tile(const u16* A,int lda,const u16* B,int ldb,int K,int m0,int n0,f32x4 acc[2][2],float sgn){
  int lane=threadIdx.x&63; int fr=lane&15,fq=lane>>4;
  for(int k=0;k<K;k+=32){
    bf16x8 a[2],b[2];
    for(int i=0;i<2;i++){ a[i]=*(const bf16x8*)(A+(size_t)(m0+i*16+fr)*lda+k+fq*8); b[i]=*(const bf16x8*)(B+(size_t)(n0+i*16+fr)*ldb+k+fq*8);}
    if(sgn<0){ for(int i=0;i<2;i++) for(int j=0;j<8;j++) a[i][j]^=(short)0x8000; }
    for(int i=0;i<2;i++)for(int j=0;j<2;j++) acc[i][j]=__builtin_amdgcn_mfma_f32_16x16x32_bf16(a[i],b[j],acc[i][j],0,0,0);
  }
}
__device__ __forceinline__ void nn_tile(const u16* A,int lda,const u16* B,int ldb,int K,int m0,int n0,f32x4 acc[2][2]){
  int lane=threadIdx.x&63; int fr=lane&15,fq=lane>>4;
  for(int k=0;k<K;k+=32){
    bf16x8 a[2],b[2];
    for(int i=0;i<2;i++){ a[i]=*(const bf16x8*)(A+(size_t)(m0+i*16+fr)*lda+k+fq*8);
      for(int j=0;j<8;j++) b[i][j]=(short)B[(size_t)(k+fq*8+j)*ldb+n0+i*16+fr]; }
    for(int i=0;i<2;i++)for(int j=0;j<2;j++) acc[i][j]=__builtin_amdgcn_mfma_f32_16x16x32_bf16(a[i],b[j],acc[i][j],0,0,0);
  }
}

__global__ void k_gemm1(const u16* Hr,const u16* Hi,const u16* ln1,u16* abT){
  int w=blockIdx.x*4+(threadIdx.x>>6); int mt=w/(S/32), nt=w%(S/32);
  int m0=mt*32,n0=nt*32; const u16* H=(m0<D)?Hr:Hi; int mm=(m0<D)?m0:m0-D;
  f32x4 acc[2][2]={};
  nt_tile(H,D,ln1,D,D,mm,n0,acc,1.f);
  int lane=threadIdx.x&63; int fr=lane&15,fq=lane>>4;
  for(int i=0;i<2;i++)for(int j=0;j<2;j++)for(int r=0;r<4;r++) abT[(size_t)(m0+i*16+fq*4+r)*S+n0+j*16+fr]=f2bf(acc[i][j][r]);
}
__global__ void k_gemm2(const u16* Cr,const u16* Ci,const u16* abT,const u16* x,u16* h){
  int w=blockIdx.x*4+(threadIdx.x>>6); int mt=w/(D/32), nt=w%(D/32);
  int m0=mt*32,n0=nt*32;
  f32x4 acc[2][2]={};
  nt_tile(Cr,S,abT,S,S,m0,n0,acc,1.f);
  nt_tile(Ci,S,abT+(size_t)D*S,S,S,m0,n0,acc,-1.f);
  int lane=threadIdx.x&63; int fr=lane&15,fq=lane>>4;
  for(int i=0;i<2;i++)for(int j=0;j<2;j++)for(int r=0;r<4;r++){ size_t o=(size_t)(m0+i*16+fq*4+r)*D+n0+j*16+fr; h[o]=f2bf(acc[i][j][r]+bf2f(x[o])); }
}
__global__ void k_gemm3(const u16* ln2,const u16* Wg,const u16* Wu,u16* hh){
  int w=blockIdx.x*4+(threadIdx.x>>6); int mt=w/(F/32), nt=w%(F/32);
  int m0=mt*32,n0=nt*32;
  f32x4 g[2][2]={},u[2][2]={};
  nn_tile(ln2,D,Wg,F,D,m0,n0,g);
  nn_tile(ln2,D,Wu,F,D,m0,n0,u);
  int lane=threadIdx.x&63; int fr=lane&15,fq=lane>>4;
  for(int i=0;i<2;i++)for(int j=0;j<2;j++)for(int r=0;r<4;r++){ size_t o=(size_t)(m0+i*16+fq*4+r)*F+n0+j*16+fr; float sg=1.f/(1.f+__expf(-g[i][j][r])); hh[o]=f2bf(sg*u[i][j][r]); }
}
__global__ void k_gemm4(const u16* hh,const u16* Wd,const u16* h,u16* out){
  int w=blockIdx.x*4+(threadIdx.x>>6); int mt=w/(D/32), nt=w%(D/32);
  int m0=mt*32,n0=nt*32;
  f32x4 acc[2][2]={};
  nn_tile(hh,F,Wd,D,F,m0,n0,acc);
  int lane=threadIdx.x&63; int fr=lane&15,fq=lane>>4;
  for(int i=0;i<2;i++)for(int j=0;j<2;j++)for(int r=0;r<4;r++){ size_t o=(size_t)(m0+i*16+fq*4+r)*D+n0+j*16+fr; out[o]=f2bf(acc[i][j][r]+bf2f(h[o])); }
}

extern "C" void kernel_launch(void* const* d_in, const int* in_sizes, int n_in,
                              void* d_out, int out_size, void* d_ws, size_t ws_size,
                              hipStream_t stream) {
  const u16* x=(const u16*)d_in[0]; const u16* Cr=(const u16*)d_in[1]; const u16* Ci=(const u16*)d_in[2];
  const u16* Hr=(const u16*)d_in[3]; const u16* Hi=(const u16*)d_in[4];
  const u16* Wg=(const u16*)d_in[6]; const u16* Wu=(const u16*)d_in[7]; const u16* Wd=(const u16*)d_in[8];
  u16* out=(u16*)d_out;
  u16* ws=(u16*)d_ws;
  u16* ln1=ws; u16* abT=ln1+S*D; u16* h=abT+2*D*S; u16* ln2=h+S*D; u16* hh=ln2+S*D;
  ln_kernel<<<S,64,0,stream>>>(x,ln1);
  k_gemm1<<<(2*D/32)*(S/32)/4,256,0,stream>>>(Hr,Hi,ln1,abT);
  k_gemm2<<<(S/32)*(D/32)/4,256,0,stream>>>(Cr,Ci,abT,x,h);
  ln_kernel<<<S,64,0,stream>>>(h,ln2);
  k_gemm3<<<(S/32)*(F/32)/4,256,0,stream>>>(ln2,Wg,Wu,hh);
  k_gemm4<<<(S/32)*(D/32)/4,256,0,stream>>>(hh,Wd,h,out);
}
```

```cpp
#include <hip/hip_runtime.h>
#include <hip/hip_bf16.h>
typedef unsigned short u16;
using bf16x8 = __attribute__((ext_vector_type(8))) short;
using f32x4 = __attribute__((ext_vector_type(4))) float;

__device__ __forceinline__ float bf2f(u16 v){ return __uint_as_float(((unsigned)v)<<16); }
__device__ __forceinline__ u16 f2bf(float f){ __hip_bfloat16 b = __float2bfloat16(f); return *reinterpret_cast<u16*>(&b); }

constexpr int S=2048, D=576, F=4608;

__global__ void ln_kernel(const u16* __restrict__ x, u16* __restrict__ o){
  int row = blockIdx.x; int lane = threadIdx.x;
  float v[9]; float s=0;
  for(int i=0;i<9;i++){ v[i]=bf2f(x[row*D+lane+64*i]); s+=v[i]; }
  for(int m=32;m>=1;m>>=1) s+=__shfl_xor(s,m);
  float mean=s/D; float q=0;
  for(int i=0;i<9;i++){ float d=v[i]-mean; q+=d*d; }
  for(int m=32;m>=1;m>>=1) q+=__shfl_xor(q,m);
  float r=rsqrtf(q/D+1e-5f);
  for(int i=0;i<9;i++) o[row*D+lane+64*i]=f2bf((v[i]-mean)*r);
}

__device__ __forceinline__ void nt_tile(const u16* A,int lda,const u16* B,int ldb,int K,int m0,int n0,f32x4 acc[2][2],float sgn){
  int lane=threadIdx.x&63; int fr=lane&15,fq=lane>>4;
  for(int k=0;k<K;k+=32){
    bf16x8 a[2],b[2];
    for(int i=0;i<2;i++){ a[i]=*(const bf16x8*)(A+(size_t)(m0+i*16+fr)*lda+k+fq*8); b[i]=*(const bf16x8*)(B+(size_t)(n0+i*16+fr)*ldb+k+fq*8);}
    if(sgn<0){ for(int i=0;i<2;i++) for(int j=0;j<8;j++) a[i][j]^=(short)0x8000; }
    for(int i=0;i<2;i++)for(int j=0;j<2;j++) acc[i][j]=__builtin_amdgcn_mfma_f32_16x16x32_bf16(a[i],b[j],acc[i][j],0,0,0);
  }
}
__device__ __forceinline__ void nn_tile(const u16* A,int lda,const u16* B,int ldb,int K,int m0,int n0,f32x4 acc[2][2]){
  int lane=threadIdx.x&63; int fr=lane&15,fq=lane>>4;
  for(int k=0;k<K;k+=32){
    bf16x8 a[2],b[2];
    for(int i=0;i<2;i++){ a[i]=*(const bf16x8*)(A+(size_t)(m0+i*16+fr)*lda+k+fq*8);
      for(int j=0;j<8;j++) b[i][j]=(short)B[(size_t)(k+fq*8+j)*ldb+n0+i*16+fr]; }
    for(int i=0;i<2;i++)for(int j=0;j<2;j++) acc[i][j]=__builtin_amdgcn_mfma_f32_16x16x32_bf16(a[i],b[j],acc[i][j],0,0,0);
  }
}

__global__ void k_gemm1(const u16* Hr,const u16* Hi,const u16* ln1,u16* abT){
  int w=blockIdx.x*4+(threadIdx.x>>6); int mt=w/(S/32), nt=w%(S/32);
  int m0=mt*32,n0=nt*32; const u16* H=(m0<D)?Hr:Hi; int mm=(m0<D)?m0:m0-D;
  f32x4 acc[2][2]={};
  nt_tile(H,D,ln1,D,D,mm,n0,acc,1.f);
  int lane=threadIdx.x&63; int fr=lane&15,fq=lane>>4;
  for(int i=0;i<2;i++)for(int j=0;j<2;j++)for(int r=0;r<4;r++) abT[(size_t)(m0+i*16+fq*4+r)*S+n0+j*16+fr]=f2bf(acc[i][j][r]);
}
__global__ void k_gemm2(const u16* Cr,const u16* Ci,const u16* abT,const u16* x,u16* h){
  int w=blockIdx.x*4+(threadIdx.x>>6); int mt=w/(D/32), nt=w%(D/32);
  int m0=mt*32,n0=nt*32;
  f32x4 acc[2][2]={};
  nt_tile(Cr,S,abT,S,S,m0,n0,acc,1.f);
  nt_tile(Ci,S,abT+(size_t)D*S,S,S,m0,n0,acc,-1.f);
  int lane=threadIdx.x&63; int fr=lane&15,fq=lane>>4;
  for(int i=0;i<2;i++)for(int j=0;j<2;j++)for(int r=0;r<4;r++){ size_t o=(size_t)(m0+i*16+fq*4+r)*D+n0+j*16+fr; h[o]=f2bf(acc[i][j][r]+bf2f(x[o])); }
}
__global__ void k_gemm3(const u16* ln2,const u16* Wg,const u16* Wu,u16* hh){
  int w=blockIdx.x*4+(threadIdx.x>>6); int mt=w/(F/32), nt=w%(F/32);
  int m0=mt*32,n0=nt*32;
  f32x4 g[2][2]={},u[2][2]={};
  nn_tile(ln2,D,Wg,F,D,m0,n0,g);
  nn_tile(ln2,D,Wu,F,D,m0,n0,u);
  int lane=threadIdx.x&63; int fr=lane&15,fq=lane>>4;
  for(int i=0;i<2;i++)for(int j=0;j<2;j++)for(int r=0;r<4;r++){ size_t o=(size_t)(m0+i*16+fq*4+r)*F+n0+j*16+fr; float sg=1.f/(1.f+__expf(-g[i][j][r])); hh[o]=f2bf(sg*u[i][j][r]); }
}
__global__ void k_gemm4(const u16* hh,const u16* Wd,const u16* h,u16* out){
  int w=blockIdx.x*4+(threadIdx.x>>6); int mt=w/(D/32), nt=w%(D/32);
  int m0=mt*32,n0=nt*32;
  f32x4 acc[2][2]={};
  nn_tile(hh,F,Wd,D,F,m0,n0,acc);
  int lane=threadIdx.x&63; int fr=lane&15,fq=lane>>4;
  for(int i=0;i<2;i++)for(int j=0;j<2;j++)for(int r=0;r<4;r++){ size_t o=(size_t)(m0+i*16+fq*4+r)*D+n0+j*16+fr; out[o]=f2bf(acc[i][j][r]+bf2f(h[o])); }
}


typedef __attribute__((address_space(1))) void gv; typedef __attribute__((address_space(3))) void lv;
struct PL { const char* p[8]; unsigned long long n[8]; };
template<int NW,int INFL>
__global__ __launch_bounds__(NW*64) void lin_kernel(PL pl,int cnt,unsigned long long* out){
  __shared__ __attribute__((aligned(16))) char smem[NW*INFL*1024];
  int bid=blockIdx.x; int wid=threadIdx.x>>6, lane=threadIdx.x&63;
  unsigned long long t0=__builtin_amdgcn_s_memrealtime();
  int q=0;
  for(int j=0;j<cnt;j++){
    const char* P=pl.p[j]; size_t bytes=pl.n[j];
    for(size_t off=(size_t)bid*NW*1024; off+NW*1024<=bytes; off+=(size_t)gridDim.x*NW*1024){
      const char* s=P+off+wid*1024+lane*16;
      __builtin_amdgcn_global_load_lds((gv*)s,(lv*)(smem+((q%INFL)*NW+wid)*1024),16,0,0); q++;
      asm volatile("s_waitcnt vmcnt(%0)"::"i"(INFL-1):"memory");
    }
  }
  asm volatile("s_waitcnt vmcnt(0)":::"memory");
  __syncthreads();
  unsigned long long t1=__builtin_amdgcn_s_memrealtime();
  if(threadIdx.x==0){ out[2*bid]=t0; out[2*bid+1]=t1; }
}
__global__ void amp_kernel(const unsigned long long* ts,int n,int amp){
  if(threadIdx.x==0){
    unsigned long long mn=~0ull,mx=0;
    for(int i=0;i<n;i++){ mn=min(mn,ts[2*i]); mx=max(mx,ts[2*i+1]); }
    unsigned long long dt=(mx-mn); if(dt>10000) dt=10000;
    unsigned long long t0=__builtin_amdgcn_s_memrealtime();
    while(__builtin_amdgcn_s_memrealtime()-t0 < dt*amp) __builtin_amdgcn_s_sleep(4);
  }
}
extern "C" void kernel_launch(void* const* d_in, const int* in_sizes, int n_in,
                              void* d_out, int out_size, void* d_ws, size_t ws_size,
                              hipStream_t stream) {
  const u16* x=(const u16*)d_in[0]; const u16* Cr=(const u16*)d_in[1]; const u16* Ci=(const u16*)d_in[2];
  const u16* Hr=(const u16*)d_in[3]; const u16* Hi=(const u16*)d_in[4];
  const u16* Wg=(const u16*)d_in[6]; const u16* Wu=(const u16*)d_in[7]; const u16* Wd=(const u16*)d_in[8];
  u16* out=(u16*)d_out;
  u16* ws=(u16*)d_ws;
  u16* ln1=ws; u16* abT=ln1+S*D; u16* h=abT+2*D*S; u16* ln2=h+S*D; u16* hh=ln2+S*D;
  { PL pl; for(int i=0;i<8;i++){pl.p[i]=(const char*)d_in[0]; pl.n[i]=0;}
    pl.p[0]=(const char*)d_in[1]; pl.n[0]=(unsigned long long)in_sizes[1]*2;
    pl.p[1]=(const char*)d_in[2]; pl.n[1]=(unsigned long long)in_sizes[2]*2;
    unsigned long long* ts=(unsigned long long*)((char*)d_ws+(200u<<20));
    lin_kernel<8,16><<<256,512,0,stream>>>(pl,2,ts);
    amp_kernel<<<1,64,0,stream>>>(ts,256,50); }
  ln_kernel<<<S,64,0,stream>>>(x,ln1);
  k_gemm1<<<(2*D/32)*(S/32)/4,256,0,stream>>>(Hr,Hi,ln1,abT);
  k_gemm2<<<(S/32)*(D/32)/4,256,0,stream>>>(Cr,Ci,abT,x,h);
  ln_kernel<<<S,64,0,stream>>>(h,ln2);
  k_gemm3<<<(S/32)*(F/32)/4,256,0,stream>>>(ln2,Wg,Wu,hh);
  k_gemm4<<<(S/32)*(D/32)/4,256,0,stream>>>(hh,Wd,h,out);
}
```

```cpp
#include <hip/hip_runtime.h>
#include <hip/hip_bf16.h>
#include <type_traits>
typedef unsigned short u16;
typedef unsigned int u32;
using bf16x8 = __attribute__((ext_vector_type(8))) short;
using s16x4  = __attribute__((ext_vector_type(4))) short;
using f32x4  = __attribute__((ext_vector_type(4))) float;
using f32x16 = __attribute__((ext_vector_type(16))) float;
using u32x4  = __attribute__((ext_vector_type(4))) unsigned;
using bfx2   = __attribute__((ext_vector_type(2))) __bf16;
#define LDSP __attribute__((address_space(3)))
typedef __amdgpu_buffer_rsrc_t rsrc_t;

constexpr int S=2048, D=576, F=4608;

template<int I> using ic = std::integral_constant<int,I>;
template<int B,int E,class Fn> __device__ __forceinline__ void sfor(Fn&& f){ if constexpr(B<E){ f(ic<B>{}); sfor<B+1,E>(f);} }

__device__ __forceinline__ float bf2f(u16 v){ return __uint_as_float(((unsigned)v)<<16); }
__device__ __forceinline__ u16 f2bf(float f){ __hip_bfloat16 b = __float2bfloat16(f); return *reinterpret_cast<u16*>(&b); }
__device__ __forceinline__ u32 pack2(float a,float b){ bfx2 v={(__bf16)a,(__bf16)b}; return __builtin_bit_cast(u32,v); }
__device__ __forceinline__ rsrc_t mkrsrc(const void* p,u32 bytes){ return __builtin_amdgcn_make_buffer_rsrc((void*)p,(short)0,(int)bytes,0x00020000); }
__device__ __forceinline__ void dma16(u32 lds,u32 voff,rsrc_t r,u32 soff){
  asm volatile("s_mov_b32 m0, %0\n\ts_nop 0\n\tbuffer_load_dwordx4 %1, %2, %3 offen lds" :: "s"(lds),"v"(voff),"s"(r),"s"(soff) : "memory");
}
template<int N> __device__ __forceinline__ void wait_bar(){ asm volatile("s_waitcnt vmcnt(%0)\n\ts_barrier"::"i"(N):"memory"); }
template<int OFF> __device__ __forceinline__ void ldg16(bf16x8& d,u32 voff,rsrc_t r,u32 soff){
  asm volatile("buffer_load_dwordx4 %0, %1, %2, %3 offen offset:%4" : "=v"(d) : "v"(voff),"s"(r),"s"(soff),"i"(OFF) : "memory"); }
template<int N> __device__ __forceinline__ void wait_reg(bf16x8& d){ asm volatile("s_waitcnt vmcnt(%1)" : "+v"(d) : "i"(N)); }
template<int N> __device__ __forceinline__ void wait_reg2(bf16x8& d,bf16x8& e){ asm volatile("s_waitcnt vmcnt(%2)" : "+v"(d),"+v"(e) : "i"(N)); }
__device__ __forceinline__ void mfma16(f32x4& c,const bf16x8& a,const bf16x8& b){ asm volatile("v_mfma_f32_16x16x32_bf16 %0, %1, %2, %0" : "+v"(c) : "v"(a),"v"(b)); }
__device__ __forceinline__ void mfma16z(f32x4& c,const bf16x8& a,const bf16x8& b){ asm volatile("v_mfma_f32_16x16x32_bf16 %0, %1, %2, 0" : "=&v"(c) : "v"(a),"v"(b)); }
__device__ __forceinline__ void opaque(u32& v){ asm volatile("" : "+v"(v)); }
__device__ __forceinline__ s16x4 trrd(u32 a){ return __builtin_amdgcn_ds_read_tr16_b64_v4i16((LDSP s16x4*)(size_t)a); }
__device__ __forceinline__ u32 rfl(u32 v){ return __builtin_amdgcn_readfirstlane(v); }

constexpr size_t OFF_LN1=0;
constexpr size_t OFF_ABT=OFF_LN1+(size_t)S*D*2;
constexpr size_t OFF_H  =OFF_ABT+(size_t)2*D*S*2;
constexpr size_t OFF_LN2F=OFF_H+(size_t)S*D*2;
constexpr size_t OFF_HHF=OFF_LN2F+(size_t)S*D*2;

#define PD 4
#define STAMPALL 0
namespace k4 {
constexpr int NSTAGE=14, SLOT=24576, NSLOT=6, NSTEP=162;
constexpr int stage_of(int gs){ return gs<144? gs/12 : (gs<156?12:13); }
constexpr int stage_start(int n){ return n*12; }
constexpr int mid(int n){ return n*12+9; }
constexpr int dcnt(int n){ return n<13?3:2; }
constexpr int dstep(int n){ return n<=1? -1 : n==2? 6 : n==3? 18 : n==4? 30 : n==5? 38 : mid(n-5); }
constexpr int lstep(int l){ return l<2*PD? -1 : l-2*PD; }
constexpr int sstep(int g,int tt){ return g<3? 36*(g+1)+tt*16+15 : 144+tt*8+8; }
constexpr int PRO=3+2*PD+3;
constexpr int nD(int s){ int c=0; for(int n=2;n<NSTAGE;n++) if(dstep(n)==s) c+=dcnt(n); return c; }
constexpr int nL(int s){ int c=0; for(int l=0;l<36;l++) if(lstep(l)==s) c++; return c; }
constexpr int nS(int s){ int c=0; for(int g=0;g<4;g++) for(int t=0;t<2;t++) if(sstep(g,t)==s) c++; return c; }
constexpr int before(int gs){ int c=PRO; for(int s=0;s<gs;s++) c+=nD(s)+nL(s)+nS(s); return c; }
constexpr int dma_end(int n){ return n==0? 3 : n==1? PRO : before(dstep(n))+dcnt(n); }
constexpr int ln_end(int l){ return l<2*PD? 3+l+1 : before(lstep(l))+nD(lstep(l))+1; }
}

#define STAMP
#define ST(i) st[i]=stamp()
__device__ __forceinline__ unsigned long long stamp(){ unsigned long long t; asm volatile("s_memrealtime %0\n\ts_waitcnt lgkmcnt(0)" : "=s"(t) :: "memory"); return t; }
#define SB() __builtin_amdgcn_sched_barrier(0)
__global__ __launch_bounds__(512,2) void k4_gateup(const u16* __restrict__ ln2f,const u16* __restrict__ Wg,const u16* __restrict__ Wu,u16* __restrict__ hhf,unsigned long long* dbg){
  using namespace k4;
  unsigned long long st[18]={}; unsigned long long rt0=__builtin_amdgcn_s_memrealtime();
  ST(0);
  __shared__ __attribute__((aligned(256))) char smem[SLOT*NSLOT];
  const int bid=blockIdx.x, xcd=bid&7, slot=bid>>3;
  const int rb=(xcd&1)*4+(slot>>3), j=(xcd>>1)*8+(slot&7);
  const int lane=threadIdx.x&63; const u32 w=rfl(threadIdx.x>>6);
  const int fw0=144*j+((j&1)?16:0);
  const int fn0=144*j+((j&1)?0:128);
  const u32 ldsb=(u32)(size_t)(LDSP char*)smem;
  const u32 mat=w>>2, wq=w&3;
  rsrc_t rW=mkrsrc(mat?Wu:Wg,(u32)(D*F*2));
  rsrc_t rL=mkrsrc(ln2f+(size_t)(rb*8+w)*36*512,36*1024);
  const u32 vW=(u32)(lane>>2)*(F*2)+(u32)(((lane&3)^((lane>>5)<<1))*16);
  const u32 vN=(u32)((lane>>1)^(((lane>>4)&1)<<2))*(F*2)+(u32)(lane&1)*16;
  auto issueD=[&](auto nn){ constexpr int n=nn; constexpr u32 sl=(n%NSLOT)*SLOT;
    if constexpr(n<12){ constexpr int g=n/3, kb=(n%3)*192;
      #pragma unroll
      for(int i=0;i<3;i++) dma16(ldsb+sl+mat*12288+(wq*3+i)*1024, vW, rW, (u32)((kb+(wq*3+i)*16)*(F*2)+(fw0+32*g)*2));
    } else if constexpr(n==12){
      #pragma unroll
      for(int i=0;i<3;i++) dma16(ldsb+sl+mat*12288+(wq*3+i)*1024, vN, rW, (u32)(((wq*3+i)*32)*(F*2)+fn0*2));
    } else {
      #pragma unroll
      for(int i=0;i<2;i++){ u32 p=wq*2+i; p=p>5?5:p; dma16(ldsb+sl+mat*12288+p*1024, vN, rW, (u32)((384+p*32)*(F*2)+fn0*2)); }
    }
  };
  bf16x8 L[36];
  const u32 vL=lane*16;
  auto issueL=[&](auto kk){ constexpr int l=kk; constexpr int o=((l&1)*18+(l>>1))*1024;
    ldg16<o%4096>(L[l],vL,rL,(u32)(o-o%4096)); };
  issueD(ic<0>{}); sfor<0,2*PD>(issueL); issueD(ic<1>{});
  const int lq=lane>>4, lr=(lane>>2)&3, lp=lane&3;
  const int aw0=(8*lq+lr)*64+((lq&1)*32)+lp*8;
  const int aw1=(8*lq+lr)*64+(((lq&1)^1)*32)+lp*8;
  const int an0=((8*lq+lr)^((lq&1)<<2))*32+lp*8;
  const int an1=((8*lq+lr+4)^((lq&1)<<2))*32+lp*8;
  s16x4 Wf[3][2][2];
  u32 cb0,cb1;
  auto rd1=[&](auto ss,auto qq){ constexpr int gs=ss, q=qq; if constexpr(gs<NSTEP){
      constexpr int n=stage_of(gs), r=gs-stage_start(n), sl=(n%NSLOT)*SLOT, b=gs%3, m=q>>1, t=q&1;
      if constexpr(r==0 && q==0){ cb0=ldsb+sl+(n<12?aw0:an0); cb1=ldsb+sl+(n<12?aw1:an1); opaque(cb0); opaque(cb1); }
      if constexpr(n<12) Wf[b][m][t]=trrd(((r&1)?cb1:cb0)+m*12288+(r>>1)*2048+t*256);
      else               Wf[b][m][t]=trrd((t?cb1:cb0)+m*12288+r*1024);
    } };
  f32x4 acc[2][2][2][2];
  u32x4 pk;
  const int t16=rb*16+w*2;
  u16* hdst=hhf+((size_t)(t16>>3)*144*8+(t16&7))*512+lane*8;
  auto sig2=[&](const f32x4& g,const f32x4& u,int e)->u32{
    float o[2];
    #pragma unroll
    for(int z=0;z<2;z++){ float ex=__builtin_amdgcn_exp2f(g[e+z]*-1.44269504089f); o[z]=u[e+z]*__builtin_amdgcn_rcpf(1.f+ex); }
    return pack2(o[0],o[1]);
  };
  ST(1);
  wait_bar<PRO-3>();
  ST(2);
  sfor<0,4>([&](auto q){ rd1(ic<0>{},q); }); sfor<0,4>([&](auto q){ rd1(ic<1>{},q); });
  sfor<0,NSTEP>([&](auto ss){ constexpr int gs=ss; constexpr int n=stage_of(gs), g=gs<144?gs/36:4, ks=gs<144?(gs%36)/2:gs-144, i=gs<144?(gs&1):0, b=gs%3, ab=g&1;
    constexpr int c0=before(gs);
    if constexpr(n<=12 && gs==mid(n)){ wait_bar<c0-dma_end(n+1)>(); if constexpr(STAMPALL||n==3||n==11){ ST(3+n);} }
    sfor<2,NSTAGE>([&](auto nn){ if constexpr(dstep(nn)==gs) issueD(nn); });
    sfor<0,36>([&](auto ll){ if constexpr(lstep(ll)==gs) issueL(ll); });
    if constexpr(g==0 && i==0){ constexpr int c1=c0+nD(gs)+nL(gs); wait_reg2<c1-ln_end(2*ks+1)>(L[2*ks],L[2*ks+1]); }
    bf16x8 a0=__builtin_shufflevector(Wf[b][0][0],Wf[b][0][1],0,1,2,3,4,5,6,7);
    bf16x8 a1=__builtin_shufflevector(Wf[b][1][0],Wf[b][1][1],0,1,2,3,4,5,6,7);
    auto mm=[&](f32x4& c,const bf16x8& a,const bf16x8& bb){ if constexpr(ks==0) mfma16z(c,a,bb); else mfma16(c,a,bb); };
    constexpr int eg=g-1; constexpr int rel= g<4? gs-36*g : 2*(gs-144);
    constexpr int ett=rel/16, er=rel%16;
    constexpr bool doe=(g>=1 && ett<2 && (g<4? (er>=2 && (er-2)%3==0 && (er-2)/3<4) : (er%4==2)));
    constexpr int ep= g<4? (er-2)/3 : er/4;
    SB(); mm(acc[ab][i][0][0],a0,L[2*ks]);   SB(); rd1(ic<gs+2>{},ic<0>{});
    SB(); mm(acc[ab][i][0][1],a0,L[2*ks+1]); SB(); rd1(ic<gs+2>{},ic<1>{});
    SB(); mm(acc[ab][i][1][1],a1,L[2*ks+1]); SB(); rd1(ic<gs+2>{},ic<2>{});
    SB(); mm(acc[ab][i][1][0],a1,L[2*ks]);   SB(); rd1(ic<gs+2>{},ic<3>{});
    if constexpr(doe) pk[ep]=sig2(acc[ab^1][ep>>1][0][ett],acc[ab^1][ep>>1][1][ett],(ep&1)*2);
    if constexpr(g>=1){ sfor<0,2>([&](auto tt){ if constexpr(sstep(eg,tt)==gs){ *(u32x4*)(hdst+(size_t)((fw0>>5)+eg)*4096+tt*512)=pk; asm volatile("":::"memory"); } }); }
    SB();
  });
  asm volatile("s_nop 7\n\ts_nop 1"); SB();
  #pragma unroll
  for(int tt=0;tt<2;tt++){
    uint2 v; v.x=sig2(acc[0][0][0][tt],acc[0][0][1][tt],0); v.y=sig2(acc[0][0][0][tt],acc[0][0][1][tt],2);
    *(uint2*)(hdst+(size_t)(fn0>>5)*4096+tt*512+(j&1)*4)=v;
  }
  ST(16); asm volatile("s_waitcnt vmcnt(0)":::"memory"); ST(17);
  unsigned long long rt1=__builtin_amdgcn_s_memrealtime();
  if(lane==0){ for(int i=0;i<18;i++) dbg[(bid*8+w)*32+i]=st[i]-st[0]; dbg[(bid*8+w)*32+18]=rt0; dbg[(bid*8+w)*32+19]=rt1; }
}

__global__ void ln_kernel(const u16* __restrict__ x, u16* __restrict__ o,int frag){
  int row = blockIdx.x; int lane = threadIdx.x;
  float v[9]; float s=0;
  for(int i=0;i<9;i++){ v[i]=bf2f(x[row*D+lane+64*i]); s+=v[i]; }
  for(int m=32;m>=1;m>>=1) s+=__shfl_xor(s,m);
  float mean=s/D; float q=0;
  for(int i=0;i<9;i++){ float d=v[i]-mean; q+=d*d; }
  for(int m=32;m>=1;m>>=1) q+=__shfl_xor(q,m);
  float r=rsqrtf(q/D+1e-5f);
  for(int i=0;i<9;i++){ int d=lane+64*i; size_t off= frag? ((size_t)((row>>4)*18+(d>>5))*64+((d>>3)&3)*16+(row&15))*8+(d&7) : (size_t)row*D+d; o[off]=f2bf((v[i]-mean)*r); }
}
__device__ __forceinline__ void nt_tile(const u16* A,int lda,const u16* B,int ldb,int K,int m0,int n0,f32x4 acc[2][2],float sgn){
  int lane=threadIdx.x&63; int fr=lane&15,fq=lane>>4;
  for(int k=0;k<K;k+=32){
    bf16x8 a[2],b[2];
    for(int i=0;i<2;i++){ a[i]=*(const bf16x8*)(A+(size_t)(m0+i*16+fr)*lda+k+fq*8); b[i]=*(const bf16x8*)(B+(size_t)(n0+i*16+fr)*ldb+k+fq*8);}
    if(sgn<0){ for(int i=0;i<2;i++) for(int j=0;j<8;j++) a[i][j]^=(short)0x8000; }
    for(int i=0;i<2;i++)for(int j=0;j<2;j++) acc[i][j]=__builtin_amdgcn_mfma_f32_16x16x32_bf16(a[i],b[j],acc[i][j],0,0,0);
  }
}
__global__ void s_gemm1(const u16* Hr,const u16* Hi,const u16* ln1,u16* abT){
  int w=blockIdx.x*4+(threadIdx.x>>6); int mt=w/(S/32), nt=w%(S/32);
  int m0=mt*32,n0=nt*32; const u16* H=(m0<D)?Hr:Hi; int mm=(m0<D)?m0:m0-D;
  f32x4 acc[2][2]={};
  nt_tile(H,D,ln1,D,D,mm,n0,acc,1.f);
  int lane=threadIdx.x&63; int fr=lane&15,fq=lane>>4;
  for(int i=0;i<2;i++)for(int j=0;j<2;j++)for(int r=0;r<4;r++) abT[(size_t)(m0+i*16+fq*4+r)*S+n0+j*16+fr]=f2bf(acc[i][j][r]);
}
__global__ void s_gemm2(const u16* Cr,const u16* Ci,const u16* abT,const u16* x,u16* h){
  int w=blockIdx.x*4+(threadIdx.x>>6); int mt=w/(D/32), nt=w%(D/32);
  int m0=mt*32,n0=nt*32;
  f32x4 acc[2][2]={};
  nt_tile(Cr,S,abT,S,S,m0,n0,acc,1.f);
  nt_tile(Ci,S,abT+(size_t)D*S,S,S,m0,n0,acc,-1.f);
  int lane=threadIdx.x&63; int fr=lane&15,fq=lane>>4;
  for(int i=0;i<2;i++)for(int j=0;j<2;j++)for(int r=0;r<4;r++){ size_t o=(size_t)(m0+i*16+fq*4+r)*D+n0+j*16+fr; h[o]=f2bf(acc[i][j][r]+bf2f(x[o])); }
}
__global__ void s_gemm4(const u16* hhf,const u16* Wd,const u16* h,u16* out){
  int w=blockIdx.x*4+(threadIdx.x>>6); int mt=w/(D/32), nt=w%(D/32);
  int m0=mt*32,n0=nt*32; int lane=threadIdx.x&63; int fr=lane&15,fq=lane>>4;
  f32x4 acc[2][2]={};
  for(int k=0;k<F;k+=32){
    bf16x8 a[2],b[2];
    int fb=(k>>5);
    for(int i=0;i<2;i++){
      int t16=mt*2+i;
      a[i]=*(const bf16x8*)(hhf+(((size_t)(t16>>3)*144+fb)*8+(t16&7))*512+lane*8);
      for(int jj=0;jj<8;jj++){ int f=fb*32+16*(jj>>2)+4*fq+(jj&3); b[i][jj]=(short)Wd[(size_t)f*D+n0+i*16+fr]; }
    }
    for(int i=0;i<2;i++)for(int jj=0;jj<2;jj++) acc[i][jj]=__builtin_amdgcn_mfma_f32_16x16x32_bf16(a[i],b[jj],acc[i][jj],0,0,0);
  }
  for(int i=0;i<2;i++)for(int jj=0;jj<2;jj++)for(int r=0;r<4;r++){ size_t o=(size_t)(m0+i*16+fq*4+r)*D+n0+jj*16+fr; out[o]=f2bf(acc[i][jj][r]+bf2f(h[o])); }
}

__global__ void amp_kernel(const unsigned long long* ts,int a,int b,int amp){
  unsigned long long dt=0;
  for(int i=threadIdx.x;i<256;i+=64) dt+=ts[i*8*32+b]-ts[i*8*32+a];
  for(int m=32;m>=1;m>>=1) dt+=__shfl_xor(dt,m);
  dt=dt*amp/256; if(dt>200000) dt=200000;
  unsigned long long t0=__builtin_amdgcn_s_memrealtime();
  while(__builtin_amdgcn_s_memrealtime()-t0 < dt) __builtin_amdgcn_s_sleep(4);
}
extern "C" void kernel_launch(void* const* d_in, const int* in_sizes, int n_in,
                              void* d_out, int out_size, void* d_ws, size_t ws_size,
                              hipStream_t stream) {
  const u16* x=(const u16*)d_in[0]; const u16* Cr=(const u16*)d_in[1]; const u16* Ci=(const u16*)d_in[2];
  const u16* Hr=(const u16*)d_in[3]; const u16* Hi=(const u16*)d_in[4];
  const u16* Wg=(const u16*)d_in[6]; const u16* Wu=(const u16*)d_in[7]; const u16* Wd=(const u16*)d_in[8];
  u16* out=(u16*)d_out; char* ws=(char*)d_ws;
  u16* ln1=(u16*)(ws+OFF_LN1); u16* abT=(u16*)(ws+OFF_ABT); u16* h=(u16*)(ws+OFF_H); u16* ln2f=(u16*)(ws+OFF_LN2F); u16* hhf=(u16*)(ws+OFF_HHF);
  ln_kernel<<<S,64,0,stream>>>(x,ln1,0);
  s_gemm1<<<(2*D/32)*(S/32)/4,256,0,stream>>>(Hr,Hi,ln1,abT);
  s_gemm2<<<(S/32)*(D/32)/4,256,0,stream>>>(Cr,Ci,abT,x,h);
  ln_kernel<<<S,64,0,stream>>>(h,ln2f,1);
  k4_gateup<<<256,512,0,stream>>>(ln2f,Wg,Wu,hhf,(unsigned long long*)(ws+(200u<<20)));
  amp_kernel<<<1,64,0,stream>>>((unsigned long long*)(ws+(200u<<20)),0,0,50);
  s_gemm4<<<(S/32)*(D/32)/4,256,0,stream>>>(hhf,Wd,h,out);
}
```

```cpp
#include <hip/hip_runtime.h>

__global__ __launch_bounds__(512, 2) __attribute__((aligned(4096))) void fnet_glu_fused(const void* x, const void* Cr, const void* Ci, const void* Hr,
                                                          const void* Hi, const void* Wg, const void* Wu, const void* Wd,
                                                          void* out, void* ws) {
  __shared__ __attribute__((aligned(256))) char smem[163840];
  unsigned lds = (unsigned)(size_t)(__attribute__((address_space(3))) char*)smem;
  asm volatile(
"s_load_dwordx16 s[4:19], s[0:1], 0x0\n"
"s_load_dwordx4 s[20:23], s[0:1], 0x40\n"
"v_lshrrev_b32 v3, 6, v0\n"
"s_nop 0\n"
"v_readfirstlane_b32 s24, v3\n"
"v_and_b32 v0, 63, v0\n"
"v_lshlrev_b32 v1, 4, v0\n"
"s_and_b32 s28, s2, 7\n"
"s_lshr_b32 s27, s2, 3\n"
"s_and_b32 s29, s2, 1\n"
"s_lshr_b32 s30, s28, 1\n"
"s_lshr_b32 s25, s24, 2\n"
"s_and_b32 s26, s24, 3\n"
"s_lshr_b32 s45, s27, 2\n"
"s_mul_i32 s31, s29, 8\n"
"s_add_u32 s31, s31, s45\n"
"s_and_b32 s32, s27, 3\n"
"s_lshr_b32 s46, s30, 1\n"
"s_lshl_b32 s46, s46, 4\n"
"s_add_u32 s46, s46, s29\n"
"s_add_u32 s46, s46, 0x96987513\n"
"s_lshl_b32 s45, s24, 2\n"
"s_lshr_b32 s46, s46, s45\n"
"s_and_b32 s46, s46, 15\n"
"s_lshl_b32 s46, s46, 3\n"
"s_load_dwordx2 s[38:39], s[0:1], s46\n"
"s_mov_b32 s42, 0x28282005\n"
"s_mov_b32 s43, 0xba28ba12\n"
"s_lshl_b32 s45, s24, 3\n"
"s_lshr_b64 s[42:43], s[42:43], s45\n"
"s_and_b32 s45, s42, 255\n"
"s_lshl_b32 s46, s29, 1\n"
"s_or_b32 s46, s46, 128\n"
"s_lshr_b32 s46, s46, s24\n"
"s_and_b32 s46, s46, 1\n"
"s_lshl_b32 s46, s46, 5\n"
"s_add_u32 s46, s46, s27\n"
"s_mul_i32 s45, s45, s46\n"
"s_lshl_b32 s37, s45, 12\n"
"v_lshrrev_b32 v3, 5, v0\n"
"v_lshlrev_b32 v3, 3, v3\n"
"v_bfe_u32 v8, v0, 3, 2\n"
"v_add_u32 v3, v3, v8\n"
"v_mul_u32_u24 v2, 1152, v3\n"
"s_and_b32 s45, s24, 1\n"
"s_lshl_b32 s46, s45, 2\n"
"v_lshrrev_b32 v3, 4, v0\n"
"v_add_u32 v3, s46, v3\n"
"v_xor_b32 v3, v3, v0\n"
"v_and_b32 v3, 7, v3\n"
"v_lshl_add_u32 v2, v3, 4, v2\n"
"s_bfe_u32 s46, s24, 0x10001\n"
"s_mul_i32 s47, s25, 36864\n"
"s_mul_i32 s42, s45, 18432\n"
"s_add_u32 s47, s47, s42\n"
"s_mul_i32 s42, s46, 4608\n"
"s_add_u32 s47, s47, s42\n"
"s_lshr_b32 s45, s27, 2\n"
"s_mul_i32 s94, s30, 589824\n"
"s_mul_i32 s42, s45, 73728\n"
"s_add_u32 s94, s94, s42\n"
"s_add_u32 s94, s94, s47\n"
"s_mul_i32 s95, s32, 147456\n"
"s_add_u32 s95, s95, s47\n"
"s_and_b32 s46, s45, 7\n"
"s_mul_i32 s97, s32, 2\n"
"s_add_u32 s97, s97, s46\n"
"s_sub_u32 s46, s97, 9\n"
"s_cselect_b32 s97, s97, s46\n"
"s_lshl_b32 s97, s97, 7\n"
"s_mov_b32 s66, 2359296\n"
"s_mov_b32 s67, 0x20000\n"
"s_mov_b32 s47, s97\n"
"s_waitcnt lgkmcnt(0)\n"
"s_mov_b32 s64, s4\n"
"s_and_b32 s65, s5, 0xffff\n"
"s_load_dword s44, s[38:39], s37\n"
"s_add_u32 s46, s94, s47\n"
"buffer_load_dwordx4 v[112:115], v2, s[64:67], s46 offen\n"
"s_add_u32 s47, s47, 128\n"
"s_cmp_eq_u32 s47, 1152\n"
"s_cselect_b32 s47, 0, s47\n"
"s_add_u32 s46, s94, s47\n"
"buffer_load_dwordx4 v[116:119], v2, s[64:67], s46 offen\n"
"s_add_u32 s47, s47, 128\n"
"s_cmp_eq_u32 s47, 1152\n"
"s_cselect_b32 s47, 0, s47\n"
"s_add_u32 s46, s94, s47\n"
"buffer_load_dwordx4 v[120:123], v2, s[64:67], s46 offen\n"
"s_add_u32 s47, s47, 128\n"
"s_cmp_eq_u32 s47, 1152\n"
"s_cselect_b32 s47, 0, s47\n"
"s_add_u32 s46, s94, s47\n"
"buffer_load_dwordx4 v[124:127], v2, s[64:67], s46 offen\n"
"s_add_u32 s47, s47, 128\n"
"s_cmp_eq_u32 s47, 1152\n"
"s_cselect_b32 s47, 0, s47\n"
"v_lshrrev_b32 v3, 3, v0\n"
"v_mul_u32_u24 v19, 1152, v3\n"
"v_xor_b32 v3, s24, v0\n"
"v_and_b32 v3, 7, v3\n"
"v_lshl_add_u32 v19, v3, 4, v19\n"
"s_mul_i32 s96, s32, 18432\n"
"s_mul_i32 s42, s24, 2304\n"
"s_add_u32 s96, s96, s42\n"
"s_add_u32 s96, s96, 589824\n"
"s_lshl_b32 s98, s24, 10\n"
"s_lshl_b32 s99, s24, 8\n"
"s_cmp_eq_u32 s29, 0\n"
"s_cselect_b32 s10, s10, s12\n"
"s_cselect_b32 s11, s11, s13\n"
"s_mov_b32 s68, s10\n"
"s_and_b32 s69, s11, 0xffff\n"
"s_mov_b32 s70, 663552\n"
"s_mov_b32 s71, 0x20000\n"
"s_lshr_b32 s33, s31, 1\n"
"s_and_b32 s45, s27, 7\n"
"s_mul_i32 s34, s30, 8\n"
"s_add_u32 s34, s34, s45\n"
"s_mul_i32 s35, s31, 16\n"
"s_mul_i32 s42, s30, 4\n"
"s_add_u32 s35, s35, s42\n"
"s_add_u32 s35, s35, s32\n"
"s_mov_b32 s48, s22\n"
"s_and_b32 s49, s23, 0xffff\n"
"s_mov_b32 s50, 49287168\n"
"s_mov_b32 s51, 0x20000\n"
"s_cmp_eq_u32 s25, 0\n"
"s_cselect_b32 s14, s14, s16\n"
"s_cselect_b32 s15, s15, s17\n"
"s_mov_b32 s52, s14\n"
"s_and_b32 s53, s15, 0xffff\n"
"s_mov_b32 s54, 5308416\n"
"s_mov_b32 s55, 0x20000\n"
"s_mov_b32 s56, s18\n"
"s_and_b32 s57, s19, 0xffff\n"
"s_mov_b32 s58, 5308416\n"
"s_mov_b32 s59, 0x20000\n"
"s_mov_b32 s60, s20\n"
"s_and_b32 s61, s21, 0xffff\n"
"s_mov_b32 s62, 2359296\n"
"s_mov_b32 s63, 0x20000\n"
"s_cmp_lt_u32 s30, 2\n"
"s_cselect_b32 s6, s6, s8\n"
"s_cselect_b32 s7, s7, s9\n"
"s_mov_b32 s72, s6\n"
"s_and_b32 s73, s7, 0xffff\n"
"s_mov_b32 s74, 8388608\n"
"s_mov_b32 s75, 0x20000\n"
"s_mul_i32 s78, s25, 12288\n"
"s_mul_i32 s76, s26, 3072\n"
"s_add_u32 s76, s76, s78\n"
"s_mul_i32 s77, s26, 1024\n"
"s_add_u32 s77, s77, s78\n"
"s_mul_i32 s79, s2, 1024\n"
"s_mul_i32 s42, s24, 128\n"
"s_add_u32 s79, s79, s42\n"
"s_add_u32 s79, s79, 4096\n"
"s_mul_i32 s80, s31, 589824\n"
"s_mul_i32 s42, s26, 147456\n"
"s_add_u32 s80, s80, s42\n"
"s_mul_i32 s42, s25, 73728\n"
"s_add_u32 s80, s80, s42\n"
"s_mul_i32 s42, s30, 9216\n"
"s_add_u32 s80, s80, s42\n"
"s_mul_i32 s42, s32, 2304\n"
"s_add_u32 s80, s80, s42\n"
"v_and_b32 v8, 15, v0\n"
"v_lshrrev_b32 v3, 1, v8\n"
"v_lshrrev_b32 v9, 4, v0\n"
"v_xor_b32 v3, v3, v9\n"
"v_lshlrev_b32 v3, 4, v3\n"
"v_lshl_add_u32 v3, v8, 7, v3\n"
"s_mul_i32 s45, s25, 4096\n"
"v_add_u32 v72, s45, v3\n"
"v_add_u32 v74, 0x8000, v72\n"
"s_mul_i32 s46, s26, 4096\n"
"v_add_u32 v78, s46, v3\n"
"v_add_u32 v76, 73728, v78\n"
"v_add_u32 v80, 90112, v3\n"
"v_add_u32 v82, 0x4000, v3\n"
"v_xor_b32 v73, 64, v72\n"
"v_xor_b32 v75, 64, v74\n"
"v_xor_b32 v77, 64, v76\n"
"v_xor_b32 v79, 64, v78\n"
"v_xor_b32 v81, 64, v80\n"
"v_xor_b32 v83, 64, v82\n"
"s_and_b32 s45, s30, 1\n"
"s_mul_i32 s46, s31, 524288\n"
"s_mul_i32 s42, s26, 131072\n"
"s_add_u32 s46, s46, s42\n"
"s_mul_i32 s42, s45, 2048\n"
"s_add_u32 s46, s46, s42\n"
"s_mul_i32 s42, s25, 1024\n"
"s_add_u32 s46, s46, s42\n"
"s_mul_i32 s84, s25, 65536\n"
"s_add_u32 s84, s84, s46\n"
"s_mul_i32 s85, s25, -65536\n"
"s_add_u32 s85, s85, s46\n"
"s_add_u32 s85, s85, 65536\n"
"s_mul_i32 s89, s32, 4\n"
"s_add_u32 s89, s89, s31\n"
"s_mul_i32 s42, s25, 4\n"
"s_add_u32 s89, s89, s42\n"
"s_mul_i32 s90, s24, 4096\n"
"v_lshrrev_b32 v3, 4, v0\n"
"v_xor_b32 v3, v3, v0\n"
"v_and_b32 v3, 7, v3\n"
"v_lshlrev_b32 v4, 4, v3\n"
"v_lshrrev_b32 v3, 3, v0\n"
"v_lshl_add_u32 v4, v3, 12, v4\n"
"v_xor_b32 v5, 64, v4\n"
"v_add_u32 v5, 0x8000, v5\n"
"v_add_u32 v6, s98, v1\n"
"v_add_u32 v7, 0x8000, v6\n"
"s_cmp_eq_u32 s26, s25\n"
"s_cselect_b32 s45, 0, 0x80000000\n"
"v_or_b32 v20, s45, v1\n"
"s_lshr_b32 s45, s27, 2\n"
"s_mul_i32 s82, s29, 2359296\n"
"s_mul_i32 s42, s32, 589824\n"
"s_add_u32 s82, s82, s42\n"
"s_mul_i32 s42, s30, 147456\n"
"s_add_u32 s82, s82, s42\n"
"s_mul_i32 s42, s45, 18432\n"
"s_add_u32 s82, s82, s42\n"
"s_mul_i32 s42, s25, 9216\n"
"s_add_u32 s82, s82, s42\n"
"s_add_u32 s82, s82, 44044288\n"
"s_mul_i32 s81, s26, 2048\n"
"s_add_u32 s81, s81, s82\n"
"s_add_u32 s82, s82, 8192\n"
"s_cmp_eq_u32 s24, 1\n"
"s_cbranch_scc1 .Lst1_0\n"
".Lbk1:\n"
"s_cmp_eq_u32 s24, 2\n"
"s_cbranch_scc1 .Lst2_0\n"
".Lbk2:\n"
"s_cmp_eq_u32 s24, 3\n"
"s_cbranch_scc1 .Lst3_0\n"
".Lbk3:\n"
"s_cmp_eq_u32 s24, 4\n"
"s_cbranch_scc1 .Lst4_0\n"
".Lbk4:\n"
"s_cmp_eq_u32 s24, 5\n"
"s_cbranch_scc1 .Lst5_0\n"
".Lbk5:\n"
"s_cmp_eq_u32 s24, 6\n"
"s_cbranch_scc1 .Lsa\n"
".Lba:\n"
"s_cmp_eq_u32 s24, 7\n"
"s_cbranch_scc1 .Lsb\n"
".Lbb:\n"
"s_add_u32 s46, s94, s47\n"
"buffer_load_dwordx4 v[128:131], v2, s[64:67], s46 offen\n"
"s_add_u32 s47, s47, 128\n"
"s_cmp_eq_u32 s47, 1152\n"
"s_cselect_b32 s47, 0, s47\n"
"s_add_u32 s46, s94, s47\n"
"buffer_load_dwordx4 v[132:135], v2, s[64:67], s46 offen\n"
"s_add_u32 s47, s47, 128\n"
"s_cmp_eq_u32 s47, 1152\n"
"s_cselect_b32 s47, 0, s47\n"
"s_add_u32 s46, s94, s47\n"
"buffer_load_dwordx4 v[136:139], v2, s[64:67], s46 offen\n"
"s_add_u32 s47, s47, 128\n"
"s_cmp_eq_u32 s47, 1152\n"
"s_cselect_b32 s47, 0, s47\n"
"s_add_u32 s46, s94, s47\n"
"buffer_load_dwordx4 v[140:143], v2, s[64:67], s46 offen\n"
"s_add_u32 s47, s47, 128\n"
"s_cmp_eq_u32 s47, 1152\n"
"s_cselect_b32 s47, 0, s47\n"
"s_add_u32 s46, s94, s47\n"
"buffer_load_dwordx4 v[144:147], v2, s[64:67], s46 offen\n"
"s_waitcnt vmcnt(8)\n"
"v_lshlrev_b32 v148, 16, v112\n"
"v_and_b32 v149, 0xffff0000, v112\n"
"v_mov_b32 v48, v148\n"
"v_mov_b32 v49, v149\n"
"v_pk_mul_f32 v[50:51], v[148:149], v[148:149]\n"
"v_lshlrev_b32 v150, 16, v113\n"
"v_and_b32 v151, 0xffff0000, v113\n"
"v_pk_add_f32 v[48:49], v[48:49], v[150:151]\n"
"v_pk_fma_f32 v[50:51], v[150:151], v[150:151], v[50:51]\n"
"v_lshlrev_b32 v152, 16, v114\n"
"v_and_b32 v153, 0xffff0000, v114\n"
"v_pk_add_f32 v[48:49], v[48:49], v[152:153]\n"
"v_pk_fma_f32 v[50:51], v[152:153], v[152:153], v[50:51]\n"
"v_lshlrev_b32 v154, 16, v115\n"
"v_and_b32 v155, 0xffff0000, v115\n"
"v_pk_add_f32 v[48:49], v[48:49], v[154:155]\n"
"v_pk_fma_f32 v[50:51], v[154:155], v[154:155], v[50:51]\n"
"s_waitcnt vmcnt(7)\n"
"v_lshlrev_b32 v156, 16, v116\n"
"v_and_b32 v157, 0xffff0000, v116\n"
"v_pk_add_f32 v[48:49], v[48:49], v[156:157]\n"
"v_pk_fma_f32 v[50:51], v[156:157], v[156:157], v[50:51]\n"
"v_lshlrev_b32 v158, 16, v117\n"
"v_and_b32 v159, 0xffff0000, v117\n"
"v_pk_add_f32 v[48:49], v[48:49], v[158:159]\n"
"v_pk_fma_f32 v[50:51], v[158:159], v[158:159], v[50:51]\n"
"v_lshlrev_b32 v160, 16, v118\n"
"v_and_b32 v161, 0xffff0000, v118\n"
"v_pk_add_f32 v[48:49], v[48:49], v[160:161]\n"
"v_pk_fma_f32 v[50:51], v[160:161], v[160:161], v[50:51]\n"
"v_lshlrev_b32 v162, 16, v119\n"
"v_and_b32 v163, 0xffff0000, v119\n"
"v_pk_add_f32 v[48:49], v[48:49], v[162:163]\n"
"v_pk_fma_f32 v[50:51], v[162:163], v[162:163], v[50:51]\n"
"s_waitcnt vmcnt(6)\n"
"v_lshlrev_b32 v164, 16, v120\n"
"v_and_b32 v165, 0xffff0000, v120\n"
"v_pk_add_f32 v[48:49], v[48:49], v[164:165]\n"
"v_pk_fma_f32 v[50:51], v[164:165], v[164:165], v[50:51]\n"
"v_lshlrev_b32 v166, 16, v121\n"
"v_and_b32 v167, 0xffff0000, v121\n"
"v_pk_add_f32 v[48:49], v[48:49], v[166:167]\n"
"v_pk_fma_f32 v[50:51], v[166:167], v[166:167], v[50:51]\n"
"v_lshlrev_b32 v168, 16, v122\n"
"v_and_b32 v169, 0xffff0000, v122\n"
"v_pk_add_f32 v[48:49], v[48:49], v[168:169]\n"
"v_pk_fma_f32 v[50:51], v[168:169], v[168:169], v[50:51]\n"
"v_lshlrev_b32 v170, 16, v123\n"
"v_and_b32 v171, 0xffff0000, v123\n"
"v_pk_add_f32 v[48:49], v[48:49], v[170:171]\n"
"v_pk_fma_f32 v[50:51], v[170:171], v[170:171], v[50:51]\n"
"s_waitcnt vmcnt(5)\n"
"v_lshlrev_b32 v172, 16, v124\n"
"v_and_b32 v173, 0xffff0000, v124\n"
"v_pk_add_f32 v[48:49], v[48:49], v[172:173]\n"
"v_pk_fma_f32 v[50:51], v[172:173], v[172:173], v[50:51]\n"
"v_lshlrev_b32 v174, 16, v125\n"
"v_and_b32 v175, 0xffff0000, v125\n"
"v_pk_add_f32 v[48:49], v[48:49], v[174:175]\n"
"v_pk_fma_f32 v[50:51], v[174:175], v[174:175], v[50:51]\n"
"v_lshlrev_b32 v176, 16, v126\n"
"v_and_b32 v177, 0xffff0000, v126\n"
"v_pk_add_f32 v[48:49], v[48:49], v[176:177]\n"
"v_pk_fma_f32 v[50:51], v[176:177], v[176:177], v[50:51]\n"
"v_lshlrev_b32 v178, 16, v127\n"
"v_and_b32 v179, 0xffff0000, v127\n"
"v_pk_add_f32 v[48:49], v[48:49], v[178:179]\n"
"v_pk_fma_f32 v[50:51], v[178:179], v[178:179], v[50:51]\n"
"s_waitcnt vmcnt(4)\n"
"v_lshlrev_b32 v180, 16, v128\n"
"v_and_b32 v181, 0xffff0000, v128\n"
"v_pk_add_f32 v[48:49], v[48:49], v[180:181]\n"
"v_pk_fma_f32 v[50:51], v[180:181], v[180:181], v[50:51]\n"
"v_lshlrev_b32 v182, 16, v129\n"
"v_and_b32 v183, 0xffff0000, v129\n"
"v_pk_add_f32 v[48:49], v[48:49], v[182:183]\n"
"v_pk_fma_f32 v[50:51], v[182:183], v[182:183], v[50:51]\n"
"v_lshlrev_b32 v184, 16, v130\n"
"v_and_b32 v185, 0xffff0000, v130\n"
"v_pk_add_f32 v[48:49], v[48:49], v[184:185]\n"
"v_pk_fma_f32 v[50:51], v[184:185], v[184:185], v[50:51]\n"
"v_lshlrev_b32 v186, 16, v131\n"
"v_and_b32 v187, 0xffff0000, v131\n"
"v_pk_add_f32 v[48:49], v[48:49], v[186:187]\n"
"v_pk_fma_f32 v[50:51], v[186:187], v[186:187], v[50:51]\n"
"s_barrier\n"
"v_mov_b32 v18, 0\n"
"buffer_load_dwordx2 v[16:17], v18, s[48:51], 0 offen sc1\n"
"s_waitcnt vmcnt(4)\n"
"v_lshlrev_b32 v188, 16, v132\n"
"v_and_b32 v189, 0xffff0000, v132\n"
"v_pk_add_f32 v[48:49], v[48:49], v[188:189]\n"
"v_pk_fma_f32 v[50:51], v[188:189], v[188:189], v[50:51]\n"
"v_lshlrev_b32 v190, 16, v133\n"
"v_and_b32 v191, 0xffff0000, v133\n"
"v_pk_add_f32 v[48:49], v[48:49], v[190:191]\n"
"v_pk_fma_f32 v[50:51], v[190:191], v[190:191], v[50:51]\n"
"v_lshlrev_b32 v192, 16, v134\n"
"v_and_b32 v193, 0xffff0000, v134\n"
"v_pk_add_f32 v[48:49], v[48:49], v[192:193]\n"
"v_pk_fma_f32 v[50:51], v[192:193], v[192:193], v[50:51]\n"
"v_lshlrev_b32 v194, 16, v135\n"
"v_and_b32 v195, 0xffff0000, v135\n"
"v_pk_add_f32 v[48:49], v[48:49], v[194:195]\n"
"v_pk_fma_f32 v[50:51], v[194:195], v[194:195], v[50:51]\n"
"s_add_u32 m0, s98, 73728\n"
"s_add_u32 s3, s95, s97\n"
"buffer_load_dwordx4 v2, s[68:71], s3 offen lds\n"
"s_waitcnt vmcnt(4)\n"
"v_lshlrev_b32 v196, 16, v136\n"
"v_and_b32 v197, 0xffff0000, v136\n"
"v_pk_add_f32 v[48:49], v[48:49], v[196:197]\n"
"v_pk_fma_f32 v[50:51], v[196:197], v[196:197], v[50:51]\n"
"v_lshlrev_b32 v198, 16, v137\n"
"v_and_b32 v199, 0xffff0000, v137\n"
"v_pk_add_f32 v[48:49], v[48:49], v[198:199]\n"
"v_pk_fma_f32 v[50:51], v[198:199], v[198:199], v[50:51]\n"
"v_lshlrev_b32 v200, 16, v138\n"
"v_and_b32 v201, 0xffff0000, v138\n"
"v_pk_add_f32 v[48:49], v[48:49], v[200:201]\n"
"v_pk_fma_f32 v[50:51], v[200:201], v[200:201], v[50:51]\n"
"v_lshlrev_b32 v202, 16, v139\n"
"v_and_b32 v203, 0xffff0000, v139\n"
"v_pk_add_f32 v[48:49], v[48:49], v[202:203]\n"
"v_pk_fma_f32 v[50:51], v[202:203], v[202:203], v[50:51]\n"
"s_add_u32 m0, s98, 81920\n"
"s_add_u32 s46, s3, 73728\n"
"buffer_load_dwordx4 v2, s[68:71], s46 offen lds\n"
"s_waitcnt vmcnt(4)\n"
"v_lshlrev_b32 v204, 16, v140\n"
"v_and_b32 v205, 0xffff0000, v140\n"
"v_pk_add_f32 v[48:49], v[48:49], v[204:205]\n"
"v_pk_fma_f32 v[50:51], v[204:205], v[204:205], v[50:51]\n"
"v_lshlrev_b32 v206, 16, v141\n"
"v_and_b32 v207, 0xffff0000, v141\n"
"v_pk_add_f32 v[48:49], v[48:49], v[206:207]\n"
"v_pk_fma_f32 v[50:51], v[206:207], v[206:207], v[50:51]\n"
"v_lshlrev_b32 v208, 16, v142\n"
"v_and_b32 v209, 0xffff0000, v142\n"
"v_pk_add_f32 v[48:49], v[48:49], v[208:209]\n"
"v_pk_fma_f32 v[50:51], v[208:209], v[208:209], v[50:51]\n"
"v_lshlrev_b32 v210, 16, v143\n"
"v_and_b32 v211, 0xffff0000, v143\n"
"v_pk_add_f32 v[48:49], v[48:49], v[210:211]\n"
"v_pk_fma_f32 v[50:51], v[210:211], v[210:211], v[50:51]\n"
"s_add_u32 m0, s99, 90112\n"
"s_add_u32 s46, s96, s97\n"
"s_mov_b64 exec, 0xffff\n"
"buffer_load_dwordx4 v19, s[68:71], s46 offen lds\n"
"s_mov_b64 exec, -1\n"
"s_add_u32 s97, s97, 128\n"
"s_cmp_eq_u32 s97, 1152\n"
"s_cselect_b32 s97, 0, s97\n"
"s_waitcnt vmcnt(4)\n"
"v_lshlrev_b32 v212, 16, v144\n"
"v_and_b32 v213, 0xffff0000, v144\n"
"v_pk_add_f32 v[48:49], v[48:49], v[212:213]\n"
"v_pk_fma_f32 v[50:51], v[212:213], v[212:213], v[50:51]\n"
"v_lshlrev_b32 v214, 16, v145\n"
"v_and_b32 v215, 0xffff0000, v145\n"
"v_pk_add_f32 v[48:49], v[48:49], v[214:215]\n"
"v_pk_fma_f32 v[50:51], v[214:215], v[214:215], v[50:51]\n"
"v_lshlrev_b32 v216, 16, v146\n"
"v_and_b32 v217, 0xffff0000, v146\n"
"v_pk_add_f32 v[48:49], v[48:49], v[216:217]\n"
"v_pk_fma_f32 v[50:51], v[216:217], v[216:217], v[50:51]\n"
"v_lshlrev_b32 v218, 16, v147\n"
"v_and_b32 v219, 0xffff0000, v147\n"
"v_pk_add_f32 v[48:49], v[48:49], v[218:219]\n"
"v_pk_fma_f32 v[50:51], v[218:219], v[218:219], v[50:51]\n"
"s_add_u32 m0, s98, 92160\n"
"s_add_u32 s3, s95, s97\n"
"buffer_load_dwordx4 v2, s[68:71], s3 offen lds\n"
"v_add_f32 v48, v48, v49\n"
"v_add_f32 v50, v50, v51\n"
"s_nop 0\n"
"v_add_f32 v48, v48, v48 quad_perm:[1,0,3,2] row_mask:0xf bank_mask:0xf\n"
"v_add_f32 v50, v50, v50 quad_perm:[1,0,3,2] row_mask:0xf bank_mask:0xf\n"
"s_nop 0\n"
"v_add_f32 v48, v48, v48 quad_perm:[2,3,0,1] row_mask:0xf bank_mask:0xf\n"
"v_add_f32 v50, v50, v50 quad_perm:[2,3,0,1] row_mask:0xf bank_mask:0xf\n"
"s_nop 0\n"
"v_add_f32 v48, v48, v48 row_half_mirror row_mask:0xf bank_mask:0xf\n"
"v_add_f32 v50, v50, v50 row_half_mirror row_mask:0xf bank_mask:0xf\n"
"v_mul_f32 v48, 0x3ae38e39, v48\n"
"v_mul_f32 v49, v48, v48\n"
"v_mul_f32 v50, 0x3ae38e39, v50\n"
"v_sub_f32 v50, v50, v49\n"
"v_max_f32 v50, 0, v50\n"
"v_add_f32 v50, 0x3727c5ac, v50\n"
"v_rsq_f32 v50, v50\n"
"s_lshl_b32 s46, s29, 31\n"
"v_xor_b32 v50, s46, v50\n"
"v_mul_f32 v48, -v48, v50\n"
"v_pk_fma_f32 v[148:149], v[148:149], v[50:51], v[48:49] op_sel_hi:[1,0,0]\n"
"v_pk_fma_f32 v[150:151], v[150:151], v[50:51], v[48:49] op_sel_hi:[1,0,0]\n"
"v_pk_fma_f32 v[152:153], v[152:153], v[50:51], v[48:49] op_sel_hi:[1,0,0]\n"
"v_pk_fma_f32 v[154:155], v[154:155], v[50:51], v[48:49] op_sel_hi:[1,0,0]\n"
"v_cvt_pk_bf16_f32 v148, v148, v149\n"
"v_cvt_pk_bf16_f32 v149, v150, v151\n"
"v_cvt_pk_bf16_f32 v150, v152, v153\n"
"v_cvt_pk_bf16_f32 v151, v154, v155\n"
"ds_write_b128 v6, v[148:151]\n"
"s_add_u32 m0, s98, 100352\n"
"s_add_u32 s46, s3, 73728\n"
"buffer_load_dwordx4 v2, s[68:71], s46 offen lds\n"
"v_pk_fma_f32 v[156:157], v[156:157], v[50:51], v[48:49] op_sel_hi:[1,0,0]\n"
"v_pk_fma_f32 v[158:159], v[158:159], v[50:51], v[48:49] op_sel_hi:[1,0,0]\n"
"v_pk_fma_f32 v[160:161], v[160:161], v[50:51], v[48:49] op_sel_hi:[1,0,0]\n"
"v_pk_fma_f32 v[162:163], v[162:163], v[50:51], v[48:49] op_sel_hi:[1,0,0]\n"
"v_cvt_pk_bf16_f32 v156, v156, v157\n"
"v_cvt_pk_bf16_f32 v157, v158, v159\n"
"v_cvt_pk_bf16_f32 v158, v160, v161\n"
"v_cvt_pk_bf16_f32 v159, v162, v163\n"
"ds_write_b128 v6, v[156:159] offset:8192\n"
"s_add_u32 m0, s99, 108544\n"
"s_add_u32 s46, s96, s97\n"
"s_mov_b64 exec, 0xffff\n"
"buffer_load_dwordx4 v19, s[68:71], s46 offen lds\n"
"s_mov_b64 exec, -1\n"
"s_add_u32 s97, s97, 128\n"
"s_cmp_eq_u32 s97, 1152\n"
"s_cselect_b32 s97, 0, s97\n"
"v_pk_fma_f32 v[164:165], v[164:165], v[50:51], v[48:49] op_sel_hi:[1,0,0]\n"
"v_pk_fma_f32 v[166:167], v[166:167], v[50:51], v[48:49] op_sel_hi:[1,0,0]\n"
"v_pk_fma_f32 v[168:169], v[168:169], v[50:51], v[48:49] op_sel_hi:[1,0,0]\n"
"v_pk_fma_f32 v[170:171], v[170:171], v[50:51], v[48:49] op_sel_hi:[1,0,0]\n"
"v_cvt_pk_bf16_f32 v164, v164, v165\n"
"v_cvt_pk_bf16_f32 v165, v166, v167\n"
"v_cvt_pk_bf16_f32 v166, v168, v169\n"
"v_cvt_pk_bf16_f32 v167, v170, v171\n"
"ds_write_b128 v6, v[164:167] offset:16384\n"
"v_pk_fma_f32 v[172:173], v[172:173], v[50:51], v[48:49] op_sel_hi:[1,0,0]\n"
"v_pk_fma_f32 v[174:175], v[174:175], v[50:51], v[48:49] op_sel_hi:[1,0,0]\n"
"v_pk_fma_f32 v[176:177], v[176:177], v[50:51], v[48:49] op_sel_hi:[1,0,0]\n"
"v_pk_fma_f32 v[178:179], v[178:179], v[50:51], v[48:49] op_sel_hi:[1,0,0]\n"
"v_cvt_pk_bf16_f32 v172, v172, v173\n"
"v_cvt_pk_bf16_f32 v173, v174, v175\n"
"v_cvt_pk_bf16_f32 v174, v176, v177\n"
"v_cvt_pk_bf16_f32 v175, v178, v179\n"
"ds_write_b128 v6, v[172:175] offset:24576\n"
"v_pk_fma_f32 v[180:181], v[180:181], v[50:51], v[48:49] op_sel_hi:[1,0,0]\n"
"v_pk_fma_f32 v[182:183], v[182:183], v[50:51], v[48:49] op_sel_hi:[1,0,0]\n"
"v_pk_fma_f32 v[184:185], v[184:185], v[50:51], v[48:49] op_sel_hi:[1,0,0]\n"
"v_pk_fma_f32 v[186:187], v[186:187], v[50:51], v[48:49] op_sel_hi:[1,0,0]\n"
"v_cvt_pk_bf16_f32 v180, v180, v181\n"
"v_cvt_pk_bf16_f32 v181, v182, v183\n"
"v_cvt_pk_bf16_f32 v182, v184, v185\n"
"v_cvt_pk_bf16_f32 v183, v186, v187\n"
"ds_write_b128 v7, v[180:183]\n"
"v_pk_fma_f32 v[188:189], v[188:189], v[50:51], v[48:49] op_sel_hi:[1,0,0]\n"
"v_pk_fma_f32 v[190:191], v[190:191], v[50:51], v[48:49] op_sel_hi:[1,0,0]\n"
"v_pk_fma_f32 v[192:193], v[192:193], v[50:51], v[48:49] op_sel_hi:[1,0,0]\n"
"v_pk_fma_f32 v[194:195], v[194:195], v[50:51], v[48:49] op_sel_hi:[1,0,0]\n"
"v_cvt_pk_bf16_f32 v188, v188, v189\n"
"v_cvt_pk_bf16_f32 v189, v190, v191\n"
"v_cvt_pk_bf16_f32 v190, v192, v193\n"
"v_cvt_pk_bf16_f32 v191, v194, v195\n"
"ds_write_b128 v7, v[188:191] offset:8192\n"
"v_pk_fma_f32 v[196:197], v[196:197], v[50:51], v[48:49] op_sel_hi:[1,0,0]\n"
"v_pk_fma_f32 v[198:199], v[198:199], v[50:51], v[48:49] op_sel_hi:[1,0,0]\n"
"v_pk_fma_f32 v[200:201], v[200:201], v[50:51], v[48:49] op_sel_hi:[1,0,0]\n"
"v_pk_fma_f32 v[202:203], v[202:203], v[50:51], v[48:49] op_sel_hi:[1,0,0]\n"
"v_cvt_pk_bf16_f32 v196, v196, v197\n"
"v_cvt_pk_bf16_f32 v197, v198, v199\n"
"v_cvt_pk_bf16_f32 v198, v200, v201\n"
"v_cvt_pk_bf16_f32 v199, v202, v203\n"
"ds_write_b128 v7, v[196:199] offset:16384\n"
"v_pk_fma_f32 v[204:205], v[204:205], v[50:51], v[48:49] op_sel_hi:[1,0,0]\n"
"v_pk_fma_f32 v[206:207], v[206:207], v[50:51], v[48:49] op_sel_hi:[1,0,0]\n"
"v_pk_fma_f32 v[208:209], v[208:209], v[50:51], v[48:49] op_sel_hi:[1,0,0]\n"
"v_pk_fma_f32 v[210:211], v[210:211], v[50:51], v[48:49] op_sel_hi:[1,0,0]\n"
"v_cvt_pk_bf16_f32 v204, v204, v205\n"
"v_cvt_pk_bf16_f32 v205, v206, v207\n"
"v_cvt_pk_bf16_f32 v206, v208, v209\n"
"v_cvt_pk_bf16_f32 v207, v210, v211\n"
"ds_write_b128 v7, v[204:207] offset:24576\n"
"v_pk_fma_f32 v[212:213], v[212:213], v[50:51], v[48:49] op_sel_hi:[1,0,0]\n"
"v_pk_fma_f32 v[214:215], v[214:215], v[50:51], v[48:49] op_sel_hi:[1,0,0]\n"
"v_pk_fma_f32 v[216:217], v[216:217], v[50:51], v[48:49] op_sel_hi:[1,0,0]\n"
"v_pk_fma_f32 v[218:219], v[218:219], v[50:51], v[48:49] op_sel_hi:[1,0,0]\n"
"v_cvt_pk_bf16_f32 v212, v212, v213\n"
"v_cvt_pk_bf16_f32 v213, v214, v215\n"
"v_cvt_pk_bf16_f32 v214, v216, v217\n"
"v_cvt_pk_bf16_f32 v215, v218, v219\n"
"ds_write_b128 v7, v[212:215] offset:32768\n"
"s_waitcnt vmcnt(0) lgkmcnt(0)\n"
"s_barrier\n"
"s_cmp_eq_u32 s26, s25\n"
"s_cbranch_scc1 .Lg1h\n"
"ds_read_b128 v[112:115], v72\n"
"ds_read_b128 v[120:123], v76\n"
"ds_read_b128 v[116:119], v72 offset:2048\n"
"ds_read_b128 v[124:127], v76 offset:2048\n"
"ds_read_b128 v[132:135], v73\n"
"ds_read_b128 v[140:143], v77\n"
"ds_read_b128 v[136:139], v73 offset:2048\n"
"ds_read_b128 v[144:147], v77 offset:2048\n"
"s_waitcnt lgkmcnt(6)\n"
"v_mfma_f32_16x16x32_bf16 v[48:51], v[112:115], v[120:123], 0\n"
"ds_read_b128 v[152:155], v72 offset:8192\n"
"s_waitcnt lgkmcnt(6)\n"
"v_mfma_f32_16x16x32_bf16 v[52:55], v[116:119], v[120:123], 0\n"
"ds_read_b128 v[160:163], v76 offset:18432\n"
"s_add_u32 m0, s98, 110592\n"
"s_add_u32 s3, s95, s97\n"
"buffer_load_dwordx4 v2, s[68:71], s3 offen lds\n"
"s_waitcnt lgkmcnt(6)\n"
"v_mfma_f32_16x16x32_bf16 v[60:63], v[116:119], v[124:127], 0\n"
"ds_read_b128 v[156:159], v72 offset:10240\n"
"v_mfma_f32_16x16x32_bf16 v[56:59], v[112:115], v[124:127], 0\n"
"ds_read_b128 v[164:167], v76 offset:20480\n"
"s_add_u32 m0, s98, 118784\n"
"s_add_u32 s46, s3, 73728\n"
"buffer_load_dwordx4 v2, s[68:71], s46 offen lds\n"
"s_waitcnt lgkmcnt(6)\n"
"v_mfma_f32_16x16x32_bf16 v[48:51], v[132:135], v[140:143], v[48:51]\n"
"ds_read_b128 v[172:175], v73 offset:8192\n"
"s_waitcnt lgkmcnt(6)\n"
"v_mfma_f32_16x16x32_bf16 v[52:55], v[136:139], v[140:143], v[52:55]\n"
"ds_read_b128 v[180:183], v77 offset:18432\n"
"s_add_u32 m0, s99, 126976\n"
"s_add_u32 s46, s96, s97\n"
"s_mov_b64 exec, 0xffff\n"
"buffer_load_dwordx4 v19, s[68:71], s46 offen lds\n"
"s_mov_b64 exec, -1\n"
"s_add_u32 s97, s97, 128\n"
"s_cmp_eq_u32 s97, 1152\n"
"s_cselect_b32 s97, 0, s97\n"
"s_waitcnt lgkmcnt(6)\n"
"v_mfma_f32_16x16x32_bf16 v[60:63], v[136:139], v[144:147], v[60:63]\n"
"ds_read_b128 v[176:179], v73 offset:10240\n"
"v_mfma_f32_16x16x32_bf16 v[56:59], v[132:135], v[144:147], v[56:59]\n"
"ds_read_b128 v[184:187], v77 offset:20480\n"
"s_add_u32 m0, s98, 129024\n"
"s_add_u32 s3, s95, s97\n"
"buffer_load_dwordx4 v2, s[68:71], s3 offen lds\n"
"s_waitcnt vmcnt(1)\n"
"s_barrier\n"
"s_waitcnt lgkmcnt(6)\n"
"v_mfma_f32_16x16x32_bf16 v[48:51], v[152:155], v[160:163], v[48:51]\n"
"ds_read_b128 v[112:115], v72 offset:16384\n"
"s_waitcnt lgkmcnt(6)\n"
"v_mfma_f32_16x16x32_bf16 v[52:55], v[156:159], v[160:163], v[52:55]\n"
"ds_read_b128 v[120:123], v76 offset:36864\n"
"s_add_u32 m0, s98, 137216\n"
"s_add_u32 s46, s3, 73728\n"
"buffer_load_dwordx4 v2, s[68:71], s46 offen lds\n"
"s_waitcnt lgkmcnt(6)\n"
"v_mfma_f32_16x16x32_bf16 v[60:63], v[156:159], v[164:167], v[60:63]\n"
"ds_read_b128 v[116:119], v72 offset:18432\n"
"v_mfma_f32_16x16x32_bf16 v[56:59], v[152:155], v[164:167], v[56:59]\n"
"ds_read_b128 v[124:127], v76 offset:38912\n"
"s_add_u32 m0, s99, 145408\n"
"s_add_u32 s46, s96, s97\n"
"s_mov_b64 exec, 0xffff\n"
"buffer_load_dwordx4 v19, s[68:71], s46 offen lds\n"
"s_mov_b64 exec, -1\n"
"s_add_u32 s97, s97, 128\n"
"s_cmp_eq_u32 s97, 1152\n"
"s_cselect_b32 s97, 0, s97\n"
"s_waitcnt lgkmcnt(6)\n"
"v_mfma_f32_16x16x32_bf16 v[48:51], v[172:175], v[180:183], v[48:51]\n"
"ds_read_b128 v[132:135], v73 offset:16384\n"
"s_waitcnt lgkmcnt(6)\n"
"v_mfma_f32_16x16x32_bf16 v[52:55], v[176:179], v[180:183], v[52:55]\n"
"ds_read_b128 v[140:143], v77 offset:36864\n"
"s_add_u32 m0, s98, 73728\n"
"s_add_u32 s3, s95, s97\n"
"s_branch .Lsk1_0\n"
".Lst1_0:\n"
"s_branch .Lbk1\n"
".Lsk1_0:\n"
"buffer_load_dwordx4 v2, s[68:71], s3 offen lds\n"
"s_waitcnt lgkmcnt(6)\n"
"v_mfma_f32_16x16x32_bf16 v[60:63], v[176:179], v[184:187], v[60:63]\n"
"ds_read_b128 v[136:139], v73 offset:18432\n"
"v_mfma_f32_16x16x32_bf16 v[56:59], v[172:175], v[184:187], v[56:59]\n"
"ds_read_b128 v[144:147], v77 offset:38912\n"
"s_add_u32 m0, s98, 81920\n"
"s_add_u32 s46, s3, 73728\n"
"buffer_load_dwordx4 v2, s[68:71], s46 offen lds\n"
"s_waitcnt vmcnt(2)\n"
"s_barrier\n"
"s_waitcnt lgkmcnt(6)\n"
"v_mfma_f32_16x16x32_bf16 v[48:51], v[112:115], v[120:123], v[48:51]\n"
"ds_read_b128 v[152:155], v72 offset:24576\n"
"s_waitcnt lgkmcnt(6)\n"
"v_mfma_f32_16x16x32_bf16 v[52:55], v[116:119], v[120:123], v[52:55]\n"
"ds_read_b128 v[160:163], v76 offset:55296\n"
"s_add_u32 m0, s99, 90112\n"
"s_add_u32 s46, s96, s97\n"
"s_mov_b64 exec, 0xffff\n"
"buffer_load_dwordx4 v19, s[68:71], s46 offen lds\n"
"s_mov_b64 exec, -1\n"
"s_add_u32 s97, s97, 128\n"
"s_cmp_eq_u32 s97, 1152\n"
"s_cselect_b32 s97, 0, s97\n"
"s_waitcnt lgkmcnt(6)\n"
"v_mfma_f32_16x16x32_bf16 v[60:63], v[116:119], v[124:127], v[60:63]\n"
"ds_read_b128 v[156:159], v72 offset:26624\n"
"v_mfma_f32_16x16x32_bf16 v[56:59], v[112:115], v[124:127], v[56:59]\n"
"ds_read_b128 v[164:167], v76 offset:57344\n"
"s_add_u32 m0, s98, 92160\n"
"s_add_u32 s3, s95, s97\n"
"buffer_load_dwordx4 v2, s[68:71], s3 offen lds\n"
"s_waitcnt lgkmcnt(6)\n"
"v_mfma_f32_16x16x32_bf16 v[48:51], v[132:135], v[140:143], v[48:51]\n"
"ds_read_b128 v[172:175], v73 offset:24576\n"
"s_waitcnt lgkmcnt(6)\n"
"v_mfma_f32_16x16x32_bf16 v[52:55], v[136:139], v[140:143], v[52:55]\n"
"ds_read_b128 v[180:183], v77 offset:55296\n"
"s_add_u32 m0, s98, 100352\n"
"s_add_u32 s46, s3, 73728\n"
"buffer_load_dwordx4 v2, s[68:71], s46 offen lds\n"
"s_waitcnt lgkmcnt(6)\n"
"v_mfma_f32_16x16x32_bf16 v[60:63], v[136:139], v[144:147], v[60:63]\n"
"ds_read_b128 v[176:179], v73 offset:26624\n"
"v_mfma_f32_16x16x32_bf16 v[56:59], v[132:135], v[144:147], v[56:59]\n"
"ds_read_b128 v[184:187], v77 offset:57344\n"
"s_add_u32 m0, s99, 108544\n"
"s_add_u32 s46, s96, s97\n"
"s_mov_b64 exec, 0xffff\n"
"buffer_load_dwordx4 v19, s[68:71], s46 offen lds\n"
"s_mov_b64 exec, -1\n"
"s_add_u32 s97, s97, 128\n"
"s_cmp_eq_u32 s97, 1152\n"
"s_cselect_b32 s97, 0, s97\n"
"s_waitcnt vmcnt(3)\n"
"s_barrier\n"
"s_waitcnt lgkmcnt(6)\n"
"v_mfma_f32_16x16x32_bf16 v[48:51], v[152:155], v[160:163], v[48:51]\n"
"ds_read_b128 v[112:115], v74\n"
"s_waitcnt lgkmcnt(6)\n"
"v_mfma_f32_16x16x32_bf16 v[52:55], v[156:159], v[160:163], v[52:55]\n"
"ds_read_b128 v[120:123], v76\n"
"s_add_u32 m0, s98, 110592\n"
"s_add_u32 s3, s95, s97\n"
"buffer_load_dwordx4 v2, s[68:71], s3 offen lds\n"
"s_waitcnt lgkmcnt(6)\n"
"v_mfma_f32_16x16x32_bf16 v[60:63], v[156:159], v[164:167], v[60:63]\n"
"ds_read_b128 v[116:119], v74 offset:2048\n"
"v_mfma_f32_16x16x32_bf16 v[56:59], v[152:155], v[164:167], v[56:59]\n"
"ds_read_b128 v[124:127], v76 offset:2048\n"
"s_add_u32 m0, s98, 118784\n"
"s_add_u32 s46, s3, 73728\n"
"buffer_load_dwordx4 v2, s[68:71], s46 offen lds\n"
"s_waitcnt lgkmcnt(6)\n"
"v_mfma_f32_16x16x32_bf16 v[48:51], v[172:175], v[180:183], v[48:51]\n"
"ds_read_b128 v[132:135], v75\n"
"s_waitcnt lgkmcnt(6)\n"
"v_mfma_f32_16x16x32_bf16 v[52:55], v[176:179], v[180:183], v[52:55]\n"
"ds_read_b128 v[140:143], v77\n"
"s_add_u32 m0, s99, 126976\n"
"s_add_u32 s46, s96, s97\n"
"s_mov_b64 exec, 0xffff\n"
"buffer_load_dwordx4 v19, s[68:71], s46 offen lds\n"
"s_mov_b64 exec, -1\n"
"s_add_u32 s97, s97, 128\n"
"s_cmp_eq_u32 s97, 1152\n"
"s_cselect_b32 s97, 0, s97\n"
"s_waitcnt lgkmcnt(6)\n"
"v_mfma_f32_16x16x32_bf16 v[60:63], v[176:179], v[184:187], v[60:63]\n"
"ds_read_b128 v[136:139], v75 offset:2048\n"
"v_mfma_f32_16x16x32_bf16 v[56:59], v[172:175], v[184:187], v[56:59]\n"
"ds_read_b128 v[144:147], v77 offset:2048\n"
"s_waitcnt vmcnt(3)\n"
"s_barrier\n"
"s_waitcnt lgkmcnt(6)\n"
"v_mfma_f32_16x16x32_bf16 v[48:51], v[112:115], v[120:123], v[48:51]\n"
"ds_read_b128 v[152:155], v74 offset:8192\n"
"s_waitcnt lgkmcnt(6)\n"
"v_mfma_f32_16x16x32_bf16 v[52:55], v[116:119], v[120:123], v[52:55]\n"
"ds_read_b128 v[160:163], v76 offset:18432\n"
"s_add_u32 m0, s98, 129024\n"
"s_add_u32 s3, s95, s97\n"
"buffer_load_dwordx4 v2, s[68:71], s3 offen lds\n"
"s_waitcnt lgkmcnt(6)\n"
"v_mfma_f32_16x16x32_bf16 v[60:63], v[116:119], v[124:127], v[60:63]\n"
"ds_read_b128 v[156:159], v74 offset:10240\n"
"v_mfma_f32_16x16x32_bf16 v[56:59], v[112:115], v[124:127], v[56:59]\n"
"ds_read_b128 v[164:167], v76 offset:20480\n"
"s_add_u32 m0, s98, 137216\n"
"s_add_u32 s46, s3, 73728\n"
"buffer_load_dwordx4 v2, s[68:71], s46 offen lds\n"
"s_waitcnt lgkmcnt(6)\n"
"v_mfma_f32_16x16x32_bf16 v[48:51], v[132:135], v[140:143], v[48:51]\n"
"ds_read_b128 v[172:175], v75 offset:8192\n"
"s_waitcnt lgkmcnt(6)\n"
"v_mfma_f32_16x16x32_bf16 v[52:55], v[136:139], v[140:143], v[52:55]\n"
"ds_read_b128 v[180:183], v77 offset:18432\n"
"s_add_u32 m0, s99, 145408\n"
"s_add_u32 s46, s96, s97\n"
"s_mov_b64 exec, 0xffff\n"
"buffer_load_dwordx4 v19, s[68:71], s46 offen lds\n"
"s_mov_b64 exec, -1\n"
"s_add_u32 s97, s97, 128\n"
"s_cmp_eq_u32 s97, 1152\n"
"s_cselect_b32 s97, 0, s97\n"
"s_waitcnt lgkmcnt(6)\n"
"v_mfma_f32_16x16x32_bf16 v[60:63], v[136:139], v[144:147], v[60:63]\n"
"ds_read_b128 v[176:179], v75 offset:10240\n"
"v_mfma_f32_16x16x32_bf16 v[56:59], v[132:135], v[144:147], v[56:59]\n"
"ds_read_b128 v[184:187], v77 offset:20480\n"
"s_waitcnt vmcnt(3)\n"
"s_barrier\n"
"s_waitcnt lgkmcnt(6)\n"
"v_mfma_f32_16x16x32_bf16 v[48:51], v[152:155], v[160:163], v[48:51]\n"
"ds_read_b128 v[112:115], v74 offset:16384\n"
"s_waitcnt lgkmcnt(6)\n"
"v_mfma_f32_16x16x32_bf16 v[52:55], v[156:159], v[160:163], v[52:55]\n"
"ds_read_b128 v[120:123], v76 offset:36864\n"
"s_add_u32 m0, s98, 73728\n"
"s_add_u32 s3, s95, s97\n"
"buffer_load_dwordx4 v2, s[68:71], s3 offen lds\n"
"s_waitcnt lgkmcnt(6)\n"
"v_mfma_f32_16x16x32_bf16 v[60:63], v[156:159], v[164:167], v[60:63]\n"
"ds_read_b128 v[116:119], v74 offset:18432\n"
"v_mfma_f32_16x16x32_bf16 v[56:59], v[152:155], v[164:167], v[56:59]\n"
"ds_read_b128 v[124:127], v76 offset:38912\n"
"s_add_u32 m0, s98, 81920\n"
"s_add_u32 s46, s3, 73728\n"
"buffer_load_dwordx4 v2, s[68:71], s46 offen lds\n"
"s_waitcnt lgkmcnt(6)\n"
"v_mfma_f32_16x16x32_bf16 v[48:51], v[172:175], v[180:183], v[48:51]\n"
"ds_read_b128 v[132:135], v75 offset:16384\n"
"s_waitcnt lgkmcnt(6)\n"
"v_mfma_f32_16x16x32_bf16 v[52:55], v[176:179], v[180:183], v[52:55]\n"
"ds_read_b128 v[140:143], v77 offset:36864\n"
"s_add_u32 m0, s99, 90112\n"
"s_add_u32 s46, s96, s97\n"
"s_mov_b64 exec, 0xffff\n"
"buffer_load_dwordx4 v19, s[68:71], s46 offen lds\n"
"s_mov_b64 exec, -1\n"
"s_waitcnt lgkmcnt(6)\n"
"v_mfma_f32_16x16x32_bf16 v[60:63], v[176:179], v[184:187], v[60:63]\n"
"ds_read_b128 v[136:139], v75 offset:18432\n"
"v_mfma_f32_16x16x32_bf16 v[56:59], v[172:175], v[184:187], v[56:59]\n"
"ds_read_b128 v[144:147], v77 offset:38912\n"
"s_waitcnt vmcnt(3)\n"
"s_barrier\n"
"s_waitcnt lgkmcnt(6)\n"
"v_mfma_f32_16x16x32_bf16 v[48:51], v[112:115], v[120:123], v[48:51]\n"
"ds_read_b128 v[152:155], v74 offset:24576\n"
"s_waitcnt lgkmcnt(6)\n"
"v_mfma_f32_16x16x32_bf16 v[52:55], v[116:119], v[120:123], v[52:55]\n"
"ds_read_b128 v[160:163], v76 offset:55296\n"
"v_readfirstlane_b32 s36, v16\n"
"v_readfirstlane_b32 s37, v17\n"
"s_nop 1\n"
"s_mul_i32 s36, s36, 0x85ebca6b\n"
"s_add_u32 s36, s36, 0x7f4a7c15\n"
"s_add_u32 s37, s37, 0x9e3779b9\n"
"s_and_b32 s36, s36, 0xfffffff0\n"
"s_or_b32 s38, s36, 15\n"
"s_mov_b32 s39, s37\n"
"s_mov_b32 s41, s37\n"
"s_waitcnt lgkmcnt(6)\n"
"v_mfma_f32_16x16x32_bf16 v[60:63], v[116:119], v[124:127], v[60:63]\n"
"ds_read_b128 v[156:159], v74 offset:26624\n"
"v_mfma_f32_16x16x32_bf16 v[56:59], v[112:115], v[124:127], v[56:59]\n"
"ds_read_b128 v[164:167], v76 offset:57344\n"
"v_mov_b32 v12, 0\n"
"v_mov_b32 v13, 0\n"
"v_lshrrev_b32 v3, 4, v0\n"
"v_mul_u32_u24 v3, 262016, v3\n"
"v_lshl_add_u32 v3, v0, 3, v3\n"
"s_add_u32 s45, s79, 524288\n"
"buffer_store_dwordx2 v[12:13], v3, s[48:51], s45 offen sc1\n"
"s_mov_b64 exec, 0xffffffff\n"
"s_mov_b32 exec_hi, 0\n"
"buffer_store_dwordx2 v[12:13], v3, s[48:51], s79 offen sc1\n"
"s_mov_b64 exec, -1\n"
"s_waitcnt lgkmcnt(6)\n"
"v_mfma_f32_16x16x32_bf16 v[48:51], v[132:135], v[140:143], v[48:51]\n"
"ds_read_b128 v[172:175], v75 offset:24576\n"
"s_waitcnt lgkmcnt(6)\n"
"v_mfma_f32_16x16x32_bf16 v[52:55], v[136:139], v[140:143], v[52:55]\n"
"ds_read_b128 v[180:183], v77 offset:55296\n"
"s_waitcnt lgkmcnt(6)\n"
"v_mfma_f32_16x16x32_bf16 v[60:63], v[136:139], v[144:147], v[60:63]\n"
"ds_read_b128 v[176:179], v75 offset:26624\n"
"v_mfma_f32_16x16x32_bf16 v[56:59], v[132:135], v[144:147], v[56:59]\n"
"ds_read_b128 v[184:187], v77 offset:57344\n"
"s_waitcnt vmcnt(2)\n"
"s_barrier\n"
"s_waitcnt lgkmcnt(6)\n"
"v_mfma_f32_16x16x32_bf16 v[48:51], v[152:155], v[160:163], v[48:51]\n"
"ds_read_b128 v[112:115], v74 offset:32768\n"
"s_waitcnt lgkmcnt(6)\n"
"v_mfma_f32_16x16x32_bf16 v[52:55], v[156:159], v[160:163], v[52:55]\n"
"ds_read_b128 v[120:123], v76\n"
"s_waitcnt lgkmcnt(6)\n"
"v_mfma_f32_16x16x32_bf16 v[60:63], v[156:159], v[164:167], v[60:63]\n"
"ds_read_b128 v[116:119], v74 offset:34816\n"
"v_mfma_f32_16x16x32_bf16 v[56:59], v[152:155], v[164:167], v[56:59]\n"
"ds_read_b128 v[124:127], v76 offset:2048\n"
"s_waitcnt lgkmcnt(6)\n"
"v_mfma_f32_16x16x32_bf16 v[48:51], v[172:175], v[180:183], v[48:51]\n"
"ds_read_b128 v[132:135], v75 offset:32768\n"
"s_waitcnt lgkmcnt(6)\n"
"v_mfma_f32_16x16x32_bf16 v[52:55], v[176:179], v[180:183], v[52:55]\n"
"ds_read_b128 v[140:143], v77\n"
"s_waitcnt lgkmcnt(6)\n"
"v_mfma_f32_16x16x32_bf16 v[60:63], v[176:179], v[184:187], v[60:63]\n"
"ds_read_b128 v[136:139], v75 offset:34816\n"
"v_mfma_f32_16x16x32_bf16 v[56:59], v[172:175], v[184:187], v[56:59]\n"
"ds_read_b128 v[144:147], v77 offset:2048\n"
"s_waitcnt lgkmcnt(6)\n"
"v_mfma_f32_16x16x32_bf16 v[48:51], v[112:115], v[120:123], v[48:51]\n"
"s_waitcnt lgkmcnt(5)\n"
"v_mfma_f32_16x16x32_bf16 v[52:55], v[116:119], v[120:123], v[52:55]\n"
"s_waitcnt lgkmcnt(4)\n"
"v_mfma_f32_16x16x32_bf16 v[60:63], v[116:119], v[124:127], v[60:63]\n"
"v_mfma_f32_16x16x32_bf16 v[56:59], v[112:115], v[124:127], v[56:59]\n"
"s_waitcnt lgkmcnt(2)\n"
"v_mfma_f32_16x16x32_bf16 v[48:51], v[132:135], v[140:143], v[48:51]\n"
"s_waitcnt lgkmcnt(1)\n"
"v_mfma_f32_16x16x32_bf16 v[52:55], v[136:139], v[140:143], v[52:55]\n"
"s_waitcnt lgkmcnt(0)\n"
"v_mfma_f32_16x16x32_bf16 v[60:63], v[136:139], v[144:147], v[60:63]\n"
"v_mfma_f32_16x16x32_bf16 v[56:59], v[132:135], v[144:147], v[56:59]\n"
"s_branch .Lg1j\n"
".Lsa:\n"
"s_branch .Lba\n"
".Lg1h:\n"
"s_setprio 1\n"
"ds_read_b128 v[112:115], v72\n"
"ds_read_b128 v[120:123], v76\n"
"ds_read_b128 v[116:119], v72 offset:2048\n"
"ds_read_b128 v[124:127], v76 offset:2048\n"
"ds_read_b128 v[128:131], v80\n"
"ds_read_b128 v[132:135], v73\n"
"ds_read_b128 v[140:143], v77\n"
"ds_read_b128 v[136:139], v73 offset:2048\n"
"ds_read_b128 v[144:147], v77 offset:2048\n"
"ds_read_b128 v[148:151], v81\n"
"s_waitcnt lgkmcnt(8)\n"
"v_mfma_f32_16x16x32_bf16 v[48:51], v[112:115], v[120:123], 0\n"
"ds_read_b128 v[152:155], v72 offset:8192\n"
"s_waitcnt lgkmcnt(8)\n"
"v_mfma_f32_16x16x32_bf16 v[52:55], v[116:119], v[120:123], 0\n"
"ds_read_b128 v[160:163], v76 offset:18432\n"
"s_add_u32 m0, s98, 110592\n"
"s_add_u32 s3, s95, s97\n"
"buffer_load_dwordx4 v2, s[68:71], s3 offen lds\n"
"s_waitcnt lgkmcnt(8)\n"
"v_mfma_f32_16x16x32_bf16 v[60:63], v[116:119], v[124:127], 0\n"
"ds_read_b128 v[156:159], v72 offset:10240\n"
"v_mfma_f32_16x16x32_bf16 v[56:59], v[112:115], v[124:127], 0\n"
"ds_read_b128 v[164:167], v76 offset:20480\n"
"s_add_u32 m0, s98, 118784\n"
"s_add_u32 s46, s3, 73728\n"
"buffer_load_dwordx4 v2, s[68:71], s46 offen lds\n"
"s_waitcnt lgkmcnt(9)\n"
"v_mfma_f32_16x16x32_bf16 v[64:67], v[112:115], v[128:131], 0\n"
"ds_read_b128 v[168:171], v80 offset:18432\n"
"v_mfma_f32_16x16x32_bf16 v[68:71], v[116:119], v[128:131], 0\n"
"s_waitcnt lgkmcnt(8)\n"
"v_mfma_f32_16x16x32_bf16 v[48:51], v[132:135], v[140:143], v[48:51]\n"
"ds_read_b128 v[172:175], v73 offset:8192\n"
"s_waitcnt lgkmcnt(8)\n"
"v_mfma_f32_16x16x32_bf16 v[52:55], v[136:139], v[140:143], v[52:55]\n"
"ds_read_b128 v[180:183], v77 offset:18432\n"
"s_add_u32 m0, s99, 126976\n"
"s_add_u32 s46, s96, s97\n"
"s_mov_b64 exec, 0xffff\n"
"buffer_load_dwordx4 v19, s[68:71], s46 offen lds\n"
"s_mov_b64 exec, -1\n"
"s_add_u32 s97, s97, 128\n"
"s_cmp_eq_u32 s97, 1152\n"
"s_cselect_b32 s97, 0, s97\n"
"s_waitcnt lgkmcnt(8)\n"
"v_mfma_f32_16x16x32_bf16 v[60:63], v[136:139], v[144:147], v[60:63]\n"
"ds_read_b128 v[176:179], v73 offset:10240\n"
"v_mfma_f32_16x16x32_bf16 v[56:59], v[132:135], v[144:147], v[56:59]\n"
"ds_read_b128 v[184:187], v77 offset:20480\n"
"s_add_u32 m0, s98, 129024\n"
"s_add_u32 s3, s95, s97\n"
"buffer_load_dwordx4 v2, s[68:71], s3 offen lds\n"
"s_waitcnt lgkmcnt(9)\n"
"v_mfma_f32_16x16x32_bf16 v[64:67], v[132:135], v[148:151], v[64:67]\n"
"ds_read_b128 v[188:191], v81 offset:18432\n"
"v_mfma_f32_16x16x32_bf16 v[68:71], v[136:139], v[148:151], v[68:71]\n"
"s_waitcnt vmcnt(1)\n"
"s_barrier\n"
"s_waitcnt lgkmcnt(8)\n"
"v_mfma_f32_16x16x32_bf16 v[48:51], v[152:155], v[160:163], v[48:51]\n"
"ds_read_b128 v[112:115], v72 offset:16384\n"
"s_waitcnt lgkmcnt(8)\n"
"v_mfma_f32_16x16x32_bf16 v[52:55], v[156:159], v[160:163], v[52:55]\n"
"ds_read_b128 v[120:123], v76 offset:36864\n"
"s_add_u32 m0, s98, 137216\n"
"s_add_u32 s46, s3, 73728\n"
"buffer_load_dwordx4 v2, s[68:71], s46 offen lds\n"
"s_waitcnt lgkmcnt(8)\n"
"v_mfma_f32_16x16x32_bf16 v[60:63], v[156:159], v[164:167], v[60:63]\n"
"ds_read_b128 v[116:119], v72 offset:18432\n"
"v_mfma_f32_16x16x32_bf16 v[56:59], v[152:155], v[164:167], v[56:59]\n"
"ds_read_b128 v[124:127], v76 offset:38912\n"
"s_add_u32 m0, s99, 145408\n"
"s_add_u32 s46, s96, s97\n"
"s_mov_b64 exec, 0xffff\n"
"buffer_load_dwordx4 v19, s[68:71], s46 offen lds\n"
"s_mov_b64 exec, -1\n"
"s_add_u32 s97, s97, 128\n"
"s_cmp_eq_u32 s97, 1152\n"
"s_cselect_b32 s97, 0, s97\n"
"s_waitcnt lgkmcnt(9)\n"
"v_mfma_f32_16x16x32_bf16 v[64:67], v[152:155], v[168:171], v[64:67]\n"
"ds_read_b128 v[128:131], v80 offset:36864\n"
"v_mfma_f32_16x16x32_bf16 v[68:71], v[156:159], v[168:171], v[68:71]\n"
"s_waitcnt lgkmcnt(8)\n"
"v_mfma_f32_16x16x32_bf16 v[48:51], v[172:175], v[180:183], v[48:51]\n"
"ds_read_b128 v[132:135], v73 offset:16384\n"
"s_waitcnt lgkmcnt(8)\n"
"v_mfma_f32_16x16x32_bf16 v[52:55], v[176:179], v[180:183], v[52:55]\n"
"ds_read_b128 v[140:143], v77 offset:36864\n"
"s_add_u32 m0, s98, 73728\n"
"s_add_u32 s3, s95, s97\n"
"buffer_load_dwordx4 v2, s[68:71], s3 offen lds\n"
"s_waitcnt lgkmcnt(8)\n"
"v_mfma_f32_16x16x32_bf16 v[60:63], v[176:179], v[184:187], v[60:63]\n"
"ds_read_b128 v[136:139], v73 offset:18432\n"
"v_mfma_f32_16x16x32_bf16 v[56:59], v[172:175], v[184:187], v[56:59]\n"
"ds_read_b128 v[144:147], v77 offset:38912\n"
"s_add_u32 m0, s98, 81920\n"
"s_add_u32 s46, s3, 73728\n"
"buffer_load_dwordx4 v2, s[68:71], s46 offen lds\n"
"s_waitcnt lgkmcnt(9)\n"
"v_mfma_f32_16x16x32_bf16 v[64:67], v[172:175], v[188:191], v[64:67]\n"
"ds_read_b128 v[148:151], v81 offset:36864\n"
"v_mfma_f32_16x16x32_bf16 v[68:71], v[176:179], v[188:191], v[68:71]\n"
"s_waitcnt vmcnt(2)\n"
"s_barrier\n"
"s_waitcnt lgkmcnt(8)\n"
"v_mfma_f32_16x16x32_bf16 v[48:51], v[112:115], v[120:123], v[48:51]\n"
"ds_read_b128 v[152:155], v72 offset:24576\n"
"s_waitcnt lgkmcnt(8)\n"
"v_mfma_f32_16x16x32_bf16 v[52:55], v[116:119], v[120:123], v[52:55]\n"
"ds_read_b128 v[160:163], v76 offset:55296\n"
"s_add_u32 m0, s99, 90112\n"
"s_add_u32 s46, s96, s97\n"
"s_mov_b64 exec, 0xffff\n"
"buffer_load_dwordx4 v19, s[68:71], s46 offen lds\n"
"s_mov_b64 exec, -1\n"
"s_add_u32 s97, s97, 128\n"
"s_cmp_eq_u32 s97, 1152\n"
"s_cselect_b32 s97, 0, s97\n"
"s_waitcnt lgkmcnt(8)\n"
"v_mfma_f32_16x16x32_bf16 v[60:63], v[116:119], v[124:127], v[60:63]\n"
"ds_read_b128 v[156:159], v72 offset:26624\n"
"v_mfma_f32_16x16x32_bf16 v[56:59], v[112:115], v[124:127], v[56:59]\n"
"ds_read_b128 v[164:167], v76 offset:57344\n"
"s_add_u32 m0, s98, 92160\n"
"s_add_u32 s3, s95, s97\n"
"buffer_load_dwordx4 v2, s[68:71], s3 offen lds\n"
"s_waitcnt lgkmcnt(9)\n"
"v_mfma_f32_16x16x32_bf16 v[64:67], v[112:115], v[128:131], v[64:67]\n"
"ds_read_b128 v[168:171], v80 offset:55296\n"
"v_mfma_f32_16x16x32_bf16 v[68:71], v[116:119], v[128:131], v[68:71]\n"
"s_waitcnt lgkmcnt(8)\n"
"v_mfma_f32_16x16x32_bf16 v[48:51], v[132:135], v[140:143], v[48:51]\n"
"ds_read_b128 v[172:175], v73 offset:24576\n"
"s_waitcnt lgkmcnt(8)\n"
"v_mfma_f32_16x16x32_bf16 v[52:55], v[136:139], v[140:143], v[52:55]\n"
"ds_read_b128 v[180:183], v77 offset:55296\n"
"s_add_u32 m0, s98, 100352\n"
"s_add_u32 s46, s3, 73728\n"
"buffer_load_dwordx4 v2, s[68:71], s46 offen lds\n"
"s_waitcnt lgkmcnt(8)\n"
"v_mfma_f32_16x16x32_bf16 v[60:63], v[136:139], v[144:147], v[60:63]\n"
"ds_read_b128 v[176:179], v73 offset:26624\n"
"v_mfma_f32_16x16x32_bf16 v[56:59], v[132:135], v[144:147], v[56:59]\n"
"ds_read_b128 v[184:187], v77 offset:57344\n"
"s_add_u32 m0, s99, 108544\n"
"s_add_u32 s46, s96, s97\n"
"s_mov_b64 exec, 0xffff\n"
"buffer_load_dwordx4 v19, s[68:71], s46 offen lds\n"
"s_mov_b64 exec, -1\n"
"s_add_u32 s97, s97, 128\n"
"s_cmp_eq_u32 s97, 1152\n"
"s_cselect_b32 s97, 0, s97\n"
"s_waitcnt lgkmcnt(9)\n"
"v_mfma_f32_16x16x32_bf16 v[64:67], v[132:135], v[148:151], v[64:67]\n"
"ds_read_b128 v[188:191], v81 offset:55296\n"
"v_mfma_f32_16x16x32_bf16 v[68:71], v[136:139], v[148:151], v[68:71]\n"
"s_waitcnt vmcnt(3)\n"
"s_barrier\n"
"s_waitcnt lgkmcnt(8)\n"
"v_mfma_f32_16x16x32_bf16 v[48:51], v[152:155], v[160:163], v[48:51]\n"
"ds_read_b128 v[112:115], v74\n"
"s_waitcnt lgkmcnt(8)\n"
"v_mfma_f32_16x16x32_bf16 v[52:55], v[156:159], v[160:163], v[52:55]\n"
"ds_read_b128 v[120:123], v76\n"
"s_add_u32 m0, s98, 110592\n"
"s_add_u32 s3, s95, s97\n"
"buffer_load_dwordx4 v2, s[68:71], s3 offen lds\n"
"s_waitcnt lgkmcnt(8)\n"
"v_mfma_f32_16x16x32_bf16 v[60:63], v[156:159], v[164:167], v[60:63]\n"
"ds_read_b128 v[116:119], v74 offset:2048\n"
"v_mfma_f32_16x16x32_bf16 v[56:59], v[152:155], v[164:167], v[56:59]\n"
"ds_read_b128 v[124:127], v76 offset:2048\n"
"s_add_u32 m0, s98, 118784\n"
"s_add_u32 s46, s3, 73728\n"
"buffer_load_dwordx4 v2, s[68:71], s46 offen lds\n"
"s_waitcnt lgkmcnt(9)\n"
"v_mfma_f32_16x16x32_bf16 v[64:67], v[152:155], v[168:171], v[64:67]\n"
"ds_read_b128 v[128:131], v80\n"
"v_mfma_f32_16x16x32_bf16 v[68:71], v[156:159], v[168:171], v[68:71]\n"
"s_waitcnt lgkmcnt(8)\n"
"v_mfma_f32_16x16x32_bf16 v[48:51], v[172:175], v[180:183], v[48:51]\n"
"ds_read_b128 v[132:135], v75\n"
"s_waitcnt lgkmcnt(8)\n"
"v_mfma_f32_16x16x32_bf16 v[52:55], v[176:179], v[180:183], v[52:55]\n"
"ds_read_b128 v[140:143], v77\n"
"s_add_u32 m0, s99, 126976\n"
"s_add_u32 s46, s96, s97\n"
"s_mov_b64 exec, 0xffff\n"
"buffer_load_dwordx4 v19, s[68:71], s46 offen lds\n"
"s_mov_b64 exec, -1\n"
"s_add_u32 s97, s97, 128\n"
"s_cmp_eq_u32 s97, 1152\n"
"s_cselect_b32 s97, 0, s97\n"
"s_waitcnt lgkmcnt(8)\n"
"v_mfma_f32_16x16x32_bf16 v[60:63], v[176:179], v[184:187], v[60:63]\n"
"ds_read_b128 v[136:139], v75 offset:2048\n"
"v_mfma_f32_16x16x32_bf16 v[56:59], v[172:175], v[184:187], v[56:59]\n"
"ds_read_b128 v[144:147], v77 offset:2048\n"
"s_waitcnt lgkmcnt(9)\n"
"v_mfma_f32_16x16x32_bf16 v[64:67], v[172:175], v[188:191], v[64:67]\n"
"ds_read_b128 v[148:151], v81\n"
"v_mfma_f32_16x16x32_bf16 v[68:71], v[176:179], v[188:191], v[68:71]\n"
"s_waitcnt vmcnt(3)\n"
"s_barrier\n"
"s_waitcnt lgkmcnt(8)\n"
"v_mfma_f32_16x16x32_bf16 v[48:51], v[112:115], v[120:123], v[48:51]\n"
"ds_read_b128 v[152:155], v74 offset:8192\n"
"s_waitcnt lgkmcnt(8)\n"
"v_mfma_f32_16x16x32_bf16 v[52:55], v[116:119], v[120:123], v[52:55]\n"
"ds_read_b128 v[160:163], v76 offset:18432\n"
"s_add_u32 m0, s98, 129024\n"
"s_add_u32 s3, s95, s97\n"
"buffer_load_dwordx4 v2, s[68:71], s3 offen lds\n"
"s_waitcnt lgkmcnt(8)\n"
"v_mfma_f32_16x16x32_bf16 v[60:63], v[116:119], v[124:127], v[60:63]\n"
"ds_read_b128 v[156:159], v74 offset:10240\n"
"v_mfma_f32_16x16x32_bf16 v[56:59], v[112:115], v[124:127], v[56:59]\n"
"ds_read_b128 v[164:167], v76 offset:20480\n"
"s_add_u32 m0, s98, 137216\n"
"s_add_u32 s46, s3, 73728\n"
"buffer_load_dwordx4 v2, s[68:71], s46 offen lds\n"
"s_waitcnt lgkmcnt(9)\n"
"v_mfma_f32_16x16x32_bf16 v[64:67], v[112:115], v[128:131], v[64:67]\n"
"ds_read_b128 v[168:171], v80 offset:18432\n"
"v_mfma_f32_16x16x32_bf16 v[68:71], v[116:119], v[128:131], v[68:71]\n"
"s_waitcnt lgkmcnt(8)\n"
"v_mfma_f32_16x16x32_bf16 v[48:51], v[132:135], v[140:143], v[48:51]\n"
"ds_read_b128 v[172:175], v75 offset:8192\n"
"s_waitcnt lgkmcnt(8)\n"
"v_mfma_f32_16x16x32_bf16 v[52:55], v[136:139], v[140:143], v[52:55]\n"
"ds_read_b128 v[180:183], v77 offset:18432\n"
"s_add_u32 m0, s99, 145408\n"
"s_add_u32 s46, s96, s97\n"
"s_mov_b64 exec, 0xffff\n"
"buffer_load_dwordx4 v19, s[68:71], s46 offen lds\n"
"s_mov_b64 exec, -1\n"
"s_add_u32 s97, s97, 128\n"
"s_cmp_eq_u32 s97, 1152\n"
"s_cselect_b32 s97, 0, s97\n"
"s_waitcnt lgkmcnt(8)\n"
"v_mfma_f32_16x16x32_bf16 v[60:63], v[136:139], v[144:147], v[60:63]\n"
"ds_read_b128 v[176:179], v75 offset:10240\n"
"v_mfma_f32_16x16x32_bf16 v[56:59], v[132:135], v[144:147], v[56:59]\n"
"ds_read_b128 v[184:187], v77 offset:20480\n"
"s_waitcnt lgkmcnt(9)\n"
"v_mfma_f32_16x16x32_bf16 v[64:67], v[132:135], v[148:151], v[64:67]\n"
"ds_read_b128 v[188:191], v81 offset:18432\n"
"v_mfma_f32_16x16x32_bf16 v[68:71], v[136:139], v[148:151], v[68:71]\n"
"s_waitcnt vmcnt(3)\n"
"s_barrier\n"
"s_waitcnt lgkmcnt(8)\n"
"v_mfma_f32_16x16x32_bf16 v[48:51], v[152:155], v[160:163], v[48:51]\n"
"ds_read_b128 v[112:115], v74 offset:16384\n"
"s_waitcnt lgkmcnt(8)\n"
"v_mfma_f32_16x16x32_bf16 v[52:55], v[156:159], v[160:163], v[52:55]\n"
"ds_read_b128 v[120:123], v76 offset:36864\n"
"s_add_u32 m0, s98, 73728\n"
"s_add_u32 s3, s95, s97\n"
"buffer_load_dwordx4 v2, s[68:71], s3 offen lds\n"
"s_waitcnt lgkmcnt(8)\n"
"v_mfma_f32_16x16x32_bf16 v[60:63], v[156:159], v[164:167], v[60:63]\n"
"ds_read_b128 v[116:119], v74 offset:18432\n"
"v_mfma_f32_16x16x32_bf16 v[56:59], v[152:155], v[164:167], v[56:59]\n"
"ds_read_b128 v[124:127], v76 offset:38912\n"
"s_add_u32 m0, s98, 81920\n"
"s_add_u32 s46, s3, 73728\n"
"buffer_load_dwordx4 v2, s[68:71], s46 offen lds\n"
"s_waitcnt lgkmcnt(9)\n"
"v_mfma_f32_16x16x32_bf16 v[64:67], v[152:155], v[168:171], v[64:67]\n"
"ds_read_b128 v[128:131], v80 offset:36864\n"
"v_mfma_f32_16x16x32_bf16 v[68:71], v[156:159], v[168:171], v[68:71]\n"
"s_waitcnt lgkmcnt(8)\n"
"v_mfma_f32_16x16x32_bf16 v[48:51], v[172:175], v[180:183], v[48:51]\n"
"ds_read_b128 v[132:135], v75 offset:16384\n"
"s_waitcnt lgkmcnt(8)\n"
"v_mfma_f32_16x16x32_bf16 v[52:55], v[176:179], v[180:183], v[52:55]\n"
"ds_read_b128 v[140:143], v77 offset:36864\n"
"s_add_u32 m0, s99, 90112\n"
"s_add_u32 s46, s96, s97\n"
"s_mov_b64 exec, 0xffff\n"
"buffer_load_dwordx4 v19, s[68:71], s46 offen lds\n"
"s_mov_b64 exec, -1\n"
"s_waitcnt lgkmcnt(8)\n"
"v_mfma_f32_16x16x32_bf16 v[60:63], v[176:179], v[184:187], v[60:63]\n"
"ds_read_b128 v[136:139], v75 offset:18432\n"
"v_mfma_f32_16x16x32_bf16 v[56:59], v[172:175], v[184:187], v[56:59]\n"
"ds_read_b128 v[144:147], v77 offset:38912\n"
"s_waitcnt lgkmcnt(9)\n"
"v_mfma_f32_16x16x32_bf16 v[64:67], v[172:175], v[188:191], v[64:67]\n"
"ds_read_b128 v[148:151], v81 offset:36864\n"
"v_mfma_f32_16x16x32_bf16 v[68:71], v[176:179], v[188:191], v[68:71]\n"
"s_waitcnt vmcnt(3)\n"
"s_barrier\n"
"s_waitcnt lgkmcnt(8)\n"
"v_mfma_f32_16x16x32_bf16 v[48:51], v[112:115], v[120:123], v[48:51]\n"
"ds_read_b128 v[152:155], v74 offset:24576\n"
"s_waitcnt lgkmcnt(8)\n"
"v_mfma_f32_16x16x32_bf16 v[52:55], v[116:119], v[120:123], v[52:55]\n"
"ds_read_b128 v[160:163], v76 offset:55296\n"
"v_readfirstlane_b32 s36, v16\n"
"v_readfirstlane_b32 s37, v17\n"
"s_nop 1\n"
"s_mul_i32 s36, s36, 0x85ebca6b\n"
"s_add_u32 s36, s36, 0x7f4a7c15\n"
"s_add_u32 s37, s37, 0x9e3779b9\n"
"s_and_b32 s36, s36, 0xfffffff0\n"
"s_or_b32 s38, s36, 15\n"
"s_mov_b32 s39, s37\n"
"s_mov_b32 s41, s37\n"
"s_waitcnt lgkmcnt(8)\n"
"v_mfma_f32_16x16x32_bf16 v[60:63], v[116:119], v[124:127], v[60:63]\n"
"ds_read_b128 v[156:159], v74 offset:26624\n"
"v_mfma_f32_16x16x32_bf16 v[56:59], v[112:115], v[124:127], v[56:59]\n"
"ds_read_b128 v[164:167], v76 offset:57344\n"
"v_mov_b32 v12, 0\n"
"v_mov_b32 v13, 0\n"
"v_lshrrev_b32 v3, 4, v0\n"
"v_mul_u32_u24 v3, 262016, v3\n"
"v_lshl_add_u32 v3, v0, 3, v3\n"
"s_add_u32 s45, s79, 524288\n"
"buffer_store_dwordx2 v[12:13], v3, s[48:51], s45 offen sc1\n"
"s_mov_b64 exec, 0xffffffff\n"
"s_mov_b32 exec_hi, 0\n"
"buffer_store_dwordx2 v[12:13], v3, s[48:51], s79 offen sc1\n"
"s_mov_b64 exec, -1\n"
"s_waitcnt lgkmcnt(9)\n"
"v_mfma_f32_16x16x32_bf16 v[64:67], v[112:115], v[128:131], v[64:67]\n"
"ds_read_b128 v[168:171], v80 offset:55296\n"
"v_mfma_f32_16x16x32_bf16 v[68:71], v[116:119], v[128:131], v[68:71]\n"
"s_waitcnt lgkmcnt(8)\n"
"v_mfma_f32_16x16x32_bf16 v[48:51], v[132:135], v[140:143], v[48:51]\n"
"ds_read_b128 v[172:175], v75 offset:24576\n"
"s_waitcnt lgkmcnt(8)\n"
"v_mfma_f32_16x16x32_bf16 v[52:55], v[136:139], v[140:143], v[52:55]\n"
"ds_read_b128 v[180:183], v77 offset:55296\n"
"s_waitcnt lgkmcnt(8)\n"
"v_mfma_f32_16x16x32_bf16 v[60:63], v[136:139], v[144:147], v[60:63]\n"
"ds_read_b128 v[176:179], v75 offset:26624\n"
"v_mfma_f32_16x16x32_bf16 v[56:59], v[132:135], v[144:147], v[56:59]\n"
"ds_read_b128 v[184:187], v77 offset:57344\n"
"s_waitcnt lgkmcnt(9)\n"
"v_mfma_f32_16x16x32_bf16 v[64:67], v[132:135], v[148:151], v[64:67]\n"
"ds_read_b128 v[188:191], v81 offset:55296\n"
"v_mfma_f32_16x16x32_bf16 v[68:71], v[136:139], v[148:151], v[68:71]\n"
"s_waitcnt vmcnt(2)\n"
"s_barrier\n"
"s_waitcnt lgkmcnt(8)\n"
"v_mfma_f32_16x16x32_bf16 v[48:51], v[152:155], v[160:163], v[48:51]\n"
"ds_read_b128 v[112:115], v74 offset:32768\n"
"s_waitcnt lgkmcnt(8)\n"
"v_mfma_f32_16x16x32_bf16 v[52:55], v[156:159], v[160:163], v[52:55]\n"
"ds_read_b128 v[120:123], v76\n"
"s_waitcnt lgkmcnt(8)\n"
"v_mfma_f32_16x16x32_bf16 v[60:63], v[156:159], v[164:167], v[60:63]\n"
"ds_read_b128 v[116:119], v74 offset:34816\n"
"s_branch .Lsk2_0\n"
".Lst2_0:\n"
"s_branch .Lbk2\n"
".Lsk2_0:\n"
"v_mfma_f32_16x16x32_bf16 v[56:59], v[152:155], v[164:167], v[56:59]\n"
"ds_read_b128 v[124:127], v76 offset:2048\n"
"s_waitcnt lgkmcnt(9)\n"
"v_mfma_f32_16x16x32_bf16 v[64:67], v[152:155], v[168:171], v[64:67]\n"
"ds_read_b128 v[128:131], v80\n"
"v_mfma_f32_16x16x32_bf16 v[68:71], v[156:159], v[168:171], v[68:71]\n"
"s_waitcnt lgkmcnt(8)\n"
"v_mfma_f32_16x16x32_bf16 v[48:51], v[172:175], v[180:183], v[48:51]\n"
"ds_read_b128 v[132:135], v75 offset:32768\n"
"s_waitcnt lgkmcnt(8)\n"
"v_mfma_f32_16x16x32_bf16 v[52:55], v[176:179], v[180:183], v[52:55]\n"
"ds_read_b128 v[140:143], v77\n"
"s_waitcnt lgkmcnt(8)\n"
"v_mfma_f32_16x16x32_bf16 v[60:63], v[176:179], v[184:187], v[60:63]\n"
"ds_read_b128 v[136:139], v75 offset:34816\n"
"v_mfma_f32_16x16x32_bf16 v[56:59], v[172:175], v[184:187], v[56:59]\n"
"ds_read_b128 v[144:147], v77 offset:2048\n"
"s_waitcnt lgkmcnt(9)\n"
"v_mfma_f32_16x16x32_bf16 v[64:67], v[172:175], v[188:191], v[64:67]\n"
"ds_read_b128 v[148:151], v81\n"
"v_mfma_f32_16x16x32_bf16 v[68:71], v[176:179], v[188:191], v[68:71]\n"
"s_waitcnt lgkmcnt(8)\n"
"v_mfma_f32_16x16x32_bf16 v[48:51], v[112:115], v[120:123], v[48:51]\n"
"s_waitcnt lgkmcnt(7)\n"
"v_mfma_f32_16x16x32_bf16 v[52:55], v[116:119], v[120:123], v[52:55]\n"
"s_waitcnt lgkmcnt(6)\n"
"v_mfma_f32_16x16x32_bf16 v[60:63], v[116:119], v[124:127], v[60:63]\n"
"v_mfma_f32_16x16x32_bf16 v[56:59], v[112:115], v[124:127], v[56:59]\n"
"s_waitcnt lgkmcnt(5)\n"
"v_mfma_f32_16x16x32_bf16 v[64:67], v[112:115], v[128:131], v[64:67]\n"
"v_mfma_f32_16x16x32_bf16 v[68:71], v[116:119], v[128:131], v[68:71]\n"
"s_waitcnt lgkmcnt(3)\n"
"v_mfma_f32_16x16x32_bf16 v[48:51], v[132:135], v[140:143], v[48:51]\n"
"s_waitcnt lgkmcnt(2)\n"
"v_mfma_f32_16x16x32_bf16 v[52:55], v[136:139], v[140:143], v[52:55]\n"
"s_waitcnt lgkmcnt(1)\n"
"v_mfma_f32_16x16x32_bf16 v[60:63], v[136:139], v[144:147], v[60:63]\n"
"v_mfma_f32_16x16x32_bf16 v[56:59], v[132:135], v[144:147], v[56:59]\n"
"s_waitcnt lgkmcnt(0)\n"
"v_mfma_f32_16x16x32_bf16 v[64:67], v[132:135], v[148:151], v[64:67]\n"
"v_mfma_f32_16x16x32_bf16 v[68:71], v[136:139], v[148:151], v[68:71]\n"
"s_setprio 0\n"
"s_branch .Lg1j\n"
".Lsb:\n"
"s_branch .Lbb\n"
".Lg1j:\n"
"s_nop 7\n"
"v_cvt_pk_bf16_f32 v192, v48, v49\n"
"v_cvt_pk_bf16_f32 v193, v50, v51\n"
"v_cvt_pk_bf16_f32 v194, v52, v53\n"
"v_cvt_pk_bf16_f32 v195, v54, v55\n"
"buffer_store_dwordx4 v[192:195], v1, s[48:51], s81 offen sc1\n"
"v_cvt_pk_bf16_f32 v196, v56, v57\n"
"v_cvt_pk_bf16_f32 v197, v58, v59\n"
"v_cvt_pk_bf16_f32 v198, v60, v61\n"
"v_cvt_pk_bf16_f32 v199, v62, v63\n"
"buffer_store_dwordx4 v[196:199], v1, s[48:51], s81 offen offset:1024 sc1\n"
"v_cvt_pk_bf16_f32 v200, v64, v65\n"
"v_cvt_pk_bf16_f32 v201, v66, v67\n"
"v_cvt_pk_bf16_f32 v202, v68, v69\n"
"v_cvt_pk_bf16_f32 v203, v70, v71\n"
"buffer_store_dwordx4 v[200:203], v20, s[48:51], s82 offen sc1\n"
"s_and_b32 s45, s30, 1\n"
"s_lshr_b32 s47, s30, 1\n"
"s_mul_i32 s92, s32, 8192\n"
"s_mul_i32 s42, s45, 4096\n"
"s_add_u32 s92, s92, s42\n"
"s_mul_i32 s42, s25, 2048\n"
"s_add_u32 s92, s92, s42\n"
"s_mul_i32 s42, s47, 1024\n"
"s_add_u32 s92, s92, s42\n"
"s_add_u32 s92, s92, 266240\n"
"s_mul_i32 s88, s47, 2359296\n"
"s_mul_i32 s42, s32, 589824\n"
"s_add_u32 s88, s88, s42\n"
"s_mul_i32 s42, s45, 294912\n"
"s_add_u32 s88, s88, s42\n"
"s_mul_i32 s42, s25, 147456\n"
"s_add_u32 s88, s88, s42\n"
"s_add_u32 s88, s88, 44044288\n"
"s_lshr_b32 s45, s26, 1\n"
"s_and_b32 s46, s26, 1\n"
"s_mul_i32 s87, s45, 9216\n"
"s_mul_i32 s42, s46, 3072\n"
"s_add_u32 s87, s87, s42\n"
"s_add_u32 s87, s87, s88\n"
"s_mul_i32 s47, s46, 9216\n"
"s_add_u32 s47, s47, 6144\n"
"s_add_u32 s88, s88, s47\n"
"s_add_u32 s91, s89, s45\n"
"s_add_u32 s93, s80, 34607104\n"
"v_and_b32 v8, 15, v0\n"
"v_lshrrev_b32 v3, 1, v8\n"
"v_lshrrev_b32 v9, 4, v0\n"
"v_xor_b32 v3, v3, v9\n"
"v_lshlrev_b32 v3, 4, v3\n"
"v_lshl_add_u32 v3, v8, 7, v3\n"
"v_add_u32 v6, s90, v3\n"
"v_xor_b32 v7, 64, v6\n"
"v_add_u32 v22, s78, v1\n"
"v_bfe_u32 v3, v0, 3, 1\n"
"v_mul_u32_u24 v21, 36864, v3\n"
"v_bfe_u32 v3, v0, 4, 1\n"
"v_lshl_add_u32 v21, v3, 7, v21\n"
"v_lshrrev_b32 v3, 5, v0\n"
"v_lshl_add_u32 v21, v3, 8, v21\n"
"v_and_b32 v3, 7, v0\n"
"v_lshl_add_u32 v21, v3, 4, v21\n"
"v_and_b32 v19, 7, v0\n"
"v_lshlrev_b32 v19, 15, v19\n"
"v_add_u32 v19, s92, v19\n"
"s_or_b32 s45, s36, 1\n"
"v_mov_b32 v16, s45\n"
"v_mov_b32 v17, s37\n"
"v_lshrrev_b32 v18, 4, v0\n"
"v_add_u32 v18, s24, v18\n"
"v_lshlrev_b32 v18, 28, v18\n"
"v_lshl_add_u32 v18, v0, 3, v18\n"
"s_add_u32 s45, s79, 262144\n"
"s_waitcnt vmcnt(0)\n"
"s_barrier\n"
"buffer_store_dwordx2 v[16:17], v18, s[48:51], s45 offen sc1\n"
"s_add_u32 s86, s89, 0\n"
"s_and_b32 s86, s86, 7\n"
"s_lshl_b32 s86, s86, 7\n"
"s_add_u32 m0, s90, 0\n"
"s_add_u32 s46, s84, s86\n"
"buffer_load_dwordx4 v4, s[72:75], s46 offen lds\n"
"s_add_u32 m0, s90, 1024\n"
"s_add_u32 s46, s84, s86\n"
"buffer_load_dwordx4 v5, s[72:75], s46 offen lds\n"
"s_add_u32 m0, s90, 2048\n"
"s_add_u32 s46, s85, s86\n"
"buffer_load_dwordx4 v4, s[72:75], s46 offen lds\n"
"s_add_u32 m0, s90, 3072\n"
"s_add_u32 s46, s85, s86\n"
"buffer_load_dwordx4 v5, s[72:75], s46 offen lds\n"
"s_cmp_eq_u32 s24, 6\n"
"s_cbranch_scc1 .Lst6_0\n"
".Lbk6:\n"
"s_cmp_eq_u32 s24, 7\n"
"s_cbranch_scc1 .Lst7_0\n"
".Lbk7:\n"
"s_cmp_eq_u32 s24, 0\n"
"s_cbranch_scc1 .Lst8_0\n"
".Lbk8:\n"
"s_cmp_eq_u32 s24, 1\n"
"s_cbranch_scc1 .Lst9_0\n"
".Lbk9:\n"
"s_cmp_eq_u32 s24, 2\n"
"s_cbranch_scc1 .Lst10_0\n"
".Lbk10:\n"
"s_cmp_eq_u32 s24, 3\n"
"s_cbranch_scc1 .Lst11_0\n"
".Lbk11:\n"
"s_mul_i32 s4, s26, 73728\n"
"s_mul_i32 s42, s34, 128\n"
"s_add_u32 s4, s4, s42\n"
"s_mul_i32 s5, s26, 884736\n"
"s_mul_i32 s42, s34, 32\n"
"s_add_u32 s5, s5, s42\n"
"s_add_u32 s5, s5, 8192\n"
"s_min_u32 s46, s26, 2\n"
"s_mul_i32 s6, s46, 589824\n"
"s_mul_i32 s42, s34, 32\n"
"s_add_u32 s6, s6, s42\n"
"s_add_u32 s6, s6, 3547136\n"
"s_mul_i32 s7, s46, 2048\n"
"s_add_u32 s7, s7, s78\n"
"s_mul_i32 s8, s33, 294912\n"
"s_mul_i32 s42, s24, 36864\n"
"s_add_u32 s8, s8, s42\n"
"s_add_u32 s8, s8, 1576960\n"
"s_bfe_u32 s45, s27, 65536\n"
"s_mov_b32 s45, 0\n"
"s_mul_i32 s46, s45, 2654208\n"
"s_sub_u32 s97, s4, s46\n"
"s_add_u32 s4, s4, s46\n"
"s_sub_u32 s6, s6, s46\n"
"s_sub_u32 s47, 0, s46\n"
"s_cmp_lt_u32 s26, 3\n"
"s_cselect_b32 s46, s46, s47\n"
"s_add_u32 s5, s5, s46\n"
"s_mul_i32 s46, s45, 9216\n"
"s_sub_u32 s96, s8, s46\n"
"s_add_u32 s8, s8, s46\n"
"s_mul_i32 s46, s33, 2359296\n"
"s_mul_i32 s42, s24, 294912\n"
"s_add_u32 s46, s46, s42\n"
"s_add_u32 s46, s46, 6295552\n"
"s_mul_i32 s9, s34, 2048\n"
"s_add_u32 s9, s9, s46\n"
"s_and_b32 s45, s34, 1\n"
"s_lshr_b32 s3, s34, 1\n"
"s_mul_i32 s10, s34, 512\n"
"s_add_u32 s10, s10, s46\n"
"s_add_u32 s10, s10, 131072\n"
"s_mul_i32 s46, s31, 1179648\n"
"s_mul_i32 s42, s26, 294912\n"
"s_add_u32 s46, s46, s42\n"
"s_mul_i32 s42, s25, 147456\n"
"s_add_u32 s46, s46, s42\n"
"s_add_u32 s46, s46, 6295552\n"
"s_mul_i32 s23, s30, 4096\n"
"s_mul_i32 s42, s25, 2048\n"
"s_add_u32 s23, s23, s42\n"
"s_add_u32 s23, s23, s46\n"
"s_add_u32 s23, s23, 131072\n"
"s_mul_i32 s81, s25, -294912\n"
"s_add_u32 s81, s81, 147456\n"
"s_mul_i32 s47, s26, 9216\n"
"s_mul_i32 s42, s32, 256\n"
"s_add_u32 s47, s47, s42\n"
"s_mul_i32 s15, s30, 147456\n"
"s_mul_i32 s42, s25, 73728\n"
"s_add_u32 s15, s15, s42\n"
"s_add_u32 s15, s15, s47\n"
"s_add_u32 s15, s15, 4718592\n"
"s_add_u32 s45, s32, 0\n"
"s_and_b32 s45, s45, 3\n"
"s_mul_i32 s3, s30, 16\n"
"s_mul_i32 s42, s25, 8\n"
"s_add_u32 s3, s3, s42\n"
"s_mul_i32 s42, s45, 2\n"
"s_add_u32 s3, s3, s42\n"
"s_mul_i32 s11, s3, 36864\n"
"s_add_u32 s11, s11, s47\n"
"s_mul_i32 s19, s3, 1024\n"
"s_add_u32 s19, s19, s46\n"
"s_add_u32 s45, s32, 1\n"
"s_and_b32 s45, s45, 3\n"
"s_mul_i32 s3, s30, 16\n"
"s_mul_i32 s42, s25, 8\n"
"s_add_u32 s3, s3, s42\n"
"s_mul_i32 s42, s45, 2\n"
"s_add_u32 s3, s3, s42\n"
"s_mul_i32 s12, s3, 36864\n"
"s_add_u32 s12, s12, s47\n"
"s_mul_i32 s20, s3, 1024\n"
"s_add_u32 s20, s20, s46\n"
"s_add_u32 s45, s32, 2\n"
"s_and_b32 s45, s45, 3\n"
"s_mul_i32 s3, s30, 16\n"
"s_mul_i32 s42, s25, 8\n"
"s_add_u32 s3, s3, s42\n"
"s_mul_i32 s42, s45, 2\n"
"s_add_u32 s3, s3, s42\n"
"s_mul_i32 s13, s3, 36864\n"
"s_add_u32 s13, s13, s47\n"
"s_mul_i32 s21, s3, 1024\n"
"s_add_u32 s21, s21, s46\n"
"s_add_u32 s45, s32, 3\n"
"s_and_b32 s45, s45, 3\n"
"s_mul_i32 s3, s30, 16\n"
"s_mul_i32 s42, s25, 8\n"
"s_add_u32 s3, s3, s42\n"
"s_mul_i32 s42, s45, 2\n"
"s_add_u32 s3, s3, s42\n"
"s_mul_i32 s14, s3, 36864\n"
"s_add_u32 s14, s14, s47\n"
"s_mul_i32 s22, s3, 1024\n"
"s_add_u32 s22, s22, s46\n"
"s_add_u32 s45, s32, s26\n"
"s_and_b32 s45, s45, 3\n"
"s_mul_i32 s3, s30, 16\n"
"s_mul_i32 s42, s25, 8\n"
"s_add_u32 s3, s3, s42\n"
"s_mul_i32 s42, s45, 2\n"
"s_add_u32 s3, s3, s42\n"
"s_mul_i32 s16, s3, 36864\n"
"s_mul_i32 s42, s32, 32\n"
"s_add_u32 s16, s16, s42\n"
"s_add_u32 s16, s16, 1024\n"
"s_and_b32 s45, s26, 1\n"
"s_mul_i32 s17, s30, 147456\n"
"s_mul_i32 s42, s25, 73728\n"
"s_add_u32 s17, s17, s42\n"
"s_mul_i32 s42, s45, 36864\n"
"s_add_u32 s17, s17, s42\n"
"s_mul_i32 s42, s32, 32\n"
"s_add_u32 s17, s17, s42\n"
"s_add_u32 s17, s17, 4719616\n"
"s_mul_i32 s18, s45, 1024\n"
"s_add_u32 s18, s18, s78\n"
"s_mul_i32 s82, s30, 1024\n"
"s_and_b32 s45, s27, 24\n"
"s_and_b32 s46, s31, 1\n"
"s_mul_i32 s83, s45, 8192\n"
"s_mul_i32 s42, s25, 32768\n"
"s_add_u32 s83, s83, s42\n"
"s_mul_i32 s42, s28, 1024\n"
"s_add_u32 s83, s83, s42\n"
"s_mul_i32 s42, s46, 512\n"
"s_add_u32 s83, s83, s42\n"
"s_mul_i32 s42, s26, 128\n"
"s_add_u32 s83, s83, s42\n"
"s_add_u32 s83, s83, 1052672\n"
"v_and_b32 v3, 7, v0\n"
"v_mul_u32_u24 v8, 1152, v3\n"
"v_lshrrev_b32 v3, 3, v0\n"
"v_lshl_add_u32 v9, v3, 4, v8\n"
"v_lshl_add_u32 v8, v3, 1, v8\n"
"v_mov_b32 v252, 0\n"
"s_add_u32 s86, s89, 1\n"
"s_and_b32 s86, s86, 7\n"
"s_lshl_b32 s86, s86, 7\n"
"s_add_u32 m0, s90, 32768\n"
"s_add_u32 s46, s84, s86\n"
"buffer_load_dwordx4 v4, s[72:75], s46 offen lds\n"
"s_add_u32 m0, s90, 33792\n"
"s_add_u32 s46, s84, s86\n"
"buffer_load_dwordx4 v5, s[72:75], s46 offen lds\n"
"s_add_u32 m0, s90, 34816\n"
"s_add_u32 s46, s85, s86\n"
"buffer_load_dwordx4 v4, s[72:75], s46 offen lds\n"
"s_add_u32 m0, s90, 35840\n"
"s_add_u32 s46, s85, s86\n"
"buffer_load_dwordx4 v5, s[72:75], s46 offen lds\n"
"s_mov_b64 exec, 0xff\n"
"buffer_load_dwordx2 v[10:11], v19, s[48:51], 0 offen sc1\n"
"s_mov_b64 exec, -1\n"
"s_or_b32 s40, s36, 1\n"
"s_waitcnt vmcnt(0)\n"
"s_mov_b32 s44, 0x40000\n"
"s_mov_b64 exec, 0xff\n"
".Lp1:\n"
"v_cmp_le_u64 vcc, s[40:41], v[10:11]\n"
"v_cmp_ge_u64 s[42:43], s[38:39], v[10:11]\n"
"s_nop 1\n"
"s_and_b64 s[42:43], s[42:43], vcc\n"
"s_cmp_eq_u64 s[42:43], exec\n"
"s_cbranch_scc1 .Ld1\n"
"s_sub_u32 s44, s44, 1\n"
"s_cbranch_scc1 .Ld1\n"
"s_sleep 1\n"
"buffer_load_dwordx2 v[10:11], v19, s[48:51], 0 offen sc1\n"
"s_waitcnt vmcnt(0)\n"
"s_branch .Lp1\n"
".Ld1:\n"
"s_mov_b64 exec, -1\n"
"s_add_u32 m0, s76, 73728\n"
"s_add_u32 s92, s89, 0\n"
"s_and_b32 s92, s92, 7\n"
"s_mul_i32 s92, s92, 18432\n"
"s_add_u32 s92, s92, s87\n"
"buffer_load_dwordx4 v1, s[48:51], s92 offen sc1 lds\n"
"s_add_u32 m0, s76, 73728\n"
"s_nop 0\n"
"buffer_load_dwordx4 v1, s[48:51], s92 offen offset:1024 sc1 lds\n"
"s_add_u32 m0, s76, 73728\n"
"s_nop 0\n"
"buffer_load_dwordx4 v1, s[48:51], s92 offen offset:2048 sc1 lds\n"
"s_add_u32 m0, s76, 98304\n"
"s_add_u32 s92, s89, 1\n"
"s_and_b32 s92, s92, 7\n"
"s_mul_i32 s92, s92, 18432\n"
"s_add_u32 s92, s92, s87\n"
"buffer_load_dwordx4 v1, s[48:51], s92 offen sc1 lds\n"
"s_add_u32 m0, s76, 98304\n"
"s_nop 0\n"
"buffer_load_dwordx4 v1, s[48:51], s92 offen offset:1024 sc1 lds\n"
"s_add_u32 m0, s76, 98304\n"
"s_nop 0\n"
"buffer_load_dwordx4 v1, s[48:51], s92 offen offset:2048 sc1 lds\n"
"ds_read_b128 v[112:115], v6\n"
"ds_read_b128 v[116:119], v6 offset:2048\n"
"ds_read_b128 v[120:123], v7\n"
"ds_read_b128 v[124:127], v7 offset:2048\n"
"s_waitcnt vmcnt(3)\n"
"s_barrier\n"
"v_add_u32 v19, 73728, v22\n"
"ds_read_b128 v[24:27], v19\n"
"ds_read_b128 v[28:31], v19 offset:1024\n"
"ds_read_b128 v[32:35], v19 offset:2048\n"
"ds_read_b128 v[36:39], v19 offset:3072\n"
"s_waitcnt lgkmcnt(3)\n"
"v_mfma_f32_16x16x32_bf16 v[48:51], v[24:27], v[112:115], 0\n"
"ds_read_b128 v[40:43], v19 offset:4096\n"
"s_setprio 3\n"
"v_mfma_f32_16x16x32_bf16 v[52:55], v[24:27], v[116:119], 0\n"
"s_add_u32 s86, s89, 2\n"
"s_and_b32 s86, s86, 7\n"
"s_lshl_b32 s86, s86, 7\n"
"s_add_u32 m0, s90, 0\n"
"s_add_u32 s46, s84, s86\n"
"buffer_load_dwordx4 v4, s[72:75], s46 offen lds\n"
"s_waitcnt lgkmcnt(3)\n"
"v_mfma_f32_16x16x32_bf16 v[60:63], v[28:31], v[116:119], 0\n"
"ds_read_b128 v[44:47], v19 offset:5120\n"
"v_mfma_f32_16x16x32_bf16 v[56:59], v[28:31], v[112:115], 0\n"
"s_setprio 2\n"
"s_add_u32 m0, s90, 1024\n"
"s_add_u32 s46, s84, s86\n"
"buffer_load_dwordx4 v5, s[72:75], s46 offen lds\n"
"s_waitcnt lgkmcnt(3)\n"
"v_mfma_f32_16x16x32_bf16 v[64:67], v[32:35], v[112:115], 0\n"
"ds_read_b128 v[24:27], v19 offset:6144\n"
"v_mfma_f32_16x16x32_bf16 v[68:71], v[32:35], v[116:119], 0\n"
"s_add_u32 m0, s90, 2048\n"
"s_add_u32 s46, s85, s86\n"
"buffer_load_dwordx4 v4, s[72:75], s46 offen lds\n"
"s_waitcnt lgkmcnt(3)\n"
"v_mfma_f32_16x16x32_bf16 v[76:79], v[36:39], v[116:119], 0\n"
"ds_read_b128 v[28:31], v19 offset:7168\n"
"s_setprio 1\n"
"v_mfma_f32_16x16x32_bf16 v[72:75], v[36:39], v[112:115], 0\n"
"s_add_u32 m0, s90, 3072\n"
"s_add_u32 s46, s85, s86\n"
"buffer_load_dwordx4 v5, s[72:75], s46 offen lds\n"
"s_waitcnt lgkmcnt(3)\n"
"v_mfma_f32_16x16x32_bf16 v[80:83], v[40:43], v[112:115], 0\n"
"ds_read_b128 v[32:35], v19 offset:8192\n"
"v_mfma_f32_16x16x32_bf16 v[84:87], v[40:43], v[116:119], 0\n"
"s_setprio 0\n"
"ds_read_b128 v[128:131], v6 offset:32768\n"
"s_waitcnt lgkmcnt(4)\n"
"v_mfma_f32_16x16x32_bf16 v[92:95], v[44:47], v[116:119], 0\n"
"ds_read_b128 v[36:39], v19 offset:9216\n"
"v_mfma_f32_16x16x32_bf16 v[88:91], v[44:47], v[112:115], 0\n"
"s_waitcnt vmcnt(4)\n"
"s_barrier\n"
"s_waitcnt lgkmcnt(4)\n"
"v_mfma_f32_16x16x32_bf16 v[48:51], v[24:27], v[120:123], v[48:51]\n"
"ds_read_b128 v[40:43], v19 offset:10240\n"
"s_setprio 3\n"
"v_mfma_f32_16x16x32_bf16 v[52:55], v[24:27], v[124:127], v[52:55]\n"
"ds_read_b128 v[132:135], v6 offset:34816\n"
"s_waitcnt lgkmcnt(5)\n"
"v_mfma_f32_16x16x32_bf16 v[60:63], v[28:31], v[124:127], v[60:63]\n"
"ds_read_b128 v[44:47], v19 offset:11264\n"
"v_mfma_f32_16x16x32_bf16 v[56:59], v[28:31], v[120:123], v[56:59]\n"
"s_add_u32 m0, s76, 122880\n"
"s_add_u32 s92, s89, 2\n"
"s_and_b32 s92, s92, 7\n"
"s_mul_i32 s92, s92, 18432\n"
"s_add_u32 s92, s92, s87\n"
"buffer_load_dwordx4 v1, s[48:51], s92 offen sc1 lds\n"
"s_waitcnt lgkmcnt(5)\n"
"v_mfma_f32_16x16x32_bf16 v[64:67], v[32:35], v[120:123], v[64:67]\n"
"v_add_u32 v19, 98304, v22\n"
"ds_read_b128 v[24:27], v19\n"
"v_mfma_f32_16x16x32_bf16 v[68:71], v[32:35], v[124:127], v[68:71]\n"
"ds_read_b128 v[136:139], v7 offset:32768\n"
"s_waitcnt lgkmcnt(5)\n"
"v_mfma_f32_16x16x32_bf16 v[76:79], v[36:39], v[124:127], v[76:79]\n"
"ds_read_b128 v[28:31], v19 offset:1024\n"
"s_setprio 2\n"
"v_mfma_f32_16x16x32_bf16 v[72:75], v[36:39], v[120:123], v[72:75]\n"
"s_add_u32 m0, s76, 122880\n"
"s_nop 0\n"
"buffer_load_dwordx4 v1, s[48:51], s92 offen offset:1024 sc1 lds\n"
"s_waitcnt lgkmcnt(5)\n"
"v_mfma_f32_16x16x32_bf16 v[80:83], v[40:43], v[120:123], v[80:83]\n"
"ds_read_b128 v[32:35], v19 offset:2048\n"
"v_mfma_f32_16x16x32_bf16 v[84:87], v[40:43], v[124:127], v[84:87]\n"
"ds_read_b128 v[140:143], v7 offset:34816\n"
"s_waitcnt lgkmcnt(5)\n"
"v_mfma_f32_16x16x32_bf16 v[92:95], v[44:47], v[124:127], v[92:95]\n"
"ds_read_b128 v[36:39], v19 offset:3072\n"
"v_mfma_f32_16x16x32_bf16 v[88:91], v[44:47], v[120:123], v[88:91]\n"
"s_add_u32 m0, s76, 122880\n"
"s_nop 0\n"
"buffer_load_dwordx4 v1, s[48:51], s92 offen offset:2048 sc1 lds\n"
"s_waitcnt lgkmcnt(5)\n"
"v_mfma_f32_16x16x32_bf16 v[48:51], v[24:27], v[128:131], v[48:51]\n"
"ds_read_b128 v[40:43], v19 offset:4096\n"
"s_setprio 1\n"
"v_mfma_f32_16x16x32_bf16 v[52:55], v[24:27], v[132:135], v[52:55]\n"
"s_waitcnt lgkmcnt(2)\n"
"s_add_u32 s86, s89, 3\n"
"s_and_b32 s86, s86, 7\n"
"s_lshl_b32 s86, s86, 7\n"
"s_add_u32 m0, s90, 32768\n"
"s_add_u32 s46, s84, s86\n"
"buffer_load_dwordx4 v4, s[72:75], s46 offen lds\n"
"v_mfma_f32_16x16x32_bf16 v[60:63], v[28:31], v[132:135], v[60:63]\n"
"ds_read_b128 v[44:47], v19 offset:5120\n"
"v_mfma_f32_16x16x32_bf16 v[56:59], v[28:31], v[128:131], v[56:59]\n"
"s_add_u32 m0, s90, 33792\n"
"s_add_u32 s46, s84, s86\n"
"buffer_load_dwordx4 v5, s[72:75], s46 offen lds\n"
"v_mfma_f32_16x16x32_bf16 v[64:67], v[32:35], v[128:131], v[64:67]\n"
"ds_read_b128 v[24:27], v19 offset:6144\n"
"v_mfma_f32_16x16x32_bf16 v[68:71], v[32:35], v[132:135], v[68:71]\n"
"s_add_u32 m0, s90, 34816\n"
"s_add_u32 s46, s85, s86\n"
"buffer_load_dwordx4 v4, s[72:75], s46 offen lds\n"
"s_waitcnt lgkmcnt(3)\n"
"v_mfma_f32_16x16x32_bf16 v[76:79], v[36:39], v[132:135], v[76:79]\n"
"ds_read_b128 v[28:31], v19 offset:7168\n"
"s_setprio 0\n"
"v_mfma_f32_16x16x32_bf16 v[72:75], v[36:39], v[128:131], v[72:75]\n"
"s_add_u32 m0, s90, 35840\n"
"s_add_u32 s46, s85, s86\n"
"buffer_load_dwordx4 v5, s[72:75], s46 offen lds\n"
"s_waitcnt lgkmcnt(3)\n"
"v_mfma_f32_16x16x32_bf16 v[80:83], v[40:43], v[128:131], v[80:83]\n"
"ds_read_b128 v[32:35], v19 offset:8192\n"
"v_mfma_f32_16x16x32_bf16 v[84:87], v[40:43], v[132:135], v[84:87]\n"
"s_waitcnt vmcnt(7)\n"
"ds_read_b128 v[144:147], v6\n"
"s_waitcnt lgkmcnt(4)\n"
"v_mfma_f32_16x16x32_bf16 v[92:95], v[44:47], v[132:135], v[92:95]\n"
"ds_read_b128 v[36:39], v19 offset:9216\n"
"v_mfma_f32_16x16x32_bf16 v[88:91], v[44:47], v[128:131], v[88:91]\n"
"s_waitcnt vmcnt(4)\n"
"s_barrier\n"
"s_waitcnt lgkmcnt(4)\n"
"v_mfma_f32_16x16x32_bf16 v[48:51], v[24:27], v[136:139], v[48:51]\n"
"ds_read_b128 v[40:43], v19 offset:10240\n"
"s_setprio 3\n"
"v_mfma_f32_16x16x32_bf16 v[52:55], v[24:27], v[140:143], v[52:55]\n"
"ds_read_b128 v[148:151], v6 offset:2048\n"
"s_waitcnt lgkmcnt(5)\n"
"v_mfma_f32_16x16x32_bf16 v[60:63], v[28:31], v[140:143], v[60:63]\n"
"ds_read_b128 v[44:47], v19 offset:11264\n"
"v_mfma_f32_16x16x32_bf16 v[56:59], v[28:31], v[136:139], v[56:59]\n"
"s_add_u32 m0, s76, 73728\n"
"s_add_u32 s92, s89, 3\n"
"s_and_b32 s92, s92, 7\n"
"s_mul_i32 s92, s92, 18432\n"
"s_add_u32 s92, s92, s87\n"
"buffer_load_dwordx4 v1, s[48:51], s92 offen sc1 lds\n"
"s_waitcnt lgkmcnt(5)\n"
"v_mfma_f32_16x16x32_bf16 v[64:67], v[32:35], v[136:139], v[64:67]\n"
"v_add_u32 v19, 122880, v22\n"
"ds_read_b128 v[24:27], v19\n"
"v_mfma_f32_16x16x32_bf16 v[68:71], v[32:35], v[140:143], v[68:71]\n"
"ds_read_b128 v[152:155], v7\n"
"s_waitcnt lgkmcnt(5)\n"
"v_mfma_f32_16x16x32_bf16 v[76:79], v[36:39], v[140:143], v[76:79]\n"
"ds_read_b128 v[28:31], v19 offset:1024\n"
"s_setprio 2\n"
"v_mfma_f32_16x16x32_bf16 v[72:75], v[36:39], v[136:139], v[72:75]\n"
"s_add_u32 m0, s76, 73728\n"
"s_nop 0\n"
"buffer_load_dwordx4 v1, s[48:51], s92 offen offset:1024 sc1 lds\n"
"s_waitcnt lgkmcnt(5)\n"
"v_mfma_f32_16x16x32_bf16 v[80:83], v[40:43], v[136:139], v[80:83]\n"
"ds_read_b128 v[32:35], v19 offset:2048\n"
"v_mfma_f32_16x16x32_bf16 v[84:87], v[40:43], v[140:143], v[84:87]\n"
"ds_read_b128 v[156:159], v7 offset:2048\n"
"s_waitcnt lgkmcnt(5)\n"
"v_mfma_f32_16x16x32_bf16 v[92:95], v[44:47], v[140:143], v[92:95]\n"
"ds_read_b128 v[36:39], v19 offset:3072\n"
"v_mfma_f32_16x16x32_bf16 v[88:91], v[44:47], v[136:139], v[88:91]\n"
"s_add_u32 m0, s76, 73728\n"
"s_nop 0\n"
"buffer_load_dwordx4 v1, s[48:51], s92 offen offset:2048 sc1 lds\n"
"s_waitcnt lgkmcnt(5)\n"
"v_mfma_f32_16x16x32_bf16 v[48:51], v[24:27], v[144:147], v[48:51]\n"
"ds_read_b128 v[40:43], v19 offset:4096\n"
"s_setprio 1\n"
"v_mfma_f32_16x16x32_bf16 v[52:55], v[24:27], v[148:151], v[52:55]\n"
"s_waitcnt lgkmcnt(2)\n"
"s_add_u32 s86, s89, 4\n"
"s_and_b32 s86, s86, 7\n"
"s_lshl_b32 s86, s86, 7\n"
"s_add_u32 m0, s90, 0\n"
"s_add_u32 s46, s84, s86\n"
"buffer_load_dwordx4 v4, s[72:75], s46 offen lds\n"
"v_mfma_f32_16x16x32_bf16 v[60:63], v[28:31], v[148:151], v[60:63]\n"
"ds_read_b128 v[44:47], v19 offset:5120\n"
"v_mfma_f32_16x16x32_bf16 v[56:59], v[28:31], v[144:147], v[56:59]\n"
"s_add_u32 m0, s90, 1024\n"
"s_add_u32 s46, s84, s86\n"
"buffer_load_dwordx4 v5, s[72:75], s46 offen lds\n"
"v_mfma_f32_16x16x32_bf16 v[64:67], v[32:35], v[144:147], v[64:67]\n"
"ds_read_b128 v[24:27], v19 offset:6144\n"
"v_mfma_f32_16x16x32_bf16 v[68:71], v[32:35], v[148:151], v[68:71]\n"
"s_add_u32 m0, s90, 2048\n"
"s_add_u32 s46, s85, s86\n"
"buffer_load_dwordx4 v4, s[72:75], s46 offen lds\n"
"s_waitcnt lgkmcnt(3)\n"
"v_mfma_f32_16x16x32_bf16 v[76:79], v[36:39], v[148:151], v[76:79]\n"
"ds_read_b128 v[28:31], v19 offset:7168\n"
"s_setprio 0\n"
"v_mfma_f32_16x16x32_bf16 v[72:75], v[36:39], v[144:147], v[72:75]\n"
"s_add_u32 m0, s90, 3072\n"
"s_add_u32 s46, s85, s86\n"
"buffer_load_dwordx4 v5, s[72:75], s46 offen lds\n"
"s_waitcnt lgkmcnt(3)\n"
"v_mfma_f32_16x16x32_bf16 v[80:83], v[40:43], v[144:147], v[80:83]\n"
"ds_read_b128 v[32:35], v19 offset:8192\n"
"v_mfma_f32_16x16x32_bf16 v[84:87], v[40:43], v[148:151], v[84:87]\n"
"s_waitcnt vmcnt(7)\n"
"ds_read_b128 v[160:163], v6 offset:32768\n"
"s_waitcnt lgkmcnt(4)\n"
"v_mfma_f32_16x16x32_bf16 v[92:95], v[44:47], v[148:151], v[92:95]\n"
"ds_read_b128 v[36:39], v19 offset:9216\n"
"v_mfma_f32_16x16x32_bf16 v[88:91], v[44:47], v[144:147], v[88:91]\n"
"s_waitcnt vmcnt(4)\n"
"s_barrier\n"
"s_waitcnt lgkmcnt(4)\n"
"v_mfma_f32_16x16x32_bf16 v[48:51], v[24:27], v[152:155], v[48:51]\n"
"ds_read_b128 v[40:43], v19 offset:10240\n"
"s_setprio 3\n"
"v_mfma_f32_16x16x32_bf16 v[52:55], v[24:27], v[156:159], v[52:55]\n"
"ds_read_b128 v[164:167], v6 offset:34816\n"
"s_waitcnt lgkmcnt(5)\n"
"v_mfma_f32_16x16x32_bf16 v[60:63], v[28:31], v[156:159], v[60:63]\n"
"ds_read_b128 v[44:47], v19 offset:11264\n"
"v_mfma_f32_16x16x32_bf16 v[56:59], v[28:31], v[152:155], v[56:59]\n"
"s_add_u32 m0, s76, 98304\n"
"s_add_u32 s92, s89, 4\n"
"s_and_b32 s92, s92, 7\n"
"s_mul_i32 s92, s92, 18432\n"
"s_add_u32 s92, s92, s87\n"
"buffer_load_dwordx4 v1, s[48:51], s92 offen sc1 lds\n"
"s_waitcnt lgkmcnt(5)\n"
"v_mfma_f32_16x16x32_bf16 v[64:67], v[32:35], v[152:155], v[64:67]\n"
"v_add_u32 v19, 73728, v22\n"
"ds_read_b128 v[24:27], v19\n"
"v_mfma_f32_16x16x32_bf16 v[68:71], v[32:35], v[156:159], v[68:71]\n"
"ds_read_b128 v[168:171], v7 offset:32768\n"
"s_waitcnt lgkmcnt(5)\n"
"v_mfma_f32_16x16x32_bf16 v[76:79], v[36:39], v[156:159], v[76:79]\n"
"ds_read_b128 v[28:31], v19 offset:1024\n"
"s_setprio 2\n"
"v_mfma_f32_16x16x32_bf16 v[72:75], v[36:39], v[152:155], v[72:75]\n"
"s_add_u32 m0, s76, 98304\n"
"s_nop 0\n"
"buffer_load_dwordx4 v1, s[48:51], s92 offen offset:1024 sc1 lds\n"
"s_waitcnt lgkmcnt(5)\n"
"v_mfma_f32_16x16x32_bf16 v[80:83], v[40:43], v[152:155], v[80:83]\n"
"ds_read_b128 v[32:35], v19 offset:2048\n"
"v_mfma_f32_16x16x32_bf16 v[84:87], v[40:43], v[156:159], v[84:87]\n"
"ds_read_b128 v[172:175], v7 offset:34816\n"
"s_waitcnt lgkmcnt(5)\n"
"v_mfma_f32_16x16x32_bf16 v[92:95], v[44:47], v[156:159], v[92:95]\n"
"ds_read_b128 v[36:39], v19 offset:3072\n"
"v_mfma_f32_16x16x32_bf16 v[88:91], v[44:47], v[152:155], v[88:91]\n"
"s_add_u32 m0, s76, 98304\n"
"s_nop 0\n"
"buffer_load_dwordx4 v1, s[48:51], s92 offen offset:2048 sc1 lds\n"
"s_waitcnt lgkmcnt(5)\n"
"v_mfma_f32_16x16x32_bf16 v[48:51], v[24:27], v[160:163], v[48:51]\n"
"ds_read_b128 v[40:43], v19 offset:4096\n"
"s_setprio 1\n"
"v_mfma_f32_16x16x32_bf16 v[52:55], v[24:27], v[164:167], v[52:55]\n"
"s_waitcnt lgkmcnt(2)\n"
"s_add_u32 s86, s89, 5\n"
"s_and_b32 s86, s86, 7\n"
"s_lshl_b32 s86, s86, 7\n"
"s_add_u32 m0, s90, 32768\n"
"s_add_u32 s46, s84, s86\n"
"buffer_load_dwordx4 v4, s[72:75], s46 offen lds\n"
"v_mfma_f32_16x16x32_bf16 v[60:63], v[28:31], v[164:167], v[60:63]\n"
"ds_read_b128 v[44:47], v19 offset:5120\n"
"v_mfma_f32_16x16x32_bf16 v[56:59], v[28:31], v[160:163], v[56:59]\n"
"s_add_u32 m0, s90, 33792\n"
"s_add_u32 s46, s84, s86\n"
"buffer_load_dwordx4 v5, s[72:75], s46 offen lds\n"
"v_mfma_f32_16x16x32_bf16 v[64:67], v[32:35], v[160:163], v[64:67]\n"
"ds_read_b128 v[24:27], v19 offset:6144\n"
"v_mfma_f32_16x16x32_bf16 v[68:71], v[32:35], v[164:167], v[68:71]\n"
"s_add_u32 m0, s90, 34816\n"
"s_add_u32 s46, s85, s86\n"
"buffer_load_dwordx4 v4, s[72:75], s46 offen lds\n"
"s_waitcnt lgkmcnt(3)\n"
"v_mfma_f32_16x16x32_bf16 v[76:79], v[36:39], v[164:167], v[76:79]\n"
"ds_read_b128 v[28:31], v19 offset:7168\n"
"s_setprio 0\n"
"v_mfma_f32_16x16x32_bf16 v[72:75], v[36:39], v[160:163], v[72:75]\n"
"s_add_u32 m0, s90, 35840\n"
"s_add_u32 s46, s85, s86\n"
"buffer_load_dwordx4 v5, s[72:75], s46 offen lds\n"
"s_waitcnt lgkmcnt(3)\n"
"v_mfma_f32_16x16x32_bf16 v[80:83], v[40:43], v[160:163], v[80:83]\n"
"ds_read_b128 v[32:35], v19 offset:8192\n"
"v_mfma_f32_16x16x32_bf16 v[84:87], v[40:43], v[164:167], v[84:87]\n"
"s_waitcnt vmcnt(7)\n"
"ds_read_b128 v[176:179], v6\n"
"s_branch .Lsk3_0\n"
".Lst3_0:\n"
"s_branch .Lbk3\n"
".Lsk3_0:\n"
"s_waitcnt lgkmcnt(4)\n"
"v_mfma_f32_16x16x32_bf16 v[92:95], v[44:47], v[164:167], v[92:95]\n"
"ds_read_b128 v[36:39], v19 offset:9216\n"
"v_mfma_f32_16x16x32_bf16 v[88:91], v[44:47], v[160:163], v[88:91]\n"
"s_waitcnt vmcnt(4)\n"
"s_barrier\n"
"s_waitcnt lgkmcnt(4)\n"
"v_mfma_f32_16x16x32_bf16 v[48:51], v[24:27], v[168:171], v[48:51]\n"
"ds_read_b128 v[40:43], v19 offset:10240\n"
"s_setprio 3\n"
"v_mfma_f32_16x16x32_bf16 v[52:55], v[24:27], v[172:175], v[52:55]\n"
"ds_read_b128 v[180:183], v6 offset:2048\n"
"s_waitcnt lgkmcnt(5)\n"
"v_mfma_f32_16x16x32_bf16 v[60:63], v[28:31], v[172:175], v[60:63]\n"
"ds_read_b128 v[44:47], v19 offset:11264\n"
"v_mfma_f32_16x16x32_bf16 v[56:59], v[28:31], v[168:171], v[56:59]\n"
"s_add_u32 m0, s76, 122880\n"
"s_add_u32 s92, s89, 5\n"
"s_and_b32 s92, s92, 7\n"
"s_mul_i32 s92, s92, 18432\n"
"s_add_u32 s92, s92, s87\n"
"buffer_load_dwordx4 v1, s[48:51], s92 offen sc1 lds\n"
"s_waitcnt lgkmcnt(5)\n"
"v_mfma_f32_16x16x32_bf16 v[64:67], v[32:35], v[168:171], v[64:67]\n"
"v_add_u32 v19, 98304, v22\n"
"ds_read_b128 v[24:27], v19\n"
"v_mfma_f32_16x16x32_bf16 v[68:71], v[32:35], v[172:175], v[68:71]\n"
"ds_read_b128 v[184:187], v7\n"
"s_waitcnt lgkmcnt(5)\n"
"v_mfma_f32_16x16x32_bf16 v[76:79], v[36:39], v[172:175], v[76:79]\n"
"ds_read_b128 v[28:31], v19 offset:1024\n"
"s_setprio 2\n"
"v_mfma_f32_16x16x32_bf16 v[72:75], v[36:39], v[168:171], v[72:75]\n"
"s_add_u32 m0, s76, 122880\n"
"s_nop 0\n"
"buffer_load_dwordx4 v1, s[48:51], s92 offen offset:1024 sc1 lds\n"
"s_waitcnt lgkmcnt(5)\n"
"v_mfma_f32_16x16x32_bf16 v[80:83], v[40:43], v[168:171], v[80:83]\n"
"ds_read_b128 v[32:35], v19 offset:2048\n"
"v_mfma_f32_16x16x32_bf16 v[84:87], v[40:43], v[172:175], v[84:87]\n"
"ds_read_b128 v[188:191], v7 offset:2048\n"
"s_waitcnt lgkmcnt(5)\n"
"v_mfma_f32_16x16x32_bf16 v[92:95], v[44:47], v[172:175], v[92:95]\n"
"ds_read_b128 v[36:39], v19 offset:3072\n"
"v_mfma_f32_16x16x32_bf16 v[88:91], v[44:47], v[168:171], v[88:91]\n"
"s_add_u32 m0, s76, 122880\n"
"s_nop 0\n"
"buffer_load_dwordx4 v1, s[48:51], s92 offen offset:2048 sc1 lds\n"
"s_waitcnt lgkmcnt(5)\n"
"v_mfma_f32_16x16x32_bf16 v[48:51], v[24:27], v[176:179], v[48:51]\n"
"ds_read_b128 v[40:43], v19 offset:4096\n"
"s_setprio 1\n"
"v_mfma_f32_16x16x32_bf16 v[52:55], v[24:27], v[180:183], v[52:55]\n"
"s_waitcnt lgkmcnt(2)\n"
"s_add_u32 s86, s89, 6\n"
"s_and_b32 s86, s86, 7\n"
"s_lshl_b32 s86, s86, 7\n"
"s_add_u32 m0, s90, 0\n"
"s_add_u32 s46, s84, s86\n"
"buffer_load_dwordx4 v4, s[72:75], s46 offen lds\n"
"v_mfma_f32_16x16x32_bf16 v[60:63], v[28:31], v[180:183], v[60:63]\n"
"ds_read_b128 v[44:47], v19 offset:5120\n"
"v_mfma_f32_16x16x32_bf16 v[56:59], v[28:31], v[176:179], v[56:59]\n"
"s_add_u32 m0, s90, 1024\n"
"s_add_u32 s46, s84, s86\n"
"buffer_load_dwordx4 v5, s[72:75], s46 offen lds\n"
"v_mfma_f32_16x16x32_bf16 v[64:67], v[32:35], v[176:179], v[64:67]\n"
"ds_read_b128 v[24:27], v19 offset:6144\n"
"v_mfma_f32_16x16x32_bf16 v[68:71], v[32:35], v[180:183], v[68:71]\n"
"s_add_u32 m0, s90, 2048\n"
"s_add_u32 s46, s85, s86\n"
"buffer_load_dwordx4 v4, s[72:75], s46 offen lds\n"
"s_waitcnt lgkmcnt(3)\n"
"v_mfma_f32_16x16x32_bf16 v[76:79], v[36:39], v[180:183], v[76:79]\n"
"ds_read_b128 v[28:31], v19 offset:7168\n"
"s_setprio 0\n"
"v_mfma_f32_16x16x32_bf16 v[72:75], v[36:39], v[176:179], v[72:75]\n"
"s_add_u32 m0, s90, 3072\n"
"s_add_u32 s46, s85, s86\n"
"buffer_load_dwordx4 v5, s[72:75], s46 offen lds\n"
"s_waitcnt lgkmcnt(3)\n"
"v_mfma_f32_16x16x32_bf16 v[80:83], v[40:43], v[176:179], v[80:83]\n"
"ds_read_b128 v[32:35], v19 offset:8192\n"
"v_mfma_f32_16x16x32_bf16 v[84:87], v[40:43], v[180:183], v[84:87]\n"
"s_waitcnt vmcnt(7)\n"
"ds_read_b128 v[192:195], v6 offset:32768\n"
"s_waitcnt lgkmcnt(4)\n"
"v_mfma_f32_16x16x32_bf16 v[92:95], v[44:47], v[180:183], v[92:95]\n"
"ds_read_b128 v[36:39], v19 offset:9216\n"
"v_mfma_f32_16x16x32_bf16 v[88:91], v[44:47], v[176:179], v[88:91]\n"
"s_waitcnt vmcnt(4)\n"
"s_barrier\n"
"s_waitcnt lgkmcnt(4)\n"
"v_mfma_f32_16x16x32_bf16 v[48:51], v[24:27], v[184:187], v[48:51]\n"
"ds_read_b128 v[40:43], v19 offset:10240\n"
"s_setprio 3\n"
"v_mfma_f32_16x16x32_bf16 v[52:55], v[24:27], v[188:191], v[52:55]\n"
"ds_read_b128 v[196:199], v6 offset:34816\n"
"s_waitcnt lgkmcnt(5)\n"
"v_mfma_f32_16x16x32_bf16 v[60:63], v[28:31], v[188:191], v[60:63]\n"
"ds_read_b128 v[44:47], v19 offset:11264\n"
"v_mfma_f32_16x16x32_bf16 v[56:59], v[28:31], v[184:187], v[56:59]\n"
"s_add_u32 m0, s76, 73728\n"
"s_add_u32 s92, s89, 6\n"
"s_and_b32 s92, s92, 7\n"
"s_mul_i32 s92, s92, 18432\n"
"s_add_u32 s92, s92, s87\n"
"buffer_load_dwordx4 v1, s[48:51], s92 offen sc1 lds\n"
"s_waitcnt lgkmcnt(5)\n"
"v_mfma_f32_16x16x32_bf16 v[64:67], v[32:35], v[184:187], v[64:67]\n"
"v_add_u32 v19, 122880, v22\n"
"ds_read_b128 v[24:27], v19\n"
"v_mfma_f32_16x16x32_bf16 v[68:71], v[32:35], v[188:191], v[68:71]\n"
"ds_read_b128 v[200:203], v7 offset:32768\n"
"s_waitcnt lgkmcnt(5)\n"
"v_mfma_f32_16x16x32_bf16 v[76:79], v[36:39], v[188:191], v[76:79]\n"
"ds_read_b128 v[28:31], v19 offset:1024\n"
"s_setprio 2\n"
"v_mfma_f32_16x16x32_bf16 v[72:75], v[36:39], v[184:187], v[72:75]\n"
"s_add_u32 m0, s76, 73728\n"
"s_nop 0\n"
"buffer_load_dwordx4 v1, s[48:51], s92 offen offset:1024 sc1 lds\n"
"s_waitcnt lgkmcnt(5)\n"
"v_mfma_f32_16x16x32_bf16 v[80:83], v[40:43], v[184:187], v[80:83]\n"
"ds_read_b128 v[32:35], v19 offset:2048\n"
"v_mfma_f32_16x16x32_bf16 v[84:87], v[40:43], v[188:191], v[84:87]\n"
"ds_read_b128 v[204:207], v7 offset:34816\n"
"s_waitcnt lgkmcnt(5)\n"
"v_mfma_f32_16x16x32_bf16 v[92:95], v[44:47], v[188:191], v[92:95]\n"
"ds_read_b128 v[36:39], v19 offset:3072\n"
"v_mfma_f32_16x16x32_bf16 v[88:91], v[44:47], v[184:187], v[88:91]\n"
"s_add_u32 m0, s76, 73728\n"
"s_nop 0\n"
"buffer_load_dwordx4 v1, s[48:51], s92 offen offset:2048 sc1 lds\n"
"s_waitcnt lgkmcnt(5)\n"
"v_mfma_f32_16x16x32_bf16 v[48:51], v[24:27], v[192:195], v[48:51]\n"
"ds_read_b128 v[40:43], v19 offset:4096\n"
"s_setprio 1\n"
"v_mfma_f32_16x16x32_bf16 v[52:55], v[24:27], v[196:199], v[52:55]\n"
"s_waitcnt lgkmcnt(2)\n"
"s_add_u32 s86, s89, 7\n"
"s_and_b32 s86, s86, 7\n"
"s_lshl_b32 s86, s86, 7\n"
"s_add_u32 m0, s90, 32768\n"
"s_add_u32 s46, s84, s86\n"
"buffer_load_dwordx4 v4, s[72:75], s46 offen lds\n"
"v_mfma_f32_16x16x32_bf16 v[60:63], v[28:31], v[196:199], v[60:63]\n"
"ds_read_b128 v[44:47], v19 offset:5120\n"
"v_mfma_f32_16x16x32_bf16 v[56:59], v[28:31], v[192:195], v[56:59]\n"
"s_add_u32 m0, s90, 33792\n"
"s_add_u32 s46, s84, s86\n"
"buffer_load_dwordx4 v5, s[72:75], s46 offen lds\n"
"v_mfma_f32_16x16x32_bf16 v[64:67], v[32:35], v[192:195], v[64:67]\n"
"ds_read_b128 v[24:27], v19 offset:6144\n"
"v_mfma_f32_16x16x32_bf16 v[68:71], v[32:35], v[196:199], v[68:71]\n"
"s_add_u32 m0, s90, 34816\n"
"s_add_u32 s46, s85, s86\n"
"buffer_load_dwordx4 v4, s[72:75], s46 offen lds\n"
"s_waitcnt lgkmcnt(3)\n"
"v_mfma_f32_16x16x32_bf16 v[76:79], v[36:39], v[196:199], v[76:79]\n"
"ds_read_b128 v[28:31], v19 offset:7168\n"
"s_setprio 0\n"
"v_mfma_f32_16x16x32_bf16 v[72:75], v[36:39], v[192:195], v[72:75]\n"
"s_add_u32 m0, s90, 35840\n"
"s_add_u32 s46, s85, s86\n"
"buffer_load_dwordx4 v5, s[72:75], s46 offen lds\n"
"s_waitcnt lgkmcnt(3)\n"
"v_mfma_f32_16x16x32_bf16 v[80:83], v[40:43], v[192:195], v[80:83]\n"
"ds_read_b128 v[32:35], v19 offset:8192\n"
"v_mfma_f32_16x16x32_bf16 v[84:87], v[40:43], v[196:199], v[84:87]\n"
"s_waitcnt vmcnt(7)\n"
"ds_read_b128 v[208:211], v6\n"
"s_waitcnt lgkmcnt(4)\n"
"v_mfma_f32_16x16x32_bf16 v[92:95], v[44:47], v[196:199], v[92:95]\n"
"ds_read_b128 v[36:39], v19 offset:9216\n"
"v_mfma_f32_16x16x32_bf16 v[88:91], v[44:47], v[192:195], v[88:91]\n"
"s_waitcnt vmcnt(4)\n"
"s_barrier\n"
"s_waitcnt lgkmcnt(4)\n"
"v_mfma_f32_16x16x32_bf16 v[48:51], v[24:27], v[200:203], v[48:51]\n"
"ds_read_b128 v[40:43], v19 offset:10240\n"
"s_setprio 3\n"
"v_mfma_f32_16x16x32_bf16 v[52:55], v[24:27], v[204:207], v[52:55]\n"
"ds_read_b128 v[212:215], v6 offset:2048\n"
"s_waitcnt lgkmcnt(5)\n"
"v_mfma_f32_16x16x32_bf16 v[60:63], v[28:31], v[204:207], v[60:63]\n"
"ds_read_b128 v[44:47], v19 offset:11264\n"
"v_mfma_f32_16x16x32_bf16 v[56:59], v[28:31], v[200:203], v[56:59]\n"
"s_add_u32 m0, s76, 98304\n"
"s_add_u32 s92, s89, 7\n"
"s_and_b32 s92, s92, 7\n"
"s_mul_i32 s92, s92, 18432\n"
"s_add_u32 s92, s92, s87\n"
"buffer_load_dwordx4 v1, s[48:51], s92 offen sc1 lds\n"
"s_waitcnt lgkmcnt(5)\n"
"v_mfma_f32_16x16x32_bf16 v[64:67], v[32:35], v[200:203], v[64:67]\n"
"v_add_u32 v19, 73728, v22\n"
"ds_read_b128 v[24:27], v19\n"
"v_mfma_f32_16x16x32_bf16 v[68:71], v[32:35], v[204:207], v[68:71]\n"
"ds_read_b128 v[216:219], v7\n"
"s_waitcnt lgkmcnt(5)\n"
"v_mfma_f32_16x16x32_bf16 v[76:79], v[36:39], v[204:207], v[76:79]\n"
"ds_read_b128 v[28:31], v19 offset:1024\n"
"s_setprio 2\n"
"v_mfma_f32_16x16x32_bf16 v[72:75], v[36:39], v[200:203], v[72:75]\n"
"s_add_u32 m0, s76, 98304\n"
"s_nop 0\n"
"buffer_load_dwordx4 v1, s[48:51], s92 offen offset:1024 sc1 lds\n"
"s_waitcnt lgkmcnt(5)\n"
"v_mfma_f32_16x16x32_bf16 v[80:83], v[40:43], v[200:203], v[80:83]\n"
"ds_read_b128 v[32:35], v19 offset:2048\n"
"v_mfma_f32_16x16x32_bf16 v[84:87], v[40:43], v[204:207], v[84:87]\n"
"ds_read_b128 v[220:223], v7 offset:2048\n"
"s_waitcnt lgkmcnt(5)\n"
"v_mfma_f32_16x16x32_bf16 v[92:95], v[44:47], v[204:207], v[92:95]\n"
"ds_read_b128 v[36:39], v19 offset:3072\n"
"v_mfma_f32_16x16x32_bf16 v[88:91], v[44:47], v[200:203], v[88:91]\n"
"s_add_u32 m0, s76, 98304\n"
"s_nop 0\n"
"buffer_load_dwordx4 v1, s[48:51], s92 offen offset:2048 sc1 lds\n"
"s_waitcnt lgkmcnt(5)\n"
"v_mfma_f32_16x16x32_bf16 v[48:51], v[24:27], v[208:211], v[48:51]\n"
"ds_read_b128 v[40:43], v19 offset:4096\n"
"s_setprio 1\n"
"v_mfma_f32_16x16x32_bf16 v[52:55], v[24:27], v[212:215], v[52:55]\n"
"s_waitcnt lgkmcnt(4)\n"
"v_mfma_f32_16x16x32_bf16 v[60:63], v[28:31], v[212:215], v[60:63]\n"
"ds_read_b128 v[44:47], v19 offset:5120\n"
"v_mfma_f32_16x16x32_bf16 v[56:59], v[28:31], v[208:211], v[56:59]\n"
"s_waitcnt lgkmcnt(4)\n"
"v_mfma_f32_16x16x32_bf16 v[64:67], v[32:35], v[208:211], v[64:67]\n"
"ds_read_b128 v[24:27], v19 offset:6144\n"
"v_mfma_f32_16x16x32_bf16 v[68:71], v[32:35], v[212:215], v[68:71]\n"
"s_waitcnt lgkmcnt(3)\n"
"v_mfma_f32_16x16x32_bf16 v[76:79], v[36:39], v[212:215], v[76:79]\n"
"ds_read_b128 v[28:31], v19 offset:7168\n"
"s_setprio 0\n"
"v_mfma_f32_16x16x32_bf16 v[72:75], v[36:39], v[208:211], v[72:75]\n"
"s_waitcnt lgkmcnt(3)\n"
"v_mfma_f32_16x16x32_bf16 v[80:83], v[40:43], v[208:211], v[80:83]\n"
"ds_read_b128 v[32:35], v19 offset:8192\n"
"v_mfma_f32_16x16x32_bf16 v[84:87], v[40:43], v[212:215], v[84:87]\n"
"s_waitcnt vmcnt(3)\n"
"ds_read_b128 v[224:227], v6 offset:32768\n"
"s_waitcnt lgkmcnt(4)\n"
"v_mfma_f32_16x16x32_bf16 v[92:95], v[44:47], v[212:215], v[92:95]\n"
"ds_read_b128 v[36:39], v19 offset:9216\n"
"v_mfma_f32_16x16x32_bf16 v[88:91], v[44:47], v[208:211], v[88:91]\n"
"s_waitcnt vmcnt(0)\n"
"s_barrier\n"
"s_waitcnt lgkmcnt(4)\n"
"v_mfma_f32_16x16x32_bf16 v[48:51], v[24:27], v[216:219], v[48:51]\n"
"ds_read_b128 v[40:43], v19 offset:10240\n"
"s_setprio 3\n"
"v_mfma_f32_16x16x32_bf16 v[52:55], v[24:27], v[220:223], v[52:55]\n"
"ds_read_b128 v[228:231], v6 offset:34816\n"
"s_waitcnt lgkmcnt(5)\n"
"v_mfma_f32_16x16x32_bf16 v[60:63], v[28:31], v[220:223], v[60:63]\n"
"ds_read_b128 v[44:47], v19 offset:11264\n"
"v_mfma_f32_16x16x32_bf16 v[56:59], v[28:31], v[216:219], v[56:59]\n"
"s_add_u32 m0, s76, 122880\n"
"s_add_u32 s92, s91, 0\n"
"s_and_b32 s92, s92, 7\n"
"s_mul_i32 s92, s92, 18432\n"
"s_add_u32 s92, s92, s88\n"
"buffer_load_dwordx4 v1, s[48:51], s92 offen sc1 lds\n"
"s_waitcnt lgkmcnt(5)\n"
"v_mfma_f32_16x16x32_bf16 v[64:67], v[32:35], v[216:219], v[64:67]\n"
"v_add_u32 v19, 98304, v22\n"
"ds_read_b128 v[24:27], v19\n"
"v_mfma_f32_16x16x32_bf16 v[68:71], v[32:35], v[220:223], v[68:71]\n"
"ds_read_b128 v[232:235], v7 offset:32768\n"
"s_waitcnt lgkmcnt(5)\n"
"v_mfma_f32_16x16x32_bf16 v[76:79], v[36:39], v[220:223], v[76:79]\n"
"ds_read_b128 v[28:31], v19 offset:1024\n"
"s_setprio 2\n"
"v_mfma_f32_16x16x32_bf16 v[72:75], v[36:39], v[216:219], v[72:75]\n"
"s_add_u32 m0, s76, 122880\n"
"s_nop 0\n"
"buffer_load_dwordx4 v1, s[48:51], s92 offen offset:1024 sc1 lds\n"
"s_waitcnt lgkmcnt(5)\n"
"v_mfma_f32_16x16x32_bf16 v[80:83], v[40:43], v[216:219], v[80:83]\n"
"ds_read_b128 v[32:35], v19 offset:2048\n"
"v_mfma_f32_16x16x32_bf16 v[84:87], v[40:43], v[220:223], v[84:87]\n"
"ds_read_b128 v[236:239], v7 offset:34816\n"
"s_waitcnt lgkmcnt(5)\n"
"v_mfma_f32_16x16x32_bf16 v[92:95], v[44:47], v[220:223], v[92:95]\n"
"ds_read_b128 v[36:39], v19 offset:3072\n"
"v_mfma_f32_16x16x32_bf16 v[88:91], v[44:47], v[216:219], v[88:91]\n"
"s_add_u32 m0, s76, 122880\n"
"s_nop 0\n"
"buffer_load_dwordx4 v1, s[48:51], s92 offen offset:2048 sc1 lds\n"
"s_waitcnt lgkmcnt(5)\n"
"v_mfma_f32_16x16x32_bf16 v[48:51], v[24:27], v[224:227], v[48:51]\n"
"ds_read_b128 v[40:43], v19 offset:4096\n"
"s_setprio 1\n"
"v_mfma_f32_16x16x32_bf16 v[52:55], v[24:27], v[228:231], v[52:55]\n"
"s_add_u32 m0, s76, 0\n"
"s_add_u32 s92, s91, 2\n"
"s_and_b32 s92, s92, 7\n"
"s_mul_i32 s92, s92, 18432\n"
"s_add_u32 s92, s92, s88\n"
"buffer_load_dwordx4 v1, s[48:51], s92 offen sc1 lds\n"
"s_waitcnt lgkmcnt(4)\n"
"v_mfma_f32_16x16x32_bf16 v[60:63], v[28:31], v[228:231], v[60:63]\n"
"ds_read_b128 v[44:47], v19 offset:5120\n"
"v_mfma_f32_16x16x32_bf16 v[56:59], v[28:31], v[224:227], v[56:59]\n"
"s_add_u32 m0, s76, 0\n"
"s_nop 0\n"
"buffer_load_dwordx4 v1, s[48:51], s92 offen offset:1024 sc1 lds\n"
"s_waitcnt lgkmcnt(4)\n"
"v_mfma_f32_16x16x32_bf16 v[64:67], v[32:35], v[224:227], v[64:67]\n"
"ds_read_b128 v[24:27], v19 offset:6144\n"
"v_mfma_f32_16x16x32_bf16 v[68:71], v[32:35], v[228:231], v[68:71]\n"
"s_add_u32 m0, s76, 0\n"
"s_nop 0\n"
"buffer_load_dwordx4 v1, s[48:51], s92 offen offset:2048 sc1 lds\n"
"s_waitcnt lgkmcnt(3)\n"
"v_mfma_f32_16x16x32_bf16 v[76:79], v[36:39], v[228:231], v[76:79]\n"
"ds_read_b128 v[28:31], v19 offset:7168\n"
"s_setprio 0\n"
"v_mfma_f32_16x16x32_bf16 v[72:75], v[36:39], v[224:227], v[72:75]\n"
"s_waitcnt lgkmcnt(3)\n"
"v_mfma_f32_16x16x32_bf16 v[80:83], v[40:43], v[224:227], v[80:83]\n"
"ds_read_b128 v[32:35], v19 offset:8192\n"
"v_mfma_f32_16x16x32_bf16 v[84:87], v[40:43], v[228:231], v[84:87]\n"
"s_waitcnt lgkmcnt(3)\n"
"v_mfma_f32_16x16x32_bf16 v[92:95], v[44:47], v[228:231], v[92:95]\n"
"ds_read_b128 v[36:39], v19 offset:9216\n"
"v_mfma_f32_16x16x32_bf16 v[88:91], v[44:47], v[224:227], v[88:91]\n"
"s_waitcnt vmcnt(3)\n"
"s_barrier\n"
"s_waitcnt lgkmcnt(3)\n"
"v_mfma_f32_16x16x32_bf16 v[48:51], v[24:27], v[232:235], v[48:51]\n"
"ds_read_b128 v[40:43], v19 offset:10240\n"
"s_setprio 3\n"
"v_mfma_f32_16x16x32_bf16 v[52:55], v[24:27], v[236:239], v[52:55]\n"
"s_add_u32 m0, s76, 24576\n"
"s_add_u32 s92, s91, 4\n"
"s_and_b32 s92, s92, 7\n"
"s_mul_i32 s92, s92, 18432\n"
"s_add_u32 s92, s92, s88\n"
"buffer_load_dwordx4 v1, s[48:51], s92 offen sc1 lds\n"
"s_waitcnt lgkmcnt(3)\n"
"v_mfma_f32_16x16x32_bf16 v[60:63], v[28:31], v[236:239], v[60:63]\n"
"ds_read_b128 v[44:47], v19 offset:11264\n"
"v_mfma_f32_16x16x32_bf16 v[56:59], v[28:31], v[232:235], v[56:59]\n"
"s_add_u32 m0, s76, 24576\n"
"s_nop 0\n"
"buffer_load_dwordx4 v1, s[48:51], s92 offen offset:1024 sc1 lds\n"
"s_waitcnt lgkmcnt(3)\n"
"v_mfma_f32_16x16x32_bf16 v[64:67], v[32:35], v[232:235], v[64:67]\n"
"v_add_u32 v19, 122880, v22\n"
"ds_read_b128 v[24:27], v19\n"
"s_setprio 2\n"
"v_mfma_f32_16x16x32_bf16 v[68:71], v[32:35], v[236:239], v[68:71]\n"
"s_add_u32 m0, s76, 24576\n"
"s_nop 0\n"
"buffer_load_dwordx4 v1, s[48:51], s92 offen offset:2048 sc1 lds\n"
"s_waitcnt lgkmcnt(3)\n"
"v_mfma_f32_16x16x32_bf16 v[76:79], v[36:39], v[236:239], v[76:79]\n"
"ds_read_b128 v[28:31], v19 offset:1024\n"
"v_mfma_f32_16x16x32_bf16 v[72:75], v[36:39], v[232:235], v[72:75]\n"
"s_add_u32 m0, s76, 49152\n"
"s_add_u32 s92, s91, 6\n"
"s_and_b32 s92, s92, 7\n"
"s_mul_i32 s92, s92, 18432\n"
"s_add_u32 s92, s92, s88\n"
"buffer_load_dwordx4 v1, s[48:51], s92 offen sc1 lds\n"
"s_waitcnt lgkmcnt(3)\n"
"v_mfma_f32_16x16x32_bf16 v[80:83], v[40:43], v[232:235], v[80:83]\n"
"ds_read_b128 v[32:35], v19 offset:3072\n"
"s_setprio 1\n"
"v_mfma_f32_16x16x32_bf16 v[84:87], v[40:43], v[236:239], v[84:87]\n"
"s_add_u32 m0, s76, 49152\n"
"s_nop 0\n"
"buffer_load_dwordx4 v1, s[48:51], s92 offen offset:1024 sc1 lds\n"
"s_waitcnt lgkmcnt(3)\n"
"v_mfma_f32_16x16x32_bf16 v[92:95], v[44:47], v[236:239], v[92:95]\n"
"ds_read_b128 v[36:39], v19 offset:4096\n"
"v_mfma_f32_16x16x32_bf16 v[88:91], v[44:47], v[232:235], v[88:91]\n"
"s_add_u32 m0, s76, 49152\n"
"s_nop 0\n"
"buffer_load_dwordx4 v1, s[48:51], s92 offen offset:2048 sc1 lds\n"
"s_waitcnt lgkmcnt(3)\n"
"v_mfma_f32_16x16x32_bf16 v[96:99], v[24:27], v[112:115], 0\n"
"ds_read_b128 v[40:43], v19 offset:6144\n"
"s_setprio 0\n"
"v_mfma_f32_16x16x32_bf16 v[100:103], v[24:27], v[116:119], 0\n"
"s_mul_i32 s45, s24, 6144\n"
"s_add_u32 s45, s45, 73728\n"
"v_add_u32 v20, s45, v1\n"
"s_mul_i32 s45, s27, 72\n"
"s_mul_i32 s42, s26, 18\n"
"s_add_u32 s45, s45, s42\n"
"v_add_u32 v3, s45, v0\n"
"v_lshrrev_b32 v254, 3, v3\n"
"v_mul_u32_u24 v254, 9216, v254\n"
"v_and_b32 v3, 7, v3\n"
"v_lshl_add_u32 v254, v3, 7, v254\n"
"v_cmp_gt_u32 vcc, 18, v0\n"
"v_mov_b32 v3, 0x80000000\n"
"s_nop 0\n"
"v_cndmask_b32 v254, v3, v254, vcc\n"
"s_waitcnt lgkmcnt(3)\n"
"v_mfma_f32_16x16x32_bf16 v[108:111], v[28:31], v[116:119], 0\n"
"ds_read_b128 v[44:47], v19 offset:7168\n"
"v_mfma_f32_16x16x32_bf16 v[104:107], v[28:31], v[112:115], 0\n"
"s_waitcnt vmcnt(6)\n"
"s_barrier\n"
"s_waitcnt lgkmcnt(3)\n"
"v_mfma_f32_16x16x32_bf16 v[96:99], v[32:35], v[120:123], v[96:99]\n"
"ds_read_b128 v[24:27], v19 offset:9216\n"
"s_setprio 3\n"
"v_mfma_f32_16x16x32_bf16 v[100:103], v[32:35], v[124:127], v[100:103]\n"
"ds_write_b128 v20, v[52:55]\n"
"buffer_load_dword v253, v254, s[52:55], s82 offen\n"
"s_waitcnt lgkmcnt(4)\n"
"v_mfma_f32_16x16x32_bf16 v[108:111], v[36:39], v[124:127], v[108:111]\n"
"ds_read_b128 v[28:31], v19 offset:10240\n"
"v_mfma_f32_16x16x32_bf16 v[104:107], v[36:39], v[120:123], v[104:107]\n"
"s_add_u32 s46, s82, 2654208\n"
"buffer_load_dword v255, v254, s[52:55], s46 offen\n"
"s_waitcnt lgkmcnt(4)\n"
"v_mfma_f32_16x16x32_bf16 v[96:99], v[40:43], v[128:131], v[96:99]\n"
"v_add_u32 v19, 0, v22\n"
"ds_read_b128 v[32:35], v19\n"
"s_setprio 2\n"
"v_mfma_f32_16x16x32_bf16 v[100:103], v[40:43], v[132:135], v[100:103]\n"
"s_mul_i32 s46, s35, 9216\n"
"s_mul_i32 s42, s24, 128\n"
"s_add_u32 s46, s46, s42\n"
"buffer_load_dwordx4 v[248:251], v9, s[64:67], s46 offen\n"
"ds_write_b128 v20, v[60:63] offset:1024\n"
"s_waitcnt lgkmcnt(5)\n"
"v_mfma_f32_16x16x32_bf16 v[108:111], v[44:47], v[132:135], v[108:111]\n"
"ds_read_b128 v[36:39], v19 offset:1024\n"
"v_mfma_f32_16x16x32_bf16 v[104:107], v[44:47], v[128:131], v[104:107]\n"
"s_waitcnt lgkmcnt(5)\n"
"v_mfma_f32_16x16x32_bf16 v[96:99], v[24:27], v[136:139], v[96:99]\n"
"ds_read_b128 v[40:43], v19 offset:3072\n"
"s_setprio 1\n"
"v_mfma_f32_16x16x32_bf16 v[100:103], v[24:27], v[140:143], v[100:103]\n"
"s_mul_i32 s46, s35, 9216\n"
"s_mul_i32 s42, s24, 16\n"
"s_add_u32 s46, s46, s42\n"
"buffer_load_short_d16_hi v252, v8, s[64:67], s46 offen offset:1024\n"
"ds_write_b128 v20, v[68:71] offset:2048\n"
"s_waitcnt lgkmcnt(5)\n"
"v_mfma_f32_16x16x32_bf16 v[108:111], v[28:31], v[140:143], v[108:111]\n"
"ds_read_b128 v[44:47], v19 offset:4096\n"
"v_mfma_f32_16x16x32_bf16 v[104:107], v[28:31], v[136:139], v[104:107]\n"
"s_waitcnt lgkmcnt(5)\n"
"v_mfma_f32_16x16x32_bf16 v[96:99], v[32:35], v[144:147], v[96:99]\n"
"ds_read_b128 v[24:27], v19 offset:6144\n"
"s_setprio 0\n"
"v_mfma_f32_16x16x32_bf16 v[100:103], v[32:35], v[148:151], v[100:103]\n"
"ds_write_b128 v20, v[76:79] offset:3072\n"
"s_waitcnt lgkmcnt(5)\n"
"v_mfma_f32_16x16x32_bf16 v[108:111], v[36:39], v[148:151], v[108:111]\n"
"ds_read_b128 v[28:31], v19 offset:7168\n"
"v_mfma_f32_16x16x32_bf16 v[104:107], v[36:39], v[144:147], v[104:107]\n"
"s_waitcnt vmcnt(4)\n"
"s_barrier\n"
"s_waitcnt lgkmcnt(5)\n"
"v_mfma_f32_16x16x32_bf16 v[96:99], v[40:43], v[152:155], v[96:99]\n"
"ds_read_b128 v[32:35], v19 offset:9216\n"
"s_setprio 3\n"
"v_mfma_f32_16x16x32_bf16 v[100:103], v[40:43], v[156:159], v[100:103]\n"
"ds_write_b128 v20, v[84:87] offset:4096\n"
"s_waitcnt lgkmcnt(5)\n"
"v_mfma_f32_16x16x32_bf16 v[108:111], v[44:47], v[156:159], v[108:111]\n"
"ds_read_b128 v[36:39], v19 offset:10240\n"
"v_mfma_f32_16x16x32_bf16 v[104:107], v[44:47], v[152:155], v[104:107]\n"
"s_setprio 2\n"
"s_waitcnt lgkmcnt(5)\n"
"v_mfma_f32_16x16x32_bf16 v[96:99], v[24:27], v[160:163], v[96:99]\n"
"v_add_u32 v19, 24576, v22\n"
"ds_read_b128 v[40:43], v19\n"
"v_mfma_f32_16x16x32_bf16 v[100:103], v[24:27], v[164:167], v[100:103]\n"
"ds_write_b128 v20, v[92:95] offset:5120\n"
"s_waitcnt lgkmcnt(5)\n"
"v_mfma_f32_16x16x32_bf16 v[108:111], v[28:31], v[164:167], v[108:111]\n"
"ds_read_b128 v[44:47], v19 offset:1024\n"
"s_setprio 1\n"
"v_mfma_f32_16x16x32_bf16 v[104:107], v[28:31], v[160:163], v[104:107]\n"
"s_waitcnt lgkmcnt(5)\n"
"v_mfma_f32_16x16x32_bf16 v[96:99], v[32:35], v[168:171], v[96:99]\n"
"ds_read_b128 v[24:27], v19 offset:3072\n"
"v_mfma_f32_16x16x32_bf16 v[100:103], v[32:35], v[172:175], v[100:103]\n"
"s_setprio 0\n"
"s_waitcnt lgkmcnt(4)\n"
"v_mfma_f32_16x16x32_bf16 v[108:111], v[36:39], v[172:175], v[108:111]\n"
"ds_read_b128 v[28:31], v19 offset:4096\n"
"v_mfma_f32_16x16x32_bf16 v[104:107], v[36:39], v[168:171], v[104:107]\n"
"s_xor_b32 s45, s24, 4\n"
"s_mul_i32 s45, s45, 6144\n"
"s_add_u32 s45, s45, 73728\n"
"v_add_u32 v20, s45, v1\n"
"s_waitcnt lgkmcnt(3)\n"
"s_barrier\n"
"v_mfma_f32_16x16x32_bf16 v[96:99], v[40:43], v[176:179], v[96:99]\n"
"ds_read_b128 v[32:35], v19 offset:6144\n"
"s_setprio 3\n"
"ds_read_b128 v[52:55], v20\n"
"v_mfma_f32_16x16x32_bf16 v[100:103], v[40:43], v[180:183], v[100:103]\n"
"ds_read_b128 v[60:63], v20 offset:1024\n"
"s_waitcnt lgkmcnt(5)\n"
"v_mfma_f32_16x16x32_bf16 v[108:111], v[44:47], v[180:183], v[108:111]\n"
"ds_read_b128 v[36:39], v19 offset:7168\n"
"ds_read_b128 v[68:71], v20 offset:2048\n"
"v_mfma_f32_16x16x32_bf16 v[104:107], v[44:47], v[176:179], v[104:107]\n"
"ds_read_b128 v[76:79], v20 offset:3072\n"
"s_waitcnt lgkmcnt(7)\n"
"v_mfma_f32_16x16x32_bf16 v[96:99], v[24:27], v[184:187], v[96:99]\n"
"ds_read_b128 v[40:43], v19 offset:9216\n"
"ds_read_b128 v[84:87], v20 offset:4096\n"
"v_mfma_f32_16x16x32_bf16 v[100:103], v[24:27], v[188:191], v[100:103]\n"
"ds_read_b128 v[92:95], v20 offset:5120\n"
"s_waitcnt lgkmcnt(9)\n"
"v_mfma_f32_16x16x32_bf16 v[108:111], v[28:31], v[188:191], v[108:111]\n"
"ds_read_b128 v[44:47], v19 offset:10240\n"
"s_waitcnt lgkmcnt(8)\n"
"v_add_f32 v48, v48, v52\n"
"v_mfma_f32_16x16x32_bf16 v[104:107], v[28:31], v[184:187], v[104:107]\n"
"v_add_f32 v49, v49, v53\n"
"v_mfma_f32_16x16x32_bf16 v[96:99], v[32:35], v[192:195], v[96:99]\n"
"v_add_u32 v19, 49152, v22\n"
"ds_read_b128 v[24:27], v19\n"
"v_add_f32 v50, v50, v54\n"
"v_mfma_f32_16x16x32_bf16 v[100:103], v[32:35], v[196:199], v[100:103]\n"
"v_add_f32 v51, v51, v55\n"
"s_waitcnt lgkmcnt(7)\n"
"v_mfma_f32_16x16x32_bf16 v[108:111], v[36:39], v[196:199], v[108:111]\n"
"ds_read_b128 v[28:31], v19 offset:1024\n"
"v_add_f32 v56, v56, v60\n"
"v_mfma_f32_16x16x32_bf16 v[104:107], v[36:39], v[192:195], v[104:107]\n"
"s_setprio 2\n"
"v_add_f32 v57, v57, v61\n"
"s_waitcnt lgkmcnt(5)\n"
"v_mfma_f32_16x16x32_bf16 v[96:99], v[40:43], v[200:203], v[96:99]\n"
"ds_read_b128 v[32:35], v19 offset:3072\n"
"v_add_f32 v58, v58, v62\n"
"v_mfma_f32_16x16x32_bf16 v[100:103], v[40:43], v[204:207], v[100:103]\n"
"v_add_f32 v59, v59, v63\n"
"s_waitcnt lgkmcnt(3)\n"
"v_mfma_f32_16x16x32_bf16 v[108:111], v[44:47], v[204:207], v[108:111]\n"
"ds_read_b128 v[36:39], v19 offset:4096\n"
"v_cvt_pk_bf16_f32 v52, v48, v49\n"
"v_mfma_f32_16x16x32_bf16 v[104:107], v[44:47], v[200:203], v[104:107]\n"
"v_cvt_pk_bf16_f32 v53, v50, v51\n"
"s_waitcnt lgkmcnt(3)\n"
"v_mfma_f32_16x16x32_bf16 v[96:99], v[24:27], v[208:211], v[96:99]\n"
"ds_read_b128 v[40:43], v19 offset:6144\n"
"v_cvt_pk_bf16_f32 v54, v56, v57\n"
"v_mfma_f32_16x16x32_bf16 v[100:103], v[24:27], v[212:215], v[100:103]\n"
"v_cvt_pk_bf16_f32 v55, v58, v59\n"
"s_waitcnt lgkmcnt(3)\n"
"v_mfma_f32_16x16x32_bf16 v[108:111], v[28:31], v[212:215], v[108:111]\n"
"ds_read_b128 v[44:47], v19 offset:7168\n"
"buffer_store_dwordx4 v[52:55], v21, s[48:51], s93 offen sc1\n"
"v_mfma_f32_16x16x32_bf16 v[104:107], v[28:31], v[208:211], v[104:107]\n"
"v_add_f32 v64, v64, v68\n"
"s_waitcnt lgkmcnt(3)\n"
"v_mfma_f32_16x16x32_bf16 v[96:99], v[32:35], v[216:219], v[96:99]\n"
"ds_read_b128 v[24:27], v19 offset:9216\n"
"v_add_f32 v65, v65, v69\n"
"v_mfma_f32_16x16x32_bf16 v[100:103], v[32:35], v[220:223], v[100:103]\n"
"v_add_f32 v66, v66, v70\n"
"s_waitcnt lgkmcnt(3)\n"
"v_mfma_f32_16x16x32_bf16 v[108:111], v[36:39], v[220:223], v[108:111]\n"
"ds_read_b128 v[28:31], v19 offset:10240\n"
"s_setprio 1\n"
"v_add_f32 v67, v67, v71\n"
"v_mfma_f32_16x16x32_bf16 v[104:107], v[36:39], v[216:219], v[104:107]\n"
"v_add_f32 v72, v72, v76\n"
"s_waitcnt lgkmcnt(3)\n"
"v_mfma_f32_16x16x32_bf16 v[96:99], v[40:43], v[224:227], v[96:99]\n"
"v_add_u32 v19, 122880, v22\n"
"ds_read_b128 v[32:35], v19 offset:2048\n"
"v_add_f32 v73, v73, v77\n"
"v_mfma_f32_16x16x32_bf16 v[100:103], v[40:43], v[228:231], v[100:103]\n"
"v_add_f32 v74, v74, v78\n"
"s_waitcnt lgkmcnt(3)\n"
"v_mfma_f32_16x16x32_bf16 v[108:111], v[44:47], v[228:231], v[108:111]\n"
"ds_read_b128 v[36:39], v19 offset:5120\n"
"v_add_f32 v75, v75, v79\n"
"v_mfma_f32_16x16x32_bf16 v[104:107], v[44:47], v[224:227], v[104:107]\n"
"v_cvt_pk_bf16_f32 v68, v64, v65\n"
"s_waitcnt lgkmcnt(3)\n"
"v_mfma_f32_16x16x32_bf16 v[96:99], v[24:27], v[232:235], v[96:99]\n"
"ds_read_b128 v[40:43], v19 offset:8192\n"
"v_cvt_pk_bf16_f32 v69, v66, v67\n"
"v_mfma_f32_16x16x32_bf16 v[100:103], v[24:27], v[236:239], v[100:103]\n"
"v_cvt_pk_bf16_f32 v70, v72, v73\n"
"s_waitcnt lgkmcnt(3)\n"
"v_mfma_f32_16x16x32_bf16 v[108:111], v[28:31], v[236:239], v[108:111]\n"
"ds_read_b128 v[44:47], v19 offset:11264\n"
"v_cvt_pk_bf16_f32 v71, v74, v75\n"
"s_mul_i32 s45, s24, 2048\n"
"s_add_u32 s45, s45, 147456\n"
"v_add_u32 v20, s45, v1\n"
"v_mfma_f32_16x16x32_bf16 v[104:107], v[28:31], v[232:235], v[104:107]\n"
"v_add_f32 v80, v80, v84\n"
"s_waitcnt lgkmcnt(3)\n"
"v_mfma_f32_16x16x32_bf16 v[240:243], v[32:35], v[112:115], 0\n"
"v_add_u32 v19, 0, v22\n"
"ds_read_b128 v[24:27], v19 offset:2048\n"
"v_add_f32 v81, v81, v85\n"
"v_mfma_f32_16x16x32_bf16 v[244:247], v[32:35], v[116:119], 0\n"
"s_setprio 0\n"
"v_add_f32 v82, v82, v86\n"
"ds_write_b128 v20, v[100:103]\n"
"s_waitcnt lgkmcnt(4)\n"
"v_mfma_f32_16x16x32_bf16 v[240:243], v[36:39], v[120:123], v[240:243]\n"
"ds_read_b128 v[28:31], v19 offset:5120\n"
"v_add_f32 v83, v83, v87\n"
"v_mfma_f32_16x16x32_bf16 v[244:247], v[36:39], v[124:127], v[244:247]\n"
"v_add_f32 v88, v88, v92\n"
"s_waitcnt lgkmcnt(4)\n"
"v_mfma_f32_16x16x32_bf16 v[240:243], v[40:43], v[128:131], v[240:243]\n"
"ds_read_b128 v[32:35], v19 offset:8192\n"
"v_add_f32 v89, v89, v93\n"
"ds_write_b128 v20, v[108:111] offset:1024\n"
"v_mfma_f32_16x16x32_bf16 v[244:247], v[40:43], v[132:135], v[244:247]\n"
"v_add_f32 v90, v90, v94\n"
"s_branch .Lsk4_0\n"
".Lst4_0:\n"
"s_branch .Lbk4\n"
".Lsk4_0:\n"
"s_waitcnt lgkmcnt(5)\n"
"v_mfma_f32_16x16x32_bf16 v[240:243], v[44:47], v[136:139], v[240:243]\n"
"ds_read_b128 v[36:39], v19 offset:11264\n"
"v_add_f32 v91, v91, v95\n"
"v_mfma_f32_16x16x32_bf16 v[244:247], v[44:47], v[140:143], v[244:247]\n"
"v_cvt_pk_bf16_f32 v84, v80, v81\n"
"s_waitcnt lgkmcnt(5)\n"
"v_mfma_f32_16x16x32_bf16 v[240:243], v[24:27], v[144:147], v[240:243]\n"
"v_add_u32 v19, 24576, v22\n"
"ds_read_b128 v[40:43], v19 offset:2048\n"
"v_cvt_pk_bf16_f32 v85, v82, v83\n"
"v_mfma_f32_16x16x32_bf16 v[244:247], v[24:27], v[148:151], v[244:247]\n"
"v_cvt_pk_bf16_f32 v86, v88, v89\n"
"s_waitcnt lgkmcnt(4)\n"
"v_mfma_f32_16x16x32_bf16 v[240:243], v[28:31], v[152:155], v[240:243]\n"
"ds_read_b128 v[44:47], v19 offset:5120\n"
"v_cvt_pk_bf16_f32 v87, v90, v91\n"
"v_mfma_f32_16x16x32_bf16 v[244:247], v[28:31], v[156:159], v[244:247]\n"
"s_xor_b32 s45, s24, 4\n"
"s_mul_i32 s45, s45, 2048\n"
"s_add_u32 s45, s45, 147456\n"
"v_add_u32 v20, s45, v1\n"
"s_waitcnt lgkmcnt(3)\n"
"s_barrier\n"
"v_mfma_f32_16x16x32_bf16 v[240:243], v[32:35], v[160:163], v[240:243]\n"
"ds_read_b128 v[24:27], v19 offset:8192\n"
"s_setprio 3\n"
"ds_read_b128 v[100:103], v20\n"
"v_mfma_f32_16x16x32_bf16 v[244:247], v[32:35], v[164:167], v[244:247]\n"
"ds_read_b128 v[108:111], v20 offset:1024\n"
"s_waitcnt lgkmcnt(5)\n"
"v_mfma_f32_16x16x32_bf16 v[240:243], v[36:39], v[168:171], v[240:243]\n"
"ds_read_b128 v[28:31], v19 offset:11264\n"
"v_mfma_f32_16x16x32_bf16 v[244:247], v[36:39], v[172:175], v[244:247]\n"
"s_waitcnt lgkmcnt(5)\n"
"v_mfma_f32_16x16x32_bf16 v[240:243], v[40:43], v[176:179], v[240:243]\n"
"v_add_u32 v19, 49152, v22\n"
"ds_read_b128 v[32:35], v19 offset:2048\n"
"v_mfma_f32_16x16x32_bf16 v[244:247], v[40:43], v[180:183], v[244:247]\n"
"s_setprio 2\n"
"buffer_store_dwordx4 v[68:71], v21, s[48:51], s93 offen offset:512 sc1\n"
"s_waitcnt lgkmcnt(3)\n"
"v_add_f32 v96, v96, v100\n"
"v_mfma_f32_16x16x32_bf16 v[240:243], v[44:47], v[184:187], v[240:243]\n"
"ds_read_b128 v[36:39], v19 offset:5120\n"
"v_add_f32 v97, v97, v101\n"
"v_mfma_f32_16x16x32_bf16 v[244:247], v[44:47], v[188:191], v[244:247]\n"
"v_add_f32 v98, v98, v102\n"
"v_mfma_f32_16x16x32_bf16 v[240:243], v[24:27], v[192:195], v[240:243]\n"
"ds_read_b128 v[40:43], v19 offset:8192\n"
"v_add_f32 v99, v99, v103\n"
"v_mfma_f32_16x16x32_bf16 v[244:247], v[24:27], v[196:199], v[244:247]\n"
"buffer_store_dwordx4 v[84:87], v21, s[48:51], s93 offen offset:1024 sc1\n"
"s_waitcnt lgkmcnt(4)\n"
"v_add_f32 v104, v104, v108\n"
"s_waitcnt lgkmcnt(3)\n"
"v_mfma_f32_16x16x32_bf16 v[240:243], v[28:31], v[200:203], v[240:243]\n"
"ds_read_b128 v[44:47], v19 offset:11264\n"
"s_setprio 1\n"
"v_add_f32 v105, v105, v109\n"
"v_mfma_f32_16x16x32_bf16 v[244:247], v[28:31], v[204:207], v[244:247]\n"
"v_add_f32 v106, v106, v110\n"
"s_waitcnt lgkmcnt(3)\n"
"v_mfma_f32_16x16x32_bf16 v[240:243], v[32:35], v[208:211], v[240:243]\n"
"v_add_f32 v107, v107, v111\n"
"v_mfma_f32_16x16x32_bf16 v[244:247], v[32:35], v[212:215], v[244:247]\n"
"v_cvt_pk_bf16_f32 v100, v96, v97\n"
"s_waitcnt lgkmcnt(2)\n"
"v_mfma_f32_16x16x32_bf16 v[240:243], v[36:39], v[216:219], v[240:243]\n"
"v_cvt_pk_bf16_f32 v101, v98, v99\n"
"v_mfma_f32_16x16x32_bf16 v[244:247], v[36:39], v[220:223], v[244:247]\n"
"s_setprio 0\n"
"v_cvt_pk_bf16_f32 v102, v104, v105\n"
"s_waitcnt lgkmcnt(1)\n"
"v_mfma_f32_16x16x32_bf16 v[240:243], v[40:43], v[224:227], v[240:243]\n"
"v_cvt_pk_bf16_f32 v103, v106, v107\n"
"v_mfma_f32_16x16x32_bf16 v[244:247], v[40:43], v[228:231], v[244:247]\n"
"buffer_store_dwordx4 v[100:103], v21, s[48:51], s93 offen offset:1536 sc1\n"
"s_waitcnt lgkmcnt(0)\n"
"v_mfma_f32_16x16x32_bf16 v[240:243], v[44:47], v[232:235], v[240:243]\n"
"v_mfma_f32_16x16x32_bf16 v[244:247], v[44:47], v[236:239], v[244:247]\n"
"s_nop 7\n"
"ds_write_b128 v20, v[244:247]\n"
"s_mul_i32 s45, s24, 2048\n"
"s_add_u32 s45, s45, 147456\n"
"v_add_u32 v20, s45, v1\n"
"v_bfe_u32 v3, v0, 3, 1\n"
"v_mul_u32_u24 v21, 36864, v3\n"
"v_bfe_u32 v3, v0, 4, 1\n"
"v_lshl_add_u32 v21, v3, 8, v21\n"
"v_lshrrev_b32 v3, 5, v0\n"
"v_lshl_add_u32 v21, v3, 7, v21\n"
"v_and_b32 v3, 7, v0\n"
"v_lshl_add_u32 v21, v3, 4, v21\n"
"v_bfe_u32 v3, v0, 4, 1\n"
"v_mul_u32_u24 v3, 248, v3\n"
"v_sub_u32 v21, v21, v3\n"
"s_or_b32 s45, s36, 1\n"
"v_mov_b32 v16, s45\n"
"v_mov_b32 v17, s37\n"
"v_lshrrev_b32 v18, 4, v0\n"
"v_lshlrev_b32 v18, 28, v18\n"
"v_lshl_add_u32 v18, v0, 3, v18\n"
"s_waitcnt lgkmcnt(0)\n"
"s_barrier\n"
"ds_read_b128 v[244:247], v20\n"
"s_waitcnt lgkmcnt(0)\n"
"v_add_f32 v240, v240, v244\n"
"v_add_f32 v241, v241, v245\n"
"v_add_f32 v242, v242, v246\n"
"v_add_f32 v243, v243, v247\n"
"v_cvt_pk_bf16_f32 v12, v240, v241\n"
"v_cvt_pk_bf16_f32 v13, v242, v243\n"
"buffer_store_dwordx2 v[12:13], v21, s[48:51], s93 offen offset:2048 sc1\n"
"s_and_b32 s45, s27, 28\n"
"s_lshr_b32 s46, s24, 1\n"
"s_lshr_b32 s47, s32, 1\n"
"s_mul_i32 s3, s45, 8192\n"
"s_mul_i32 s42, s46, 8192\n"
"s_add_u32 s3, s3, s42\n"
"s_mul_i32 s42, s29, 1024\n"
"s_add_u32 s3, s3, s42\n"
"s_mul_i32 s42, s47, 512\n"
"s_add_u32 s3, s3, s42\n"
"s_mul_i32 s42, s30, 128\n"
"s_add_u32 s3, s3, s42\n"
"s_add_u32 s3, s3, 528384\n"
"v_and_b32 v19, 3, v0\n"
"v_lshlrev_b32 v19, 11, v19\n"
"v_add_u32 v19, s3, v19\n"
"v_lshrrev_b32 v3, 3, v0\n"
"v_and_b32 v4, 0x70, v1\n"
"v_lshl_add_u32 v4, v3, 1, v4\n"
"s_and_b32 s45, s24, 1\n"
"s_mul_i32 s84, s35, 36864\n"
"s_mul_i32 s42, s46, 2304\n"
"s_add_u32 s84, s84, s42\n"
"s_mul_i32 s42, s45, 1024\n"
"s_add_u32 s84, s84, s42\n"
"s_add_u32 s84, s84, 34607104\n"
"s_mul_i32 s85, s35, 36864\n"
"s_mul_i32 s42, s46, 2304\n"
"s_add_u32 s85, s85, s42\n"
"s_mul_i32 s42, s45, 128\n"
"s_add_u32 s85, s85, s42\n"
"s_add_u32 s85, s85, 34609152\n"
"v_bfe_u32 v3, v0, 3, 2\n"
"v_mul_u32_u24 v20, 9216, v3\n"
"v_lshrrev_b32 v3, 5, v0\n"
"v_lshl_add_u32 v20, v3, 3, v20\n"
"v_and_b32 v3, 0x70, v1\n"
"v_add_u32 v20, v20, v3\n"
"v_and_b32 v3, 8, v0\n"
"v_cmp_ne_u32 s[0:1], 0, v3\n"
"s_waitcnt vmcnt(6)\n"
"v_lshlrev_b32 v80, 16, v248\n"
"v_and_b32 v81, 0xffff0000, v248\n"
"v_lshlrev_b32 v82, 16, v249\n"
"v_and_b32 v83, 0xffff0000, v249\n"
"v_lshlrev_b32 v84, 16, v250\n"
"v_and_b32 v85, 0xffff0000, v250\n"
"v_lshlrev_b32 v86, 16, v251\n"
"v_and_b32 v87, 0xffff0000, v251\n"
"v_mov_b32 v64, 0\n"
"v_mov_b32 v65, 0\n"
"v_mov_b32 v66, 0\n"
"v_mov_b32 v67, 0\n"
"s_add_u32 s45, s79, 524288\n"
"s_waitcnt vmcnt(0)\n"
"buffer_store_dwordx2 v[16:17], v18, s[48:51], s45 offen sc1\n"
"v_lshrrev_b32 v3, 3, v0\n"
"v_mul_u32_u24 v2, 9216, v3\n"
"s_lshl_b32 s45, s26, 2\n"
"v_and_b32 v3, 2, v3\n"
"v_xor_b32 v3, v3, v0\n"
"v_xor_b32 v3, s45, v3\n"
"v_and_b32 v3, 7, v3\n"
"v_lshl_add_u32 v2, v3, 4, v2\n"
"s_add_u32 m0, s77, 98304\n"
"s_add_u32 s46, s4, 0\n"
"buffer_load_dwordx4 v2, s[52:55], s46 offen lds\n"
"s_add_u32 m0, s77, 102400\n"
"s_add_u32 s46, s4, 294912\n"
"buffer_load_dwordx4 v2, s[52:55], s46 offen lds\n"
"s_add_u32 m0, s77, 106496\n"
"s_add_u32 s46, s4, 589824\n"
"buffer_load_dwordx4 v2, s[52:55], s46 offen lds\n"
"s_add_u32 m0, s77, 122880\n"
"s_add_u32 s46, s4, 884736\n"
"buffer_load_dwordx4 v2, s[52:55], s46 offen lds\n"
"s_add_u32 m0, s77, 126976\n"
"s_add_u32 s46, s4, 1179648\n"
"buffer_load_dwordx4 v2, s[52:55], s46 offen lds\n"
"s_add_u32 m0, s77, 131072\n"
"s_add_u32 s46, s4, 1474560\n"
"buffer_load_dwordx4 v2, s[52:55], s46 offen lds\n"
"s_add_u32 m0, s77, 49152\n"
"s_add_u32 s46, s4, 1769472\n"
"buffer_load_dwordx4 v2, s[52:55], s46 offen lds\n"
"s_add_u32 m0, s77, 53248\n"
"s_add_u32 s46, s4, 2064384\n"
"buffer_load_dwordx4 v2, s[52:55], s46 offen lds\n"
"s_add_u32 m0, s77, 57344\n"
"s_add_u32 s46, s4, 2359296\n"
"buffer_load_dwordx4 v2, s[52:55], s46 offen lds\n"
"s_sleep 8\n"
"s_mov_b64 exec, 0xf\n"
"buffer_load_dwordx2 v[10:11], v19, s[48:51], 0 offen sc1\n"
"s_mov_b64 exec, -1\n"
"s_lshr_b32 s45, s35, 1\n"
"s_and_b32 s46, s35, 1\n"
"s_mul_i32 s86, s45, 18432\n"
"s_mul_i32 s42, s46, 128\n"
"s_add_u32 s86, s86, s42\n"
"s_mul_i32 s42, s24, 2048\n"
"s_add_u32 s86, s86, s42\n"
"s_add_u32 s86, s86, 1576960\n"
"s_mul_i32 s87, s45, 18432\n"
"s_mul_i32 s42, s46, 128\n"
"s_add_u32 s87, s87, s42\n"
"s_mul_i32 s42, s24, 256\n"
"s_add_u32 s87, s87, s42\n"
"s_add_u32 s87, s87, 1593344\n"
"s_mul_i32 s88, s35, 9216\n"
"s_mul_i32 s42, s24, 1024\n"
"s_add_u32 s88, s88, s42\n"
"s_add_u32 s88, s88, 3936256\n"
"s_mul_i32 s89, s35, 9216\n"
"s_mul_i32 s42, s24, 128\n"
"s_add_u32 s89, s89, s42\n"
"s_add_u32 s89, s89, 3944448\n"
"v_lshrrev_b32 v3, 3, v0\n"
"v_lshl_add_u32 v5, v3, 7, v1\n"
"v_and_b32 v3, 7, v0\n"
"v_lshlrev_b32 v7, 6, v3\n"
"v_add_u32 v7, 148480, v7\n"
"s_lshl_b32 s45, s24, 3\n"
"v_add_u32 v6, s45, v7\n"
"s_or_b32 s45, s36, 1\n"
"v_mov_b32 v16, s45\n"
"v_mov_b32 v17, s37\n"
"v_lshrrev_b32 v18, 4, v0\n"
"v_add_u32 v18, s24, v18\n"
"v_lshlrev_b32 v18, 28, v18\n"
"v_lshl_add_u32 v18, v0, 3, v18\n"
"s_or_b32 s40, s36, 1\n"
"s_waitcnt vmcnt(0)\n"
"s_mov_b32 s44, 0x40000\n"
"s_mov_b64 exec, 0xf\n"
".Lp2:\n"
"v_cmp_le_u64 vcc, s[40:41], v[10:11]\n"
"v_cmp_ge_u64 s[42:43], s[38:39], v[10:11]\n"
"s_nop 1\n"
"s_and_b64 s[42:43], s[42:43], vcc\n"
"s_cmp_eq_u64 s[42:43], exec\n"
"s_cbranch_scc1 .Ld2\n"
"s_sub_u32 s44, s44, 1\n"
"s_cbranch_scc1 .Ld2\n"
"s_sleep 1\n"
"buffer_load_dwordx2 v[10:11], v19, s[48:51], 0 offen sc1\n"
"s_waitcnt vmcnt(0)\n"
"s_branch .Lp2\n"
".Ld2:\n"
"s_mov_b64 exec, -1\n"
"buffer_load_dwordx4 v[48:51], v1, s[48:51], s84 offen sc1\n"
"s_add_u32 s84, s84, 9216\n"
"buffer_load_dwordx4 v[52:55], v1, s[48:51], s84 offen sc1\n"
"s_add_u32 s84, s84, 9216\n"
"buffer_load_dwordx4 v[56:59], v1, s[48:51], s84 offen sc1\n"
"s_add_u32 s84, s84, 9216\n"
"buffer_load_dwordx4 v[60:63], v1, s[48:51], s84 offen sc1\n"
"buffer_load_dwordx2 v[68:69], v20, s[48:51], s85 offen sc1\n"
"s_waitcnt vmcnt(4)\n"
"v_lshlrev_b32 v8, 16, v48\n"
"v_and_b32 v9, 0xffff0000, v48\n"
"v_pk_add_f32 v[80:81], v[80:81], v[8:9]\n"
"v_lshlrev_b32 v12, 16, v49\n"
"v_and_b32 v13, 0xffff0000, v49\n"
"v_pk_add_f32 v[82:83], v[82:83], v[12:13]\n"
"v_lshlrev_b32 v8, 16, v50\n"
"v_and_b32 v9, 0xffff0000, v50\n"
"v_pk_add_f32 v[84:85], v[84:85], v[8:9]\n"
"v_lshlrev_b32 v12, 16, v51\n"
"v_and_b32 v13, 0xffff0000, v51\n"
"v_pk_add_f32 v[86:87], v[86:87], v[12:13]\n"
"s_waitcnt vmcnt(3)\n"
"v_lshlrev_b32 v8, 16, v52\n"
"v_and_b32 v9, 0xffff0000, v52\n"
"v_pk_add_f32 v[80:81], v[80:81], v[8:9]\n"
"v_lshlrev_b32 v12, 16, v53\n"
"v_and_b32 v13, 0xffff0000, v53\n"
"v_pk_add_f32 v[82:83], v[82:83], v[12:13]\n"
"v_lshlrev_b32 v8, 16, v54\n"
"v_and_b32 v9, 0xffff0000, v54\n"
"v_pk_add_f32 v[84:85], v[84:85], v[8:9]\n"
"v_lshlrev_b32 v12, 16, v55\n"
"v_and_b32 v13, 0xffff0000, v55\n"
"v_pk_add_f32 v[86:87], v[86:87], v[12:13]\n"
"s_waitcnt vmcnt(2)\n"
"v_lshlrev_b32 v8, 16, v56\n"
"v_and_b32 v9, 0xffff0000, v56\n"
"v_pk_add_f32 v[80:81], v[80:81], v[8:9]\n"
"v_lshlrev_b32 v12, 16, v57\n"
"v_and_b32 v13, 0xffff0000, v57\n"
"v_pk_add_f32 v[82:83], v[82:83], v[12:13]\n"
"v_lshlrev_b32 v8, 16, v58\n"
"v_and_b32 v9, 0xffff0000, v58\n"
"v_pk_add_f32 v[84:85], v[84:85], v[8:9]\n"
"v_lshlrev_b32 v12, 16, v59\n"
"v_and_b32 v13, 0xffff0000, v59\n"
"v_pk_add_f32 v[86:87], v[86:87], v[12:13]\n"
"s_waitcnt vmcnt(1)\n"
"v_lshlrev_b32 v8, 16, v60\n"
"v_and_b32 v9, 0xffff0000, v60\n"
"v_pk_add_f32 v[80:81], v[80:81], v[8:9]\n"
"v_lshlrev_b32 v12, 16, v61\n"
"v_and_b32 v13, 0xffff0000, v61\n"
"v_pk_add_f32 v[82:83], v[82:83], v[12:13]\n"
"v_lshlrev_b32 v8, 16, v62\n"
"v_and_b32 v9, 0xffff0000, v62\n"
"v_pk_add_f32 v[84:85], v[84:85], v[8:9]\n"
"v_lshlrev_b32 v12, 16, v63\n"
"v_and_b32 v13, 0xffff0000, v63\n"
"v_pk_add_f32 v[86:87], v[86:87], v[12:13]\n"
"v_cvt_pk_bf16_f32 v96, v80, v81\n"
"v_cvt_pk_bf16_f32 v97, v82, v83\n"
"v_cvt_pk_bf16_f32 v98, v84, v85\n"
"v_cvt_pk_bf16_f32 v99, v86, v87\n"
"v_lshlrev_b32 v80, 16, v96\n"
"v_and_b32 v81, 0xffff0000, v96\n"
"v_lshlrev_b32 v82, 16, v97\n"
"v_and_b32 v83, 0xffff0000, v97\n"
"v_lshlrev_b32 v84, 16, v98\n"
"v_and_b32 v85, 0xffff0000, v98\n"
"v_lshlrev_b32 v86, 16, v99\n"
"v_and_b32 v87, 0xffff0000, v99\n"
"v_pk_add_f32 v[104:105], v[80:81], v[82:83]\n"
"v_pk_mul_f32 v[106:107], v[80:81], v[80:81]\n"
"v_pk_add_f32 v[108:109], v[84:85], v[86:87]\n"
"v_pk_fma_f32 v[106:107], v[82:83], v[82:83], v[106:107]\n"
"v_pk_fma_f32 v[106:107], v[84:85], v[84:85], v[106:107]\n"
"v_pk_fma_f32 v[106:107], v[86:87], v[86:87], v[106:107]\n"
"v_pk_add_f32 v[104:105], v[104:105], v[108:109]\n"
"s_waitcnt vmcnt(0)\n"
"v_lshlrev_b32 v64, 16, v68\n"
"v_and_b32 v65, 0xffff0000, v68\n"
"v_lshlrev_b32 v66, 16, v69\n"
"v_and_b32 v67, 0xffff0000, v69\n"
"s_nop 0\n"
"v_permlane16_swap_b32 v64, v66\n"
"v_permlane16_swap_b32 v65, v67\n"
"v_pk_add_f32 v[64:65], v[64:65], v[66:67]\n"
"v_cndmask_b32 v66, v65, v64, s[0:1]\n"
"v_cndmask_b32 v64, v64, v65, s[0:1]\n"
"s_nop 1\n"
"v_add_f32 v64, v66, v64 row_ror:8 row_mask:0xf bank_mask:0xf\n"
"v_add_f32 v252, v252, v64\n"
"v_cvt_pk_bf16_f32 v100, v252, v252\n"
"v_add_f32 v104, v104, v105\n"
"v_add_f32 v105, v106, v107\n"
"v_lshlrev_b32 v252, 16, v100\n"
"v_add_f32 v104, v104, v252\n"
"v_fmac_f32 v105, v252, v252\n"
"s_nop 0\n"
"v_add_f32 v104, v104, v104 row_ror:8 row_mask:0xf bank_mask:0xf\n"
"v_add_f32 v105, v105, v105 row_ror:8 row_mask:0xf bank_mask:0xf\n"
"v_mov_b32 v106, v104\n"
"v_mov_b32 v107, v105\n"
"s_nop 0\n"
"v_permlane16_swap_b32 v106, v104\n"
"v_permlane16_swap_b32 v107, v105\n"
"v_pk_add_f32 v[104:105], v[104:105], v[106:107]\n"
"v_mov_b32 v106, v104\n"
"v_mov_b32 v107, v105\n"
"s_nop 0\n"
"v_permlane32_swap_b32 v106, v104\n"
"v_permlane32_swap_b32 v107, v105\n"
"v_pk_add_f32 v[104:105], v[104:105], v[106:107]\n"
"ds_write_b64 v6, v[104:105]\n"
"s_waitcnt lgkmcnt(0)\n"
"s_barrier\n"
"ds_read_b128 v[48:51], v7\n"
"ds_read_b128 v[52:55], v7 offset:16\n"
"ds_read_b128 v[56:59], v7 offset:32\n"
"ds_read_b128 v[60:63], v7 offset:48\n"
"v_lshrrev_b32 v7, 3, v0\n"
"v_lshl_add_u32 v7, v7, 28, v1\n"
"s_waitcnt lgkmcnt(3)\n"
"v_pk_add_f32 v[48:49], v[48:49], v[50:51]\n"
"s_waitcnt lgkmcnt(2)\n"
"v_pk_add_f32 v[52:53], v[52:53], v[54:55]\n"
"s_waitcnt lgkmcnt(1)\n"
"v_pk_add_f32 v[56:57], v[56:57], v[58:59]\n"
"s_waitcnt lgkmcnt(0)\n"
"v_pk_add_f32 v[60:61], v[60:61], v[62:63]\n"
"v_pk_add_f32 v[48:49], v[48:49], v[52:53]\n"
"v_pk_add_f32 v[56:57], v[56:57], v[60:61]\n"
"v_pk_add_f32 v[48:49], v[48:49], v[56:57]\n"
"v_mul_f32 v104, 0x3ae38e39, v48\n"
"v_mul_f32 v106, 0x3ae38e39, v49\n"
"v_fma_f32 v106, -v104, v104, v106\n"
"v_max_f32 v106, 0, v106\n"
"v_add_f32 v106, 0x3727c5ac, v106\n"
"v_rsq_f32 v106, v106\n"
"s_nop 0\n"
"v_mul_f32 v106, 0x3fb8aa3b, v106\n"
"v_mul_f32 v104, -v104, v106\n"
"v_pk_fma_f32 v[80:81], v[80:81], v[106:107], v[104:105] op_sel_hi:[1,0,0]\n"
"v_pk_fma_f32 v[82:83], v[82:83], v[106:107], v[104:105] op_sel_hi:[1,0,0]\n"
"v_pk_fma_f32 v[84:85], v[84:85], v[106:107], v[104:105] op_sel_hi:[1,0,0]\n"
"v_pk_fma_f32 v[86:87], v[86:87], v[106:107], v[104:105] op_sel_hi:[1,0,0]\n"
"v_fma_f32 v252, v252, v106, v104\n"
"v_cvt_pk_bf16_f32 v64, v80, v81\n"
"v_cvt_pk_bf16_f32 v65, v82, v83\n"
"v_cvt_pk_bf16_f32 v66, v84, v85\n"
"v_cvt_pk_bf16_f32 v67, v86, v87\n"
"buffer_store_dwordx4 v[64:67], v5, s[48:51], s86 offen sc1\n"
"v_cvt_pk_bf16_f32 v68, v252, 0\n"
"s_nop 1\n"
"v_mov_b32 v69, v68 row_ror:8 row_mask:0xf bank_mask:0xf\n"
"v_lshl_or_b32 v68, v69, 16, v68\n"
"v_mov_b32 v69, v68\n"
"s_nop 1\n"
"v_permlane16_swap_b32 v68, v69\n"
"v_mov_b32 v70, v68\n"
"v_mov_b32 v71, v69\n"
"s_nop 0\n"
"v_permlane32_swap_b32 v68, v70\n"
"v_permlane32_swap_b32 v69, v71\n"
"buffer_store_dwordx4 v[68:71], v7, s[48:51], s87 offen sc1\n"
"s_add_u32 s45, s79, 786432\n"
"s_waitcnt vmcnt(0)\n"
"s_barrier\n"
"buffer_store_dwordx2 v[16:17], v18, s[48:51], s45 offen sc1\n"
"buffer_store_dwordx4 v[96:99], v1, s[48:51], s88 offen sc1\n"
"buffer_store_short v100, v4, s[48:51], s89 offen sc1\n"
"s_add_u32 m0, s77, 73728\n"
"s_add_u32 s46, s97, 2654208\n"
"buffer_load_dwordx4 v2, s[52:55], s46 offen lds\n"
"s_add_u32 m0, s77, 77824\n"
"s_add_u32 s46, s97, 2949120\n"
"buffer_load_dwordx4 v2, s[52:55], s46 offen lds\n"
"s_add_u32 m0, s77, 81920\n"
"s_add_u32 s46, s97, 3244032\n"
"buffer_load_dwordx4 v2, s[52:55], s46 offen lds\n"
"s_add_u32 m0, s77, 0\n"
"s_add_u32 s46, s97, 3538944\n"
"buffer_load_dwordx4 v2, s[52:55], s46 offen lds\n"
"s_add_u32 m0, s77, 4096\n"
"s_add_u32 s46, s97, 3833856\n"
"buffer_load_dwordx4 v2, s[52:55], s46 offen lds\n"
"s_add_u32 m0, s77, 8192\n"
"s_add_u32 s46, s97, 4128768\n"
"buffer_load_dwordx4 v2, s[52:55], s46 offen lds\n"
"s_add_u32 m0, s77, 24576\n"
"s_add_u32 s46, s97, 4423680\n"
"buffer_load_dwordx4 v2, s[52:55], s46 offen lds\n"
"s_add_u32 m0, s77, 28672\n"
"s_add_u32 s46, s97, 4718592\n"
"buffer_load_dwordx4 v2, s[52:55], s46 offen lds\n"
"s_add_u32 m0, s77, 32768\n"
"s_add_u32 s46, s97, 5013504\n"
"buffer_load_dwordx4 v2, s[52:55], s46 offen lds\n"
"s_add_u32 s80, s80, 25169920\n"
"s_and_b32 s45, s33, 3\n"
"s_mul_i32 s46, s45, 65536\n"
"s_mul_i32 s42, s25, 32768\n"
"s_add_u32 s46, s46, s42\n"
"s_mul_i32 s42, s26, 2048\n"
"s_add_u32 s46, s46, s42\n"
"s_mul_i32 s42, s29, 1024\n"
"s_add_u32 s46, s46, s42\n"
"s_add_u32 s46, s46, 790528\n"
"v_and_b32 v19, 3, v0\n"
"v_lshlrev_b32 v19, 13, v19\n"
"v_add_u32 v19, s46, v19\n"
"s_sleep 8\n"
"s_mov_b64 exec, 0xf\n"
"buffer_load_dwordx2 v[10:11], v19, s[48:51], 0 offen sc1\n"
"s_mov_b64 exec, -1\n"
"v_lshrrev_b32 v3, 4, v0\n"
"v_lshlrev_b32 v22, 10, v3\n"
"v_bfe_u32 v3, v0, 3, 2\n"
"v_lshl_add_u32 v22, v3, 5, v22\n"
"v_bfe_u32 v3, v0, 2, 2\n"
"v_lshl_add_u32 v22, v3, 7, v22\n"
"v_and_b32 v3, 3, v0\n"
"v_lshl_add_u32 v22, v3, 3, v22\n"
"v_min_u32 v8, 35, v0\n"
"v_lshlrev_b32 v8, 7, v8\n"
"s_and_b32 s45, s27, 7\n"
"s_mul_i32 s47, s45, 4608\n"
"s_add_u32 s47, s47, s8\n"
"s_waitcnt vmcnt(1)\n"
"s_barrier\n"
"v_add_u32 v4, 98304, v22\n"
"ds_read_b64_tr_b16 v[24:25], v4\n"
"ds_read_b64_tr_b16 v[26:27], v4 offset:512\n"
"ds_read_b64_tr_b16 v[28:29], v4 offset:12288\n"
"ds_read_b64_tr_b16 v[30:31], v4 offset:12800\n"
"v_xor_b32 v5, 32, v4\n"
"ds_read_b64_tr_b16 v[32:33], v5\n"
"ds_read_b64_tr_b16 v[34:35], v5 offset:512\n"
"ds_read_b64_tr_b16 v[36:37], v5 offset:12288\n"
"ds_read_b64_tr_b16 v[38:39], v5 offset:12800\n"
"s_or_b32 s40, s36, 1\n"
"s_waitcnt vmcnt(0)\n"
"s_mov_b32 s44, 0x40000\n"
"s_mov_b64 exec, 0xf\n"
".Lp3:\n"
"v_cmp_le_u64 vcc, s[40:41], v[10:11]\n"
"v_cmp_ge_u64 s[42:43], s[38:39], v[10:11]\n"
"s_nop 1\n"
"s_and_b64 s[42:43], s[42:43], vcc\n"
"s_cmp_eq_u64 s[42:43], exec\n"
"s_cbranch_scc1 .Ld3\n"
"s_sub_u32 s44, s44, 1\n"
"s_cbranch_scc1 .Ld3\n"
"s_sleep 1\n"
"buffer_load_dwordx2 v[10:11], v19, s[48:51], 0 offen sc1\n"
"s_waitcnt vmcnt(0)\n"
"s_branch .Lp3\n"
".Ld3:\n"
"s_mov_b64 exec, -1\n"
".Lskip3:\n"
"s_add_u32 s46, s8, 0\n"
"buffer_load_dwordx4 v[112:115], v1, s[48:51], s46 offen sc1\n"
"s_add_u32 s46, s8, 18432\n"
"buffer_load_dwordx4 v[116:119], v1, s[48:51], s46 offen sc1\n"
"s_add_u32 s46, s8, 1024\n"
"buffer_load_dwordx4 v[120:123], v1, s[48:51], s46 offen sc1\n"
"s_add_u32 s46, s8, 19456\n"
"buffer_load_dwordx4 v[124:127], v1, s[48:51], s46 offen sc1\n"
"s_add_u32 s46, s8, 2048\n"
"buffer_load_dwordx4 v[128:131], v1, s[48:51], s46 offen sc1\n"
"s_add_u32 s46, s8, 20480\n"
"buffer_load_dwordx4 v[132:135], v1, s[48:51], s46 offen sc1\n"
"s_waitcnt vmcnt(5) lgkmcnt(6)\n"
"v_mfma_f32_16x16x32_bf16 v[48:51], v[24:27], v[112:115], 0\n"
"ds_read_b64_tr_b16 v[40:41], v4 offset:4096\n"
"s_setprio 3\n"
"s_waitcnt vmcnt(4)\n"
"v_mfma_f32_16x16x32_bf16 v[52:55], v[24:27], v[116:119], 0\n"
"ds_read_b64_tr_b16 v[42:43], v4 offset:4608\n"
"s_add_u32 s46, s8, 3072\n"
"buffer_load_dwordx4 v[136:139], v1, s[48:51], s46 offen sc1\n"
"s_waitcnt lgkmcnt(6)\n"
"v_mfma_f32_16x16x32_bf16 v[60:63], v[28:31], v[116:119], 0\n"
"ds_read_b64_tr_b16 v[44:45], v4 offset:16384\n"
"v_mfma_f32_16x16x32_bf16 v[56:59], v[28:31], v[112:115], 0\n"
"ds_read_b64_tr_b16 v[46:47], v4 offset:16896\n"
"s_waitcnt lgkmcnt(6)\n"
"v_mfma_f32_16x16x32_bf16 v[64:67], v[32:35], v[112:115], 0\n"
"ds_read_b64_tr_b16 v[24:25], v5 offset:4096\n"
"v_mfma_f32_16x16x32_bf16 v[68:71], v[32:35], v[116:119], 0\n"
"ds_read_b64_tr_b16 v[26:27], v5 offset:4608\n"
"s_add_u32 s46, s8, 21504\n"
"buffer_load_dwordx4 v[140:143], v1, s[48:51], s46 offen sc1\n"
"s_waitcnt lgkmcnt(6)\n"
"v_mfma_f32_16x16x32_bf16 v[76:79], v[36:39], v[116:119], 0\n"
"ds_read_b64_tr_b16 v[28:29], v5 offset:16384\n"
"v_mfma_f32_16x16x32_bf16 v[72:75], v[36:39], v[112:115], 0\n"
"ds_read_b64_tr_b16 v[30:31], v5 offset:16896\n"
"s_waitcnt vmcnt(5) lgkmcnt(6)\n"
"v_mfma_f32_16x16x32_bf16 v[48:51], v[40:43], v[120:123], v[48:51]\n"
"ds_read_b64_tr_b16 v[32:33], v4 offset:8192\n"
"s_waitcnt vmcnt(4)\n"
"v_mfma_f32_16x16x32_bf16 v[52:55], v[40:43], v[124:127], v[52:55]\n"
"ds_read_b64_tr_b16 v[34:35], v4 offset:8704\n"
"s_add_u32 s46, s8, 4096\n"
"buffer_load_dwordx4 v[144:147], v1, s[48:51], s46 offen sc1\n"
"s_waitcnt lgkmcnt(6)\n"
"v_mfma_f32_16x16x32_bf16 v[60:63], v[44:47], v[124:127], v[60:63]\n"
"ds_read_b64_tr_b16 v[36:37], v4 offset:20480\n"
"v_mfma_f32_16x16x32_bf16 v[56:59], v[44:47], v[120:123], v[56:59]\n"
"ds_read_b64_tr_b16 v[38:39], v4 offset:20992\n"
"s_waitcnt lgkmcnt(6)\n"
"v_mfma_f32_16x16x32_bf16 v[64:67], v[24:27], v[120:123], v[64:67]\n"
"ds_read_b64_tr_b16 v[40:41], v5 offset:8192\n"
"v_mfma_f32_16x16x32_bf16 v[68:71], v[24:27], v[124:127], v[68:71]\n"
"ds_read_b64_tr_b16 v[42:43], v5 offset:8704\n"
"s_add_u32 s46, s8, 22528\n"
"buffer_load_dwordx4 v[148:151], v1, s[48:51], s46 offen sc1\n"
"s_waitcnt lgkmcnt(6)\n"
"v_mfma_f32_16x16x32_bf16 v[76:79], v[28:31], v[124:127], v[76:79]\n"
"ds_read_b64_tr_b16 v[44:45], v5 offset:20480\n"
"v_mfma_f32_16x16x32_bf16 v[72:75], v[28:31], v[120:123], v[72:75]\n"
"ds_read_b64_tr_b16 v[46:47], v5 offset:20992\n"
"s_waitcnt vmcnt(5) lgkmcnt(6)\n"
"v_mfma_f32_16x16x32_bf16 v[48:51], v[32:35], v[128:131], v[48:51]\n"
"ds_read_b64_tr_b16 v[24:25], v4 offset:24576\n"
"s_waitcnt vmcnt(4)\n"
"v_mfma_f32_16x16x32_bf16 v[52:55], v[32:35], v[132:135], v[52:55]\n"
"ds_read_b64_tr_b16 v[26:27], v4 offset:25088\n"
"s_add_u32 s46, s8, 5120\n"
"buffer_load_dwordx4 v[152:155], v1, s[48:51], s46 offen sc1\n"
"s_waitcnt lgkmcnt(6)\n"
"v_mfma_f32_16x16x32_bf16 v[60:63], v[36:39], v[132:135], v[60:63]\n"
"ds_read_b64_tr_b16 v[28:29], v4 offset:36864\n"
"s_setprio 2\n"
"v_mfma_f32_16x16x32_bf16 v[56:59], v[36:39], v[128:131], v[56:59]\n"
"ds_read_b64_tr_b16 v[30:31], v4 offset:37376\n"
"s_waitcnt lgkmcnt(6)\n"
"v_mfma_f32_16x16x32_bf16 v[64:67], v[40:43], v[128:131], v[64:67]\n"
"ds_read_b64_tr_b16 v[32:33], v5 offset:24576\n"
"v_mfma_f32_16x16x32_bf16 v[68:71], v[40:43], v[132:135], v[68:71]\n"
"ds_read_b64_tr_b16 v[34:35], v5 offset:25088\n"
"s_add_u32 s46, s8, 23552\n"
"buffer_load_dwordx4 v[156:159], v1, s[48:51], s46 offen sc1\n"
"s_waitcnt lgkmcnt(6)\n"
"v_mfma_f32_16x16x32_bf16 v[76:79], v[44:47], v[132:135], v[76:79]\n"
"ds_read_b64_tr_b16 v[36:37], v5 offset:36864\n"
"v_mfma_f32_16x16x32_bf16 v[72:75], v[44:47], v[128:131], v[72:75]\n"
"ds_read_b64_tr_b16 v[38:39], v5 offset:37376\n"
"s_waitcnt vmcnt(5) lgkmcnt(6)\n"
"v_mfma_f32_16x16x32_bf16 v[48:51], v[24:27], v[136:139], v[48:51]\n"
"ds_read_b64_tr_b16 v[40:41], v4 offset:28672\n"
"s_waitcnt vmcnt(4)\n"
"v_mfma_f32_16x16x32_bf16 v[52:55], v[24:27], v[140:143], v[52:55]\n"
"ds_read_b64_tr_b16 v[42:43], v4 offset:29184\n"
"s_add_u32 s46, s8, 6144\n"
"buffer_load_dwordx4 v[160:163], v1, s[48:51], s46 offen sc1\n"
"s_waitcnt lgkmcnt(6)\n"
"v_mfma_f32_16x16x32_bf16 v[60:63], v[28:31], v[140:143], v[60:63]\n"
"ds_read_b64_tr_b16 v[44:45], v4 offset:40960\n"
"v_mfma_f32_16x16x32_bf16 v[56:59], v[28:31], v[136:139], v[56:59]\n"
"ds_read_b64_tr_b16 v[46:47], v4 offset:41472\n"
"s_waitcnt lgkmcnt(6)\n"
"v_mfma_f32_16x16x32_bf16 v[64:67], v[32:35], v[136:139], v[64:67]\n"
"ds_read_b64_tr_b16 v[24:25], v5 offset:28672\n"
"v_mfma_f32_16x16x32_bf16 v[68:71], v[32:35], v[140:143], v[68:71]\n"
"ds_read_b64_tr_b16 v[26:27], v5 offset:29184\n"
"s_add_u32 s46, s8, 24576\n"
"buffer_load_dwordx4 v[164:167], v1, s[48:51], s46 offen sc1\n"
"s_waitcnt lgkmcnt(6)\n"
"v_mfma_f32_16x16x32_bf16 v[76:79], v[36:39], v[140:143], v[76:79]\n"
"ds_read_b64_tr_b16 v[28:29], v5 offset:40960\n"
"v_mfma_f32_16x16x32_bf16 v[72:75], v[36:39], v[136:139], v[72:75]\n"
"ds_read_b64_tr_b16 v[30:31], v5 offset:41472\n"
"s_branch .Lsk5_0\n"
".Lst5_0:\n"
"s_branch .Lbk5\n"
".Lsk5_0:\n"
"s_waitcnt vmcnt(5) lgkmcnt(6)\n"
"v_mfma_f32_16x16x32_bf16 v[48:51], v[40:43], v[144:147], v[48:51]\n"
"ds_read_b64_tr_b16 v[32:33], v4 offset:32768\n"
"s_waitcnt vmcnt(4)\n"
"v_mfma_f32_16x16x32_bf16 v[52:55], v[40:43], v[148:151], v[52:55]\n"
"ds_read_b64_tr_b16 v[34:35], v4 offset:33280\n"
"s_add_u32 s46, s8, 7168\n"
"buffer_load_dwordx4 v[168:171], v1, s[48:51], s46 offen sc1\n"
"s_waitcnt lgkmcnt(6)\n"
"v_mfma_f32_16x16x32_bf16 v[60:63], v[44:47], v[148:151], v[60:63]\n"
"ds_read_b64_tr_b16 v[36:37], v4 offset:45056\n"
"v_mfma_f32_16x16x32_bf16 v[56:59], v[44:47], v[144:147], v[56:59]\n"
"ds_read_b64_tr_b16 v[38:39], v4 offset:45568\n"
"s_waitcnt lgkmcnt(6)\n"
"v_mfma_f32_16x16x32_bf16 v[64:67], v[24:27], v[144:147], v[64:67]\n"
"ds_read_b64_tr_b16 v[40:41], v5 offset:32768\n"
"s_setprio 1\n"
"v_mfma_f32_16x16x32_bf16 v[68:71], v[24:27], v[148:151], v[68:71]\n"
"ds_read_b64_tr_b16 v[42:43], v5 offset:33280\n"
"s_add_u32 s46, s8, 25600\n"
"buffer_load_dwordx4 v[172:175], v1, s[48:51], s46 offen sc1\n"
"s_waitcnt lgkmcnt(6)\n"
"v_mfma_f32_16x16x32_bf16 v[76:79], v[28:31], v[148:151], v[76:79]\n"
"ds_read_b64_tr_b16 v[44:45], v5 offset:45056\n"
"v_mfma_f32_16x16x32_bf16 v[72:75], v[28:31], v[144:147], v[72:75]\n"
"ds_read_b64_tr_b16 v[46:47], v5 offset:45568\n"
"s_waitcnt vmcnt(5) lgkmcnt(6)\n"
"v_mfma_f32_16x16x32_bf16 v[48:51], v[32:35], v[152:155], v[48:51]\n"
"v_add_u32 v4, 49152, v22\n"
"ds_read_b64_tr_b16 v[24:25], v4\n"
"s_waitcnt vmcnt(4)\n"
"v_mfma_f32_16x16x32_bf16 v[52:55], v[32:35], v[156:159], v[52:55]\n"
"ds_read_b64_tr_b16 v[26:27], v4 offset:512\n"
"s_add_u32 s46, s8, 8192\n"
"buffer_load_dwordx4 v[176:179], v1, s[48:51], s46 offen sc1\n"
"s_waitcnt lgkmcnt(6)\n"
"v_mfma_f32_16x16x32_bf16 v[60:63], v[36:39], v[156:159], v[60:63]\n"
"ds_read_b64_tr_b16 v[28:29], v4 offset:12288\n"
"v_mfma_f32_16x16x32_bf16 v[56:59], v[36:39], v[152:155], v[56:59]\n"
"ds_read_b64_tr_b16 v[30:31], v4 offset:12800\n"
"s_waitcnt lgkmcnt(6)\n"
"v_mfma_f32_16x16x32_bf16 v[64:67], v[40:43], v[152:155], v[64:67]\n"
"v_xor_b32 v5, 32, v4\n"
"ds_read_b64_tr_b16 v[32:33], v5\n"
"v_mfma_f32_16x16x32_bf16 v[68:71], v[40:43], v[156:159], v[68:71]\n"
"ds_read_b64_tr_b16 v[34:35], v5 offset:512\n"
"s_add_u32 s46, s8, 26624\n"
"buffer_load_dwordx4 v[180:183], v1, s[48:51], s46 offen sc1\n"
"s_waitcnt lgkmcnt(6)\n"
"v_mfma_f32_16x16x32_bf16 v[76:79], v[44:47], v[156:159], v[76:79]\n"
"ds_read_b64_tr_b16 v[36:37], v5 offset:12288\n"
"v_mfma_f32_16x16x32_bf16 v[72:75], v[44:47], v[152:155], v[72:75]\n"
"ds_read_b64_tr_b16 v[38:39], v5 offset:12800\n"
"s_waitcnt vmcnt(5) lgkmcnt(6)\n"
"v_mfma_f32_16x16x32_bf16 v[48:51], v[24:27], v[160:163], v[48:51]\n"
"ds_read_b64_tr_b16 v[40:41], v4 offset:4096\n"
"s_waitcnt vmcnt(4)\n"
"v_mfma_f32_16x16x32_bf16 v[52:55], v[24:27], v[164:167], v[52:55]\n"
"ds_read_b64_tr_b16 v[42:43], v4 offset:4608\n"
"s_add_u32 s46, s96, 9216\n"
"buffer_load_dwordx4 v[184:187], v1, s[48:51], s46 offen sc1\n"
"s_waitcnt lgkmcnt(6)\n"
"v_mfma_f32_16x16x32_bf16 v[60:63], v[28:31], v[164:167], v[60:63]\n"
"ds_read_b64_tr_b16 v[44:45], v4 offset:16384\n"
"v_mfma_f32_16x16x32_bf16 v[56:59], v[28:31], v[160:163], v[56:59]\n"
"ds_read_b64_tr_b16 v[46:47], v4 offset:16896\n"
"s_waitcnt lgkmcnt(6)\n"
"v_mfma_f32_16x16x32_bf16 v[64:67], v[32:35], v[160:163], v[64:67]\n"
"ds_read_b64_tr_b16 v[24:25], v5 offset:4096\n"
"v_mfma_f32_16x16x32_bf16 v[68:71], v[32:35], v[164:167], v[68:71]\n"
"ds_read_b64_tr_b16 v[26:27], v5 offset:4608\n"
"s_add_u32 s46, s96, 27648\n"
"buffer_load_dwordx4 v[188:191], v1, s[48:51], s46 offen sc1\n"
"s_waitcnt lgkmcnt(6)\n"
"v_mfma_f32_16x16x32_bf16 v[76:79], v[36:39], v[164:167], v[76:79]\n"
"ds_read_b64_tr_b16 v[28:29], v5 offset:16384\n"
"s_setprio 0\n"
"v_mfma_f32_16x16x32_bf16 v[72:75], v[36:39], v[160:163], v[72:75]\n"
"ds_read_b64_tr_b16 v[30:31], v5 offset:16896\n"
"s_waitcnt vmcnt(5) lgkmcnt(6)\n"
"v_mfma_f32_16x16x32_bf16 v[48:51], v[40:43], v[168:171], v[48:51]\n"
"ds_read_b64_tr_b16 v[32:33], v4 offset:8192\n"
"s_waitcnt vmcnt(4)\n"
"v_mfma_f32_16x16x32_bf16 v[52:55], v[40:43], v[172:175], v[52:55]\n"
"ds_read_b64_tr_b16 v[34:35], v4 offset:8704\n"
"s_add_u32 s46, s96, 10240\n"
"buffer_load_dwordx4 v[192:195], v1, s[48:51], s46 offen sc1\n"
"s_waitcnt lgkmcnt(6)\n"
"v_mfma_f32_16x16x32_bf16 v[60:63], v[44:47], v[172:175], v[60:63]\n"
"ds_read_b64_tr_b16 v[36:37], v4 offset:20480\n"
"v_mfma_f32_16x16x32_bf16 v[56:59], v[44:47], v[168:171], v[56:59]\n"
"ds_read_b64_tr_b16 v[38:39], v4 offset:20992\n"
"s_waitcnt lgkmcnt(6)\n"
"v_mfma_f32_16x16x32_bf16 v[64:67], v[24:27], v[168:171], v[64:67]\n"
"ds_read_b64_tr_b16 v[40:41], v5 offset:8192\n"
"v_mfma_f32_16x16x32_bf16 v[68:71], v[24:27], v[172:175], v[68:71]\n"
"ds_read_b64_tr_b16 v[42:43], v5 offset:8704\n"
"s_add_u32 s46, s96, 28672\n"
"buffer_load_dwordx4 v[196:199], v1, s[48:51], s46 offen sc1\n"
"s_waitcnt lgkmcnt(6)\n"
"v_mfma_f32_16x16x32_bf16 v[76:79], v[28:31], v[172:175], v[76:79]\n"
"ds_read_b64_tr_b16 v[44:45], v5 offset:20480\n"
"v_mfma_f32_16x16x32_bf16 v[72:75], v[28:31], v[168:171], v[72:75]\n"
"ds_read_b64_tr_b16 v[46:47], v5 offset:20992\n"
"s_waitcnt vmcnt(5) lgkmcnt(6)\n"
"v_mfma_f32_16x16x32_bf16 v[48:51], v[32:35], v[176:179], v[48:51]\n"
"ds_read_b64_tr_b16 v[24:25], v4 offset:24576\n"
"s_waitcnt vmcnt(4)\n"
"v_mfma_f32_16x16x32_bf16 v[52:55], v[32:35], v[180:183], v[52:55]\n"
"ds_read_b64_tr_b16 v[26:27], v4 offset:25088\n"
"s_add_u32 s46, s96, 11264\n"
"buffer_load_dwordx4 v[200:203], v1, s[48:51], s46 offen sc1\n"
"s_waitcnt lgkmcnt(6)\n"
"v_mfma_f32_16x16x32_bf16 v[60:63], v[36:39], v[180:183], v[60:63]\n"
"ds_read_b64_tr_b16 v[28:29], v4 offset:36864\n"
"v_mfma_f32_16x16x32_bf16 v[56:59], v[36:39], v[176:179], v[56:59]\n"
"ds_read_b64_tr_b16 v[30:31], v4 offset:37376\n"
"s_waitcnt lgkmcnt(6)\n"
"v_mfma_f32_16x16x32_bf16 v[64:67], v[40:43], v[176:179], v[64:67]\n"
"ds_read_b64_tr_b16 v[32:33], v5 offset:24576\n"
"v_mfma_f32_16x16x32_bf16 v[68:71], v[40:43], v[180:183], v[68:71]\n"
"ds_read_b64_tr_b16 v[34:35], v5 offset:25088\n"
"s_add_u32 s46, s96, 29696\n"
"buffer_load_dwordx4 v[204:207], v1, s[48:51], s46 offen sc1\n"
"s_waitcnt lgkmcnt(6)\n"
"v_mfma_f32_16x16x32_bf16 v[76:79], v[44:47], v[180:183], v[76:79]\n"
"ds_read_b64_tr_b16 v[36:37], v5 offset:36864\n"
"v_mfma_f32_16x16x32_bf16 v[72:75], v[44:47], v[176:179], v[72:75]\n"
"ds_read_b64_tr_b16 v[38:39], v5 offset:37376\n"
"s_barrier\n"
"s_waitcnt vmcnt(5) lgkmcnt(6)\n"
"v_mfma_f32_16x16x32_bf16 v[48:51], v[24:27], v[184:187], v[48:51]\n"
"ds_read_b64_tr_b16 v[40:41], v4 offset:28672\n"
"s_setprio 3\n"
"s_waitcnt vmcnt(4)\n"
"v_mfma_f32_16x16x32_bf16 v[52:55], v[24:27], v[188:191], v[52:55]\n"
"ds_read_b64_tr_b16 v[42:43], v4 offset:29184\n"
"s_add_u32 s46, s96, 12288\n"
"buffer_load_dwordx4 v[208:211], v1, s[48:51], s46 offen sc1\n"
"s_waitcnt lgkmcnt(6)\n"
"v_mfma_f32_16x16x32_bf16 v[60:63], v[28:31], v[188:191], v[60:63]\n"
"ds_read_b64_tr_b16 v[44:45], v4 offset:40960\n"
"v_mfma_f32_16x16x32_bf16 v[56:59], v[28:31], v[184:187], v[56:59]\n"
"ds_read_b64_tr_b16 v[46:47], v4 offset:41472\n"
"s_waitcnt lgkmcnt(6)\n"
"v_mfma_f32_16x16x32_bf16 v[64:67], v[32:35], v[184:187], v[64:67]\n"
"ds_read_b64_tr_b16 v[24:25], v5 offset:28672\n"
"v_mfma_f32_16x16x32_bf16 v[68:71], v[32:35], v[188:191], v[68:71]\n"
"ds_read_b64_tr_b16 v[26:27], v5 offset:29184\n"
"s_add_u32 s46, s96, 30720\n"
"buffer_load_dwordx4 v[212:215], v1, s[48:51], s46 offen sc1\n"
"s_waitcnt lgkmcnt(6)\n"
"v_mfma_f32_16x16x32_bf16 v[76:79], v[36:39], v[188:191], v[76:79]\n"
"ds_read_b64_tr_b16 v[28:29], v5 offset:40960\n"
"v_mfma_f32_16x16x32_bf16 v[72:75], v[36:39], v[184:187], v[72:75]\n"
"ds_read_b64_tr_b16 v[30:31], v5 offset:41472\n"
"s_waitcnt vmcnt(5) lgkmcnt(6)\n"
"v_mfma_f32_16x16x32_bf16 v[48:51], v[40:43], v[192:195], v[48:51]\n"
"ds_read_b64_tr_b16 v[32:33], v4 offset:32768\n"
"s_waitcnt vmcnt(4)\n"
"v_mfma_f32_16x16x32_bf16 v[52:55], v[40:43], v[196:199], v[52:55]\n"
"ds_read_b64_tr_b16 v[34:35], v4 offset:33280\n"
"s_add_u32 s46, s96, 13312\n"
"buffer_load_dwordx4 v[216:219], v1, s[48:51], s46 offen sc1\n"
"s_waitcnt lgkmcnt(6)\n"
"v_mfma_f32_16x16x32_bf16 v[60:63], v[44:47], v[196:199], v[60:63]\n"
"ds_read_b64_tr_b16 v[36:37], v4 offset:45056\n"
"v_mfma_f32_16x16x32_bf16 v[56:59], v[44:47], v[192:195], v[56:59]\n"
"ds_read_b64_tr_b16 v[38:39], v4 offset:45568\n"
"s_waitcnt lgkmcnt(6)\n"
"v_mfma_f32_16x16x32_bf16 v[64:67], v[24:27], v[192:195], v[64:67]\n"
"ds_read_b64_tr_b16 v[40:41], v5 offset:32768\n"
"v_mfma_f32_16x16x32_bf16 v[68:71], v[24:27], v[196:199], v[68:71]\n"
"ds_read_b64_tr_b16 v[42:43], v5 offset:33280\n"
"s_add_u32 s46, s96, 31744\n"
"buffer_load_dwordx4 v[220:223], v1, s[48:51], s46 offen sc1\n"
"s_waitcnt lgkmcnt(6)\n"
"v_mfma_f32_16x16x32_bf16 v[76:79], v[28:31], v[196:199], v[76:79]\n"
"ds_read_b64_tr_b16 v[44:45], v5 offset:45056\n"
"v_mfma_f32_16x16x32_bf16 v[72:75], v[28:31], v[192:195], v[72:75]\n"
"ds_read_b64_tr_b16 v[46:47], v5 offset:45568\n"
"s_waitcnt vmcnt(5) lgkmcnt(6)\n"
"v_mfma_f32_16x16x32_bf16 v[48:51], v[32:35], v[200:203], v[48:51]\n"
"v_add_u32 v4, 0, v22\n"
"ds_read_b64_tr_b16 v[24:25], v4\n"
"s_setprio 2\n"
"s_waitcnt vmcnt(4)\n"
"v_mfma_f32_16x16x32_bf16 v[52:55], v[32:35], v[204:207], v[52:55]\n"
"ds_read_b64_tr_b16 v[26:27], v4 offset:512\n"
"s_add_u32 s46, s96, 14336\n"
"buffer_load_dwordx4 v[224:227], v1, s[48:51], s46 offen sc1\n"
"s_waitcnt lgkmcnt(6)\n"
"v_mfma_f32_16x16x32_bf16 v[60:63], v[36:39], v[204:207], v[60:63]\n"
"ds_read_b64_tr_b16 v[28:29], v4 offset:12288\n"
"v_mfma_f32_16x16x32_bf16 v[56:59], v[36:39], v[200:203], v[56:59]\n"
"ds_read_b64_tr_b16 v[30:31], v4 offset:12800\n"
"s_waitcnt lgkmcnt(6)\n"
"v_mfma_f32_16x16x32_bf16 v[64:67], v[40:43], v[200:203], v[64:67]\n"
"v_xor_b32 v5, 32, v4\n"
"ds_read_b64_tr_b16 v[32:33], v5\n"
"v_mfma_f32_16x16x32_bf16 v[68:71], v[40:43], v[204:207], v[68:71]\n"
"ds_read_b64_tr_b16 v[34:35], v5 offset:512\n"
"s_add_u32 s46, s96, 32768\n"
"buffer_load_dwordx4 v[228:231], v1, s[48:51], s46 offen sc1\n"
"s_waitcnt lgkmcnt(6)\n"
"v_mfma_f32_16x16x32_bf16 v[76:79], v[44:47], v[204:207], v[76:79]\n"
"ds_read_b64_tr_b16 v[36:37], v5 offset:12288\n"
"v_mfma_f32_16x16x32_bf16 v[72:75], v[44:47], v[200:203], v[72:75]\n"
"ds_read_b64_tr_b16 v[38:39], v5 offset:12800\n"
"s_waitcnt vmcnt(5) lgkmcnt(6)\n"
"v_mfma_f32_16x16x32_bf16 v[48:51], v[24:27], v[208:211], v[48:51]\n"
"ds_read_b64_tr_b16 v[40:41], v4 offset:4096\n"
"s_waitcnt vmcnt(4)\n"
"v_mfma_f32_16x16x32_bf16 v[52:55], v[24:27], v[212:215], v[52:55]\n"
"ds_read_b64_tr_b16 v[42:43], v4 offset:4608\n"
"s_add_u32 s46, s96, 15360\n"
"buffer_load_dwordx4 v[232:235], v1, s[48:51], s46 offen sc1\n"
"s_waitcnt lgkmcnt(6)\n"
"v_mfma_f32_16x16x32_bf16 v[60:63], v[28:31], v[212:215], v[60:63]\n"
"ds_read_b64_tr_b16 v[44:45], v4 offset:16384\n"
"v_mfma_f32_16x16x32_bf16 v[56:59], v[28:31], v[208:211], v[56:59]\n"
"ds_read_b64_tr_b16 v[46:47], v4 offset:16896\n"
"s_waitcnt lgkmcnt(6)\n"
"v_mfma_f32_16x16x32_bf16 v[64:67], v[32:35], v[208:211], v[64:67]\n"
"ds_read_b64_tr_b16 v[24:25], v5 offset:4096\n"
"v_mfma_f32_16x16x32_bf16 v[68:71], v[32:35], v[212:215], v[68:71]\n"
"ds_read_b64_tr_b16 v[26:27], v5 offset:4608\n"
"s_add_u32 s46, s96, 33792\n"
"buffer_load_dwordx4 v[236:239], v1, s[48:51], s46 offen sc1\n"
"s_waitcnt lgkmcnt(6)\n"
"v_mfma_f32_16x16x32_bf16 v[76:79], v[36:39], v[212:215], v[76:79]\n"
"ds_read_b64_tr_b16 v[28:29], v5 offset:16384\n"
"v_mfma_f32_16x16x32_bf16 v[72:75], v[36:39], v[208:211], v[72:75]\n"
"ds_read_b64_tr_b16 v[30:31], v5 offset:16896\n"
"s_waitcnt vmcnt(5) lgkmcnt(6)\n"
"v_mfma_f32_16x16x32_bf16 v[48:51], v[40:43], v[216:219], v[48:51]\n"
"ds_read_b64_tr_b16 v[32:33], v4 offset:8192\n"
"s_setprio 1\n"
"s_waitcnt vmcnt(4)\n"
"v_mfma_f32_16x16x32_bf16 v[52:55], v[40:43], v[220:223], v[52:55]\n"
"ds_read_b64_tr_b16 v[34:35], v4 offset:8704\n"
"s_add_u32 s46, s96, 16384\n"
"buffer_load_dwordx4 v[240:243], v1, s[48:51], s46 offen sc1\n"
"s_waitcnt lgkmcnt(6)\n"
"v_mfma_f32_16x16x32_bf16 v[60:63], v[44:47], v[220:223], v[60:63]\n"
"ds_read_b64_tr_b16 v[36:37], v4 offset:20480\n"
"v_mfma_f32_16x16x32_bf16 v[56:59], v[44:47], v[216:219], v[56:59]\n"
"ds_read_b64_tr_b16 v[38:39], v4 offset:20992\n"
"s_waitcnt lgkmcnt(6)\n"
"v_mfma_f32_16x16x32_bf16 v[64:67], v[24:27], v[216:219], v[64:67]\n"
"ds_read_b64_tr_b16 v[40:41], v5 offset:8192\n"
"v_mfma_f32_16x16x32_bf16 v[68:71], v[24:27], v[220:223], v[68:71]\n"
"ds_read_b64_tr_b16 v[42:43], v5 offset:8704\n"
"s_add_u32 s46, s96, 34816\n"
"buffer_load_dwordx4 v[244:247], v1, s[48:51], s46 offen sc1\n"
"s_waitcnt lgkmcnt(6)\n"
"v_mfma_f32_16x16x32_bf16 v[76:79], v[28:31], v[220:223], v[76:79]\n"
"ds_read_b64_tr_b16 v[44:45], v5 offset:20480\n"
"v_mfma_f32_16x16x32_bf16 v[72:75], v[28:31], v[216:219], v[72:75]\n"
"ds_read_b64_tr_b16 v[46:47], v5 offset:20992\n"
"s_waitcnt vmcnt(5) lgkmcnt(6)\n"
"v_mfma_f32_16x16x32_bf16 v[48:51], v[32:35], v[224:227], v[48:51]\n"
"ds_read_b64_tr_b16 v[24:25], v4 offset:24576\n"
"s_waitcnt vmcnt(4)\n"
"v_mfma_f32_16x16x32_bf16 v[52:55], v[32:35], v[228:231], v[52:55]\n"
"ds_read_b64_tr_b16 v[26:27], v4 offset:25088\n"
"s_add_u32 s46, s96, 17408\n"
"buffer_load_dwordx4 v[248:251], v1, s[48:51], s46 offen sc1\n"
"s_waitcnt lgkmcnt(6)\n"
"v_mfma_f32_16x16x32_bf16 v[60:63], v[36:39], v[228:231], v[60:63]\n"
"ds_read_b64_tr_b16 v[28:29], v4 offset:36864\n"
"v_mfma_f32_16x16x32_bf16 v[56:59], v[36:39], v[224:227], v[56:59]\n"
"ds_read_b64_tr_b16 v[30:31], v4 offset:37376\n"
"s_waitcnt lgkmcnt(6)\n"
"v_mfma_f32_16x16x32_bf16 v[64:67], v[40:43], v[224:227], v[64:67]\n"
"ds_read_b64_tr_b16 v[32:33], v5 offset:24576\n"
"v_mfma_f32_16x16x32_bf16 v[68:71], v[40:43], v[228:231], v[68:71]\n"
"ds_read_b64_tr_b16 v[34:35], v5 offset:25088\n"
"s_add_u32 s46, s96, 35840\n"
"buffer_load_dwordx4 v[252:255], v1, s[48:51], s46 offen sc1\n"
"s_waitcnt lgkmcnt(6)\n"
"v_mfma_f32_16x16x32_bf16 v[76:79], v[44:47], v[228:231], v[76:79]\n"
"ds_read_b64_tr_b16 v[36:37], v5 offset:36864\n"
"v_mfma_f32_16x16x32_bf16 v[72:75], v[44:47], v[224:227], v[72:75]\n"
"ds_read_b64_tr_b16 v[38:39], v5 offset:37376\n"
"s_waitcnt vmcnt(5) lgkmcnt(6)\n"
"v_mfma_f32_16x16x32_bf16 v[48:51], v[24:27], v[232:235], v[48:51]\n"
"ds_read_b64_tr_b16 v[40:41], v4 offset:28672\n"
"s_setprio 0\n"
"s_waitcnt vmcnt(4)\n"
"v_mfma_f32_16x16x32_bf16 v[52:55], v[24:27], v[236:239], v[52:55]\n"
"ds_read_b64_tr_b16 v[42:43], v4 offset:29184\n"
"s_waitcnt lgkmcnt(6)\n"
"v_mfma_f32_16x16x32_bf16 v[60:63], v[28:31], v[236:239], v[60:63]\n"
"ds_read_b64_tr_b16 v[44:45], v4 offset:40960\n"
"v_mfma_f32_16x16x32_bf16 v[56:59], v[28:31], v[232:235], v[56:59]\n"
"ds_read_b64_tr_b16 v[46:47], v4 offset:41472\n"
"s_waitcnt lgkmcnt(6)\n"
"v_mfma_f32_16x16x32_bf16 v[64:67], v[32:35], v[232:235], v[64:67]\n"
"ds_read_b64_tr_b16 v[24:25], v5 offset:28672\n"
"v_mfma_f32_16x16x32_bf16 v[68:71], v[32:35], v[236:239], v[68:71]\n"
"ds_read_b64_tr_b16 v[26:27], v5 offset:29184\n"
"s_waitcnt lgkmcnt(6)\n"
"v_mfma_f32_16x16x32_bf16 v[76:79], v[36:39], v[236:239], v[76:79]\n"
"ds_read_b64_tr_b16 v[28:29], v5 offset:40960\n"
"v_mfma_f32_16x16x32_bf16 v[72:75], v[36:39], v[232:235], v[72:75]\n"
"ds_read_b64_tr_b16 v[30:31], v5 offset:41472\n"
"s_waitcnt vmcnt(3) lgkmcnt(6)\n"
"v_mfma_f32_16x16x32_bf16 v[48:51], v[40:43], v[240:243], v[48:51]\n"
"ds_read_b64_tr_b16 v[32:33], v4 offset:32768\n"
"s_waitcnt vmcnt(2)\n"
"v_mfma_f32_16x16x32_bf16 v[52:55], v[40:43], v[244:247], v[52:55]\n"
"ds_read_b64_tr_b16 v[34:35], v4 offset:33280\n"
"s_waitcnt lgkmcnt(6)\n"
"v_mfma_f32_16x16x32_bf16 v[60:63], v[44:47], v[244:247], v[60:63]\n"
"ds_read_b64_tr_b16 v[36:37], v4 offset:45056\n"
"v_mfma_f32_16x16x32_bf16 v[56:59], v[44:47], v[240:243], v[56:59]\n"
"ds_read_b64_tr_b16 v[38:39], v4 offset:45568\n"
"s_mul_i32 s45, s27, 36\n"
"s_mul_i32 s42, s26, 9\n"
"s_add_u32 s45, s45, s42\n"
"v_add_u32 v21, s45, v0\n"
"s_waitcnt lgkmcnt(6)\n"
"v_mfma_f32_16x16x32_bf16 v[64:67], v[24:27], v[240:243], v[64:67]\n"
"ds_read_b64_tr_b16 v[40:41], v5 offset:32768\n"
"v_mfma_f32_16x16x32_bf16 v[68:71], v[24:27], v[244:247], v[68:71]\n"
"ds_read_b64_tr_b16 v[42:43], v5 offset:33280\n"
"v_lshrrev_b32 v3, 1, v21\n"
"v_and_b32 v21, 1, v21\n"
"v_mul_u32_u24 v3, 72, v3\n"
"v_add_u32 v21, v21, v3\n"
"v_lshlrev_b32 v21, 7, v21\n"
"s_waitcnt lgkmcnt(6)\n"
"v_mfma_f32_16x16x32_bf16 v[76:79], v[28:31], v[244:247], v[76:79]\n"
"ds_read_b64_tr_b16 v[44:45], v5 offset:45056\n"
"v_mfma_f32_16x16x32_bf16 v[72:75], v[28:31], v[240:243], v[72:75]\n"
"ds_read_b64_tr_b16 v[46:47], v5 offset:45568\n"
"v_sub_u32 v3, 8, v0\n"
"v_and_b32 v3, 0x80000000, v3\n"
"v_or_b32 v21, v21, v3\n"
"s_barrier\n"
"s_waitcnt vmcnt(1) lgkmcnt(6)\n"
"v_mfma_f32_16x16x32_bf16 v[48:51], v[32:35], v[248:251], v[48:51]\n"
"s_mov_b32 s44, 98304\n"
"v_xad_u32 v4, 64, v22, s44\n"
"ds_read_b64_tr_b16 v[24:25], v4\n"
"s_setprio 3\n"
"s_waitcnt vmcnt(0)\n"
"v_mfma_f32_16x16x32_bf16 v[52:55], v[32:35], v[252:255], v[52:55]\n"
"ds_read_b64_tr_b16 v[26:27], v4 offset:512\n"
"s_mul_i32 s45, s30, 256\n"
"s_add_u32 s45, s45, 8192\n"
"buffer_load_dword v20, v21, s[52:55], s45 offen\n"
"s_waitcnt lgkmcnt(6)\n"
"v_mfma_f32_16x16x32_bf16 v[60:63], v[36:39], v[252:255], v[60:63]\n"
"ds_read_b64_tr_b16 v[28:29], v4 offset:12288\n"
"v_mfma_f32_16x16x32_bf16 v[56:59], v[36:39], v[248:251], v[56:59]\n"
"ds_read_b64_tr_b16 v[30:31], v4 offset:12800\n"
"s_mul_i32 s45, s27, 48\n"
"s_mul_i32 s42, s26, 12\n"
"s_add_u32 s45, s45, s42\n"
"v_add_u32 v21, s45, v0\n"
"s_waitcnt lgkmcnt(6)\n"
"v_mfma_f32_16x16x32_bf16 v[64:67], v[40:43], v[248:251], v[64:67]\n"
"v_xor_b32 v5, 32, v4\n"
"ds_read_b64_tr_b16 v[32:33], v5\n"
"v_mfma_f32_16x16x32_bf16 v[68:71], v[40:43], v[252:255], v[68:71]\n"
"ds_read_b64_tr_b16 v[34:35], v5 offset:512\n"
"v_lshrrev_b32 v3, 3, v21\n"
"v_and_b32 v21, 7, v21\n"
"v_mul_u32_u24 v3, 72, v3\n"
"v_add_u32 v21, v21, v3\n"
"v_lshlrev_b32 v21, 7, v21\n"
"s_waitcnt lgkmcnt(6)\n"
"v_mfma_f32_16x16x32_bf16 v[76:79], v[44:47], v[252:255], v[76:79]\n"
"ds_read_b64_tr_b16 v[36:37], v5 offset:12288\n"
"v_mfma_f32_16x16x32_bf16 v[72:75], v[44:47], v[248:251], v[72:75]\n"
"ds_read_b64_tr_b16 v[38:39], v5 offset:12800\n"
"v_sub_u32 v3, 11, v0\n"
"v_and_b32 v3, 0x80000000, v3\n"
"v_or_b32 v21, v21, v3\n"
"s_waitcnt lgkmcnt(6)\n"
"v_mfma_f32_16x16x32_bf16 v[80:83], v[24:27], v[112:115], 0\n"
"ds_read_b64_tr_b16 v[40:41], v4 offset:4096\n"
"v_exp_f32 v8, -v48\n"
"v_mfma_f32_16x16x32_bf16 v[84:87], v[24:27], v[116:119], 0\n"
"ds_read_b64_tr_b16 v[42:43], v4 offset:4608\n"
"s_mul_i32 s45, s30, 1024\n"
"s_add_u32 s45, s45, 4096\n"
"buffer_load_dword v15, v21, s[52:55], s45 offen\n"
"s_waitcnt lgkmcnt(6)\n"
"v_mfma_f32_16x16x32_bf16 v[92:95], v[28:31], v[116:119], 0\n"
"ds_read_b64_tr_b16 v[44:45], v4 offset:16384\n"
"v_exp_f32 v9, -v49\n"
"v_mfma_f32_16x16x32_bf16 v[88:91], v[28:31], v[112:115], 0\n"
"ds_read_b64_tr_b16 v[46:47], v4 offset:16896\n"
"s_mul_i32 s45, s30, 1024\n"
"s_add_u32 s45, s45, 1773568\n"
"buffer_load_dword v12, v21, s[52:55], s45 offen\n"
"s_waitcnt lgkmcnt(6)\n"
"v_mfma_f32_16x16x32_bf16 v[96:99], v[32:35], v[112:115], 0\n"
"ds_read_b64_tr_b16 v[24:25], v5 offset:4096\n"
"v_add_f32 v8, 1.0, v8\n"
"v_mfma_f32_16x16x32_bf16 v[100:103], v[32:35], v[116:119], 0\n"
"ds_read_b64_tr_b16 v[26:27], v5 offset:4608\n"
"s_mul_i32 s45, s30, 1024\n"
"s_add_u32 s45, s45, 3543040\n"
"buffer_load_dword v13, v21, s[52:55], s45 offen\n"
"s_waitcnt lgkmcnt(6)\n"
"v_mfma_f32_16x16x32_bf16 v[108:111], v[36:39], v[116:119], 0\n"
"ds_read_b64_tr_b16 v[28:29], v5 offset:16384\n"
"s_setprio 2\n"
"v_add_f32 v9, 1.0, v9\n"
"v_mfma_f32_16x16x32_bf16 v[104:107], v[36:39], v[112:115], 0\n"
"ds_read_b64_tr_b16 v[30:31], v5 offset:16896\n"
"s_waitcnt lgkmcnt(6)\n"
"v_mfma_f32_16x16x32_bf16 v[80:83], v[40:43], v[120:123], v[80:83]\n"
"ds_read_b64_tr_b16 v[32:33], v4 offset:8192\n"
"v_rcp_f32 v8, v8\n"
"v_mfma_f32_16x16x32_bf16 v[84:87], v[40:43], v[124:127], v[84:87]\n"
"ds_read_b64_tr_b16 v[34:35], v4 offset:8704\n"
"s_waitcnt lgkmcnt(6)\n"
"v_mfma_f32_16x16x32_bf16 v[92:95], v[44:47], v[124:127], v[92:95]\n"
"ds_read_b64_tr_b16 v[36:37], v4 offset:20480\n"
"v_rcp_f32 v9, v9\n"
"v_mfma_f32_16x16x32_bf16 v[88:91], v[44:47], v[120:123], v[88:91]\n"
"ds_read_b64_tr_b16 v[38:39], v4 offset:20992\n"
"s_waitcnt lgkmcnt(6)\n"
"v_mfma_f32_16x16x32_bf16 v[96:99], v[24:27], v[120:123], v[96:99]\n"
"ds_read_b64_tr_b16 v[40:41], v5 offset:8192\n"
"v_mul_f32 v8, v8, v56\n"
"v_mfma_f32_16x16x32_bf16 v[100:103], v[24:27], v[124:127], v[100:103]\n"
"ds_read_b64_tr_b16 v[42:43], v5 offset:8704\n"
"s_waitcnt lgkmcnt(6)\n"
"v_mfma_f32_16x16x32_bf16 v[108:111], v[28:31], v[124:127], v[108:111]\n"
"ds_read_b64_tr_b16 v[44:45], v5 offset:20480\n"
"v_mul_f32 v9, v9, v57\n"
"v_mfma_f32_16x16x32_bf16 v[104:107], v[28:31], v[120:123], v[104:107]\n"
"ds_read_b64_tr_b16 v[46:47], v5 offset:20992\n"
"s_waitcnt lgkmcnt(6)\n"
"v_mfma_f32_16x16x32_bf16 v[80:83], v[32:35], v[128:131], v[80:83]\n"
"ds_read_b64_tr_b16 v[24:25], v4 offset:24576\n"
"v_cvt_pk_bf16_f32 v48, v8, v9\n"
"v_mfma_f32_16x16x32_bf16 v[84:87], v[32:35], v[132:135], v[84:87]\n"
"ds_read_b64_tr_b16 v[26:27], v4 offset:25088\n"
"s_waitcnt lgkmcnt(6)\n"
"v_mfma_f32_16x16x32_bf16 v[92:95], v[36:39], v[132:135], v[92:95]\n"
"ds_read_b64_tr_b16 v[28:29], v4 offset:36864\n"
"v_exp_f32 v8, -v50\n"
"v_mfma_f32_16x16x32_bf16 v[88:91], v[36:39], v[128:131], v[88:91]\n"
"ds_read_b64_tr_b16 v[30:31], v4 offset:37376\n"
"s_waitcnt lgkmcnt(6)\n"
"v_mfma_f32_16x16x32_bf16 v[96:99], v[40:43], v[128:131], v[96:99]\n"
"ds_read_b64_tr_b16 v[32:33], v5 offset:24576\n"
"s_setprio 1\n"
"v_exp_f32 v9, -v51\n"
"v_mfma_f32_16x16x32_bf16 v[100:103], v[40:43], v[132:135], v[100:103]\n"
"ds_read_b64_tr_b16 v[34:35], v5 offset:25088\n"
"s_waitcnt lgkmcnt(6)\n"
"v_mfma_f32_16x16x32_bf16 v[108:111], v[44:47], v[132:135], v[108:111]\n"
"ds_read_b64_tr_b16 v[36:37], v5 offset:36864\n"
"v_add_f32 v8, 1.0, v8\n"
"v_mfma_f32_16x16x32_bf16 v[104:107], v[44:47], v[128:131], v[104:107]\n"
"ds_read_b64_tr_b16 v[38:39], v5 offset:37376\n"
"s_waitcnt lgkmcnt(6)\n"
"v_mfma_f32_16x16x32_bf16 v[80:83], v[24:27], v[136:139], v[80:83]\n"
"ds_read_b64_tr_b16 v[40:41], v4 offset:28672\n"
"v_add_f32 v9, 1.0, v9\n"
"v_mfma_f32_16x16x32_bf16 v[84:87], v[24:27], v[140:143], v[84:87]\n"
"ds_read_b64_tr_b16 v[42:43], v4 offset:29184\n"
"s_waitcnt lgkmcnt(6)\n"
"v_mfma_f32_16x16x32_bf16 v[92:95], v[28:31], v[140:143], v[92:95]\n"
"ds_read_b64_tr_b16 v[44:45], v4 offset:40960\n"
"v_rcp_f32 v8, v8\n"
"v_mfma_f32_16x16x32_bf16 v[88:91], v[28:31], v[136:139], v[88:91]\n"
"ds_read_b64_tr_b16 v[46:47], v4 offset:41472\n"
"s_waitcnt lgkmcnt(6)\n"
"v_mfma_f32_16x16x32_bf16 v[96:99], v[32:35], v[136:139], v[96:99]\n"
"ds_read_b64_tr_b16 v[24:25], v5 offset:28672\n"
"v_rcp_f32 v9, v9\n"
"v_mfma_f32_16x16x32_bf16 v[100:103], v[32:35], v[140:143], v[100:103]\n"
"ds_read_b64_tr_b16 v[26:27], v5 offset:29184\n"
"s_waitcnt lgkmcnt(6)\n"
"v_mfma_f32_16x16x32_bf16 v[108:111], v[36:39], v[140:143], v[108:111]\n"
"ds_read_b64_tr_b16 v[28:29], v5 offset:40960\n"
"v_mul_f32 v8, v8, v58\n"
"v_mfma_f32_16x16x32_bf16 v[104:107], v[36:39], v[136:139], v[104:107]\n"
"ds_read_b64_tr_b16 v[30:31], v5 offset:41472\n"
"s_waitcnt lgkmcnt(6)\n"
"v_mfma_f32_16x16x32_bf16 v[80:83], v[40:43], v[144:147], v[80:83]\n"
"ds_read_b64_tr_b16 v[32:33], v4 offset:32768\n"
"v_mul_f32 v9, v9, v59\n"
"v_mfma_f32_16x16x32_bf16 v[84:87], v[40:43], v[148:151], v[84:87]\n"
"ds_read_b64_tr_b16 v[34:35], v4 offset:33280\n"
"s_waitcnt lgkmcnt(6)\n"
"v_mfma_f32_16x16x32_bf16 v[92:95], v[44:47], v[148:151], v[92:95]\n"
"ds_read_b64_tr_b16 v[36:37], v4 offset:45056\n"
"s_setprio 0\n"
"v_cvt_pk_bf16_f32 v49, v8, v9\n"
"v_mfma_f32_16x16x32_bf16 v[88:91], v[44:47], v[144:147], v[88:91]\n"
"ds_read_b64_tr_b16 v[38:39], v4 offset:45568\n"
"s_waitcnt lgkmcnt(6)\n"
"v_mfma_f32_16x16x32_bf16 v[96:99], v[24:27], v[144:147], v[96:99]\n"
"ds_read_b64_tr_b16 v[40:41], v5 offset:32768\n"
"v_exp_f32 v8, -v64\n"
"v_mfma_f32_16x16x32_bf16 v[100:103], v[24:27], v[148:151], v[100:103]\n"
"ds_read_b64_tr_b16 v[42:43], v5 offset:33280\n"
"s_waitcnt lgkmcnt(6)\n"
"v_mfma_f32_16x16x32_bf16 v[108:111], v[28:31], v[148:151], v[108:111]\n"
"ds_read_b64_tr_b16 v[44:45], v5 offset:45056\n"
"v_exp_f32 v9, -v65\n"
"v_mfma_f32_16x16x32_bf16 v[104:107], v[28:31], v[144:147], v[104:107]\n"
"ds_read_b64_tr_b16 v[46:47], v5 offset:45568\n"
"s_waitcnt lgkmcnt(6)\n"
"v_mfma_f32_16x16x32_bf16 v[80:83], v[32:35], v[152:155], v[80:83]\n"
"s_mov_b32 s44, 49152\n"
"v_xad_u32 v4, 64, v22, s44\n"
"ds_read_b64_tr_b16 v[24:25], v4\n"
"v_add_f32 v8, 1.0, v8\n"
"v_mfma_f32_16x16x32_bf16 v[84:87], v[32:35], v[156:159], v[84:87]\n"
"ds_read_b64_tr_b16 v[26:27], v4 offset:512\n"
"s_waitcnt lgkmcnt(6)\n"
"v_mfma_f32_16x16x32_bf16 v[92:95], v[36:39], v[156:159], v[92:95]\n"
"ds_read_b64_tr_b16 v[28:29], v4 offset:12288\n"
"v_add_f32 v9, 1.0, v9\n"
"v_mfma_f32_16x16x32_bf16 v[88:91], v[36:39], v[152:155], v[88:91]\n"
"ds_read_b64_tr_b16 v[30:31], v4 offset:12800\n"
"s_waitcnt lgkmcnt(6)\n"
"v_mfma_f32_16x16x32_bf16 v[96:99], v[40:43], v[152:155], v[96:99]\n"
"v_xor_b32 v5, 32, v4\n"
"ds_read_b64_tr_b16 v[32:33], v5\n"
"v_rcp_f32 v8, v8\n"
"v_mfma_f32_16x16x32_bf16 v[100:103], v[40:43], v[156:159], v[100:103]\n"
"ds_read_b64_tr_b16 v[34:35], v5 offset:512\n"
"s_waitcnt lgkmcnt(6)\n"
"v_mfma_f32_16x16x32_bf16 v[108:111], v[44:47], v[156:159], v[108:111]\n"
"ds_read_b64_tr_b16 v[36:37], v5 offset:12288\n"
"v_rcp_f32 v9, v9\n"
"v_mfma_f32_16x16x32_bf16 v[104:107], v[44:47], v[152:155], v[104:107]\n"
"ds_read_b64_tr_b16 v[38:39], v5 offset:12800\n"
"s_barrier\n"
"s_waitcnt lgkmcnt(6)\n"
"v_mfma_f32_16x16x32_bf16 v[80:83], v[24:27], v[160:163], v[80:83]\n"
"ds_read_b64_tr_b16 v[40:41], v4 offset:4096\n"
"s_setprio 3\n"
"v_mul_f32 v8, v8, v72\n"
"v_mfma_f32_16x16x32_bf16 v[84:87], v[24:27], v[164:167], v[84:87]\n"
"ds_read_b64_tr_b16 v[42:43], v4 offset:4608\n"
"s_waitcnt lgkmcnt(6)\n"
"v_mfma_f32_16x16x32_bf16 v[92:95], v[28:31], v[164:167], v[92:95]\n"
"ds_read_b64_tr_b16 v[44:45], v4 offset:16384\n"
"v_mul_f32 v9, v9, v73\n"
"v_mfma_f32_16x16x32_bf16 v[88:91], v[28:31], v[160:163], v[88:91]\n"
"ds_read_b64_tr_b16 v[46:47], v4 offset:16896\n"
"s_waitcnt lgkmcnt(6)\n"
"v_mfma_f32_16x16x32_bf16 v[96:99], v[32:35], v[160:163], v[96:99]\n"
"ds_read_b64_tr_b16 v[24:25], v5 offset:4096\n"
"v_cvt_pk_bf16_f32 v50, v8, v9\n"
"v_mfma_f32_16x16x32_bf16 v[100:103], v[32:35], v[164:167], v[100:103]\n"
"ds_read_b64_tr_b16 v[26:27], v5 offset:4608\n"
"s_waitcnt lgkmcnt(6)\n"
"v_mfma_f32_16x16x32_bf16 v[108:111], v[36:39], v[164:167], v[108:111]\n"
"ds_read_b64_tr_b16 v[28:29], v5 offset:16384\n"
"v_exp_f32 v8, -v66\n"
"v_mfma_f32_16x16x32_bf16 v[104:107], v[36:39], v[160:163], v[104:107]\n"
"ds_read_b64_tr_b16 v[30:31], v5 offset:16896\n"
"s_waitcnt lgkmcnt(6)\n"
"v_mfma_f32_16x16x32_bf16 v[80:83], v[40:43], v[168:171], v[80:83]\n"
"ds_read_b64_tr_b16 v[32:33], v4 offset:8192\n"
"v_exp_f32 v9, -v67\n"
"v_mfma_f32_16x16x32_bf16 v[84:87], v[40:43], v[172:175], v[84:87]\n"
"ds_read_b64_tr_b16 v[34:35], v4 offset:8704\n"
"s_waitcnt lgkmcnt(6)\n"
"v_mfma_f32_16x16x32_bf16 v[92:95], v[44:47], v[172:175], v[92:95]\n"
"ds_read_b64_tr_b16 v[36:37], v4 offset:20480\n"
"v_add_f32 v8, 1.0, v8\n"
"v_mfma_f32_16x16x32_bf16 v[88:91], v[44:47], v[168:171], v[88:91]\n"
"ds_read_b64_tr_b16 v[38:39], v4 offset:20992\n"
"s_waitcnt lgkmcnt(6)\n"
"v_mfma_f32_16x16x32_bf16 v[96:99], v[24:27], v[168:171], v[96:99]\n"
"ds_read_b64_tr_b16 v[40:41], v5 offset:8192\n"
"s_setprio 2\n"
"v_add_f32 v9, 1.0, v9\n"
"v_mfma_f32_16x16x32_bf16 v[100:103], v[24:27], v[172:175], v[100:103]\n"
"ds_read_b64_tr_b16 v[42:43], v5 offset:8704\n"
"s_waitcnt lgkmcnt(6)\n"
"v_mfma_f32_16x16x32_bf16 v[108:111], v[28:31], v[172:175], v[108:111]\n"
"ds_read_b64_tr_b16 v[44:45], v5 offset:20480\n"
"v_rcp_f32 v8, v8\n"
"v_mfma_f32_16x16x32_bf16 v[104:107], v[28:31], v[168:171], v[104:107]\n"
"ds_read_b64_tr_b16 v[46:47], v5 offset:20992\n"
"s_waitcnt lgkmcnt(6)\n"
"v_mfma_f32_16x16x32_bf16 v[80:83], v[32:35], v[176:179], v[80:83]\n"
"s_branch .Lsk6_0\n"
".Lst6_0:\n"
"s_branch .Lbk6\n"
".Lsk6_0:\n"
"ds_read_b64_tr_b16 v[24:25], v4 offset:24576\n"
"v_rcp_f32 v9, v9\n"
"v_mfma_f32_16x16x32_bf16 v[84:87], v[32:35], v[180:183], v[84:87]\n"
"ds_read_b64_tr_b16 v[26:27], v4 offset:25088\n"
"s_waitcnt lgkmcnt(6)\n"
"v_mfma_f32_16x16x32_bf16 v[92:95], v[36:39], v[180:183], v[92:95]\n"
"ds_read_b64_tr_b16 v[28:29], v4 offset:36864\n"
"v_mul_f32 v8, v8, v74\n"
"v_mfma_f32_16x16x32_bf16 v[88:91], v[36:39], v[176:179], v[88:91]\n"
"ds_read_b64_tr_b16 v[30:31], v4 offset:37376\n"
"s_waitcnt lgkmcnt(6)\n"
"v_mfma_f32_16x16x32_bf16 v[96:99], v[40:43], v[176:179], v[96:99]\n"
"ds_read_b64_tr_b16 v[32:33], v5 offset:24576\n"
"v_mul_f32 v9, v9, v75\n"
"v_mfma_f32_16x16x32_bf16 v[100:103], v[40:43], v[180:183], v[100:103]\n"
"ds_read_b64_tr_b16 v[34:35], v5 offset:25088\n"
"v_bfe_u32 v3, v0, 4, 1\n"
"v_lshlrev_b32 v3, 3, v3\n"
"v_xor_b32 v3, v3, v0\n"
"v_lshrrev_b32 v3, 1, v3\n"
"v_mul_u32_u24 v2, 9216, v3\n"
"v_and_b32 v3, 1, v0\n"
"v_lshl_add_u32 v2, v3, 4, v2\n"
"s_waitcnt lgkmcnt(6)\n"
"v_mfma_f32_16x16x32_bf16 v[108:111], v[44:47], v[180:183], v[108:111]\n"
"ds_read_b64_tr_b16 v[36:37], v5 offset:36864\n"
"v_cvt_pk_bf16_f32 v51, v8, v9\n"
"v_mfma_f32_16x16x32_bf16 v[104:107], v[44:47], v[176:179], v[104:107]\n"
"ds_read_b64_tr_b16 v[38:39], v5 offset:37376\n"
"s_add_u32 s46, s9, 0\n"
"buffer_store_dwordx4 v[48:51], v1, s[48:51], s46 offen sc1\n"
"s_waitcnt lgkmcnt(6)\n"
"v_mfma_f32_16x16x32_bf16 v[80:83], v[24:27], v[184:187], v[80:83]\n"
"ds_read_b64_tr_b16 v[40:41], v4 offset:28672\n"
"s_setprio 1\n"
"v_exp_f32 v8, -v52\n"
"v_mfma_f32_16x16x32_bf16 v[84:87], v[24:27], v[188:191], v[84:87]\n"
"ds_read_b64_tr_b16 v[42:43], v4 offset:29184\n"
"s_add_u32 m0, s76, 98304\n"
"s_add_u32 s46, s5, 0\n"
"buffer_load_dwordx4 v2, s[52:55], s46 offen lds\n"
"s_waitcnt lgkmcnt(6)\n"
"v_mfma_f32_16x16x32_bf16 v[92:95], v[28:31], v[188:191], v[92:95]\n"
"ds_read_b64_tr_b16 v[44:45], v4 offset:40960\n"
"v_exp_f32 v9, -v53\n"
"v_mfma_f32_16x16x32_bf16 v[88:91], v[28:31], v[184:187], v[88:91]\n"
"ds_read_b64_tr_b16 v[46:47], v4 offset:41472\n"
"s_waitcnt lgkmcnt(6)\n"
"v_mfma_f32_16x16x32_bf16 v[96:99], v[32:35], v[184:187], v[96:99]\n"
"ds_read_b64_tr_b16 v[24:25], v5 offset:28672\n"
"v_add_f32 v8, 1.0, v8\n"
"v_mfma_f32_16x16x32_bf16 v[100:103], v[32:35], v[188:191], v[100:103]\n"
"ds_read_b64_tr_b16 v[26:27], v5 offset:29184\n"
"s_waitcnt lgkmcnt(6)\n"
"v_mfma_f32_16x16x32_bf16 v[108:111], v[36:39], v[188:191], v[108:111]\n"
"ds_read_b64_tr_b16 v[28:29], v5 offset:40960\n"
"v_add_f32 v9, 1.0, v9\n"
"v_mfma_f32_16x16x32_bf16 v[104:107], v[36:39], v[184:187], v[104:107]\n"
"ds_read_b64_tr_b16 v[30:31], v5 offset:41472\n"
"s_waitcnt lgkmcnt(6)\n"
"v_mfma_f32_16x16x32_bf16 v[80:83], v[40:43], v[192:195], v[80:83]\n"
"ds_read_b64_tr_b16 v[32:33], v4 offset:32768\n"
"v_rcp_f32 v8, v8\n"
"v_mfma_f32_16x16x32_bf16 v[84:87], v[40:43], v[196:199], v[84:87]\n"
"ds_read_b64_tr_b16 v[34:35], v4 offset:33280\n"
"s_waitcnt lgkmcnt(6)\n"
"v_mfma_f32_16x16x32_bf16 v[92:95], v[44:47], v[196:199], v[92:95]\n"
"ds_read_b64_tr_b16 v[36:37], v4 offset:45056\n"
"v_rcp_f32 v9, v9\n"
"v_mfma_f32_16x16x32_bf16 v[88:91], v[44:47], v[192:195], v[88:91]\n"
"ds_read_b64_tr_b16 v[38:39], v4 offset:45568\n"
"s_waitcnt lgkmcnt(6)\n"
"v_mfma_f32_16x16x32_bf16 v[96:99], v[24:27], v[192:195], v[96:99]\n"
"ds_read_b64_tr_b16 v[40:41], v5 offset:32768\n"
"s_setprio 0\n"
"v_mul_f32 v8, v8, v60\n"
"v_mfma_f32_16x16x32_bf16 v[100:103], v[24:27], v[196:199], v[100:103]\n"
"ds_read_b64_tr_b16 v[42:43], v5 offset:33280\n"
"s_waitcnt lgkmcnt(6)\n"
"v_mfma_f32_16x16x32_bf16 v[108:111], v[28:31], v[196:199], v[108:111]\n"
"ds_read_b64_tr_b16 v[44:45], v5 offset:45056\n"
"v_mul_f32 v9, v9, v61\n"
"v_mfma_f32_16x16x32_bf16 v[104:107], v[28:31], v[192:195], v[104:107]\n"
"ds_read_b64_tr_b16 v[46:47], v5 offset:45568\n"
"s_add_u32 m0, s76, 99328\n"
"s_add_u32 s46, s5, 294912\n"
"buffer_load_dwordx4 v2, s[52:55], s46 offen lds\n"
"s_waitcnt lgkmcnt(6)\n"
"v_mfma_f32_16x16x32_bf16 v[80:83], v[32:35], v[200:203], v[80:83]\n"
"s_mov_b32 s44, 0\n"
"v_xad_u32 v4, 64, v22, s44\n"
"ds_read_b64_tr_b16 v[24:25], v4\n"
"v_cvt_pk_bf16_f32 v52, v8, v9\n"
"v_mfma_f32_16x16x32_bf16 v[84:87], v[32:35], v[204:207], v[84:87]\n"
"ds_read_b64_tr_b16 v[26:27], v4 offset:512\n"
"s_waitcnt lgkmcnt(6)\n"
"v_mfma_f32_16x16x32_bf16 v[92:95], v[36:39], v[204:207], v[92:95]\n"
"ds_read_b64_tr_b16 v[28:29], v4 offset:12288\n"
"v_exp_f32 v8, -v54\n"
"v_mfma_f32_16x16x32_bf16 v[88:91], v[36:39], v[200:203], v[88:91]\n"
"ds_read_b64_tr_b16 v[30:31], v4 offset:12800\n"
"s_waitcnt lgkmcnt(6)\n"
"v_mfma_f32_16x16x32_bf16 v[96:99], v[40:43], v[200:203], v[96:99]\n"
"v_xor_b32 v5, 32, v4\n"
"ds_read_b64_tr_b16 v[32:33], v5\n"
"v_exp_f32 v9, -v55\n"
"v_mfma_f32_16x16x32_bf16 v[100:103], v[40:43], v[204:207], v[100:103]\n"
"ds_read_b64_tr_b16 v[34:35], v5 offset:512\n"
"s_waitcnt lgkmcnt(6)\n"
"v_mfma_f32_16x16x32_bf16 v[108:111], v[44:47], v[204:207], v[108:111]\n"
"ds_read_b64_tr_b16 v[36:37], v5 offset:12288\n"
"v_add_f32 v8, 1.0, v8\n"
"v_mfma_f32_16x16x32_bf16 v[104:107], v[44:47], v[200:203], v[104:107]\n"
"ds_read_b64_tr_b16 v[38:39], v5 offset:12800\n"
"s_barrier\n"
"s_waitcnt lgkmcnt(6)\n"
"v_mfma_f32_16x16x32_bf16 v[80:83], v[24:27], v[208:211], v[80:83]\n"
"ds_read_b64_tr_b16 v[40:41], v4 offset:4096\n"
"s_setprio 3\n"
"v_add_f32 v9, 1.0, v9\n"
"v_mfma_f32_16x16x32_bf16 v[84:87], v[24:27], v[212:215], v[84:87]\n"
"ds_read_b64_tr_b16 v[42:43], v4 offset:4608\n"
"v_and_b32 v19, 3, v0\n"
"v_lshlrev_b32 v19, 13, v19\n"
"v_add_u32 v19, s83, v19\n"
"s_waitcnt lgkmcnt(6)\n"
"v_mfma_f32_16x16x32_bf16 v[92:95], v[28:31], v[212:215], v[92:95]\n"
"ds_read_b64_tr_b16 v[44:45], v4 offset:16384\n"
"v_rcp_f32 v8, v8\n"
"v_mfma_f32_16x16x32_bf16 v[88:91], v[28:31], v[208:211], v[88:91]\n"
"ds_read_b64_tr_b16 v[46:47], v4 offset:16896\n"
"s_add_u32 m0, s76, 100352\n"
"s_add_u32 s46, s5, 589824\n"
"buffer_load_dwordx4 v2, s[52:55], s46 offen lds\n"
"s_waitcnt lgkmcnt(6)\n"
"v_mfma_f32_16x16x32_bf16 v[96:99], v[32:35], v[208:211], v[96:99]\n"
"ds_read_b64_tr_b16 v[24:25], v5 offset:4096\n"
"v_rcp_f32 v9, v9\n"
"v_mfma_f32_16x16x32_bf16 v[100:103], v[32:35], v[212:215], v[100:103]\n"
"ds_read_b64_tr_b16 v[26:27], v5 offset:4608\n"
"s_waitcnt lgkmcnt(6)\n"
"v_mfma_f32_16x16x32_bf16 v[108:111], v[36:39], v[212:215], v[108:111]\n"
"ds_read_b64_tr_b16 v[28:29], v5 offset:16384\n"
"v_mul_f32 v8, v8, v62\n"
"v_mfma_f32_16x16x32_bf16 v[104:107], v[36:39], v[208:211], v[104:107]\n"
"ds_read_b64_tr_b16 v[30:31], v5 offset:16896\n"
"s_waitcnt lgkmcnt(6)\n"
"v_mfma_f32_16x16x32_bf16 v[80:83], v[40:43], v[216:219], v[80:83]\n"
"ds_read_b64_tr_b16 v[32:33], v4 offset:8192\n"
"v_mul_f32 v9, v9, v63\n"
"v_mfma_f32_16x16x32_bf16 v[84:87], v[40:43], v[220:223], v[84:87]\n"
"ds_read_b64_tr_b16 v[34:35], v4 offset:8704\n"
"s_waitcnt lgkmcnt(6)\n"
"v_mfma_f32_16x16x32_bf16 v[92:95], v[44:47], v[220:223], v[92:95]\n"
"ds_read_b64_tr_b16 v[36:37], v4 offset:20480\n"
"s_setprio 2\n"
"v_cvt_pk_bf16_f32 v53, v8, v9\n"
"v_mfma_f32_16x16x32_bf16 v[88:91], v[44:47], v[216:219], v[88:91]\n"
"ds_read_b64_tr_b16 v[38:39], v4 offset:20992\n"
"s_waitcnt lgkmcnt(6)\n"
"v_mfma_f32_16x16x32_bf16 v[96:99], v[24:27], v[216:219], v[96:99]\n"
"ds_read_b64_tr_b16 v[40:41], v5 offset:8192\n"
"v_exp_f32 v8, -v68\n"
"v_mfma_f32_16x16x32_bf16 v[100:103], v[24:27], v[220:223], v[100:103]\n"
"ds_read_b64_tr_b16 v[42:43], v5 offset:8704\n"
"s_add_u32 m0, s7, 122880\n"
"s_add_u32 s46, s6, 0\n"
"buffer_load_dwordx4 v2, s[52:55], s46 offen lds\n"
"s_waitcnt lgkmcnt(6)\n"
"v_mfma_f32_16x16x32_bf16 v[108:111], v[28:31], v[220:223], v[108:111]\n"
"ds_read_b64_tr_b16 v[44:45], v5 offset:20480\n"
"v_exp_f32 v9, -v69\n"
"v_mfma_f32_16x16x32_bf16 v[104:107], v[28:31], v[216:219], v[104:107]\n"
"ds_read_b64_tr_b16 v[46:47], v5 offset:20992\n"
"s_waitcnt lgkmcnt(6)\n"
"v_mfma_f32_16x16x32_bf16 v[80:83], v[32:35], v[224:227], v[80:83]\n"
"ds_read_b64_tr_b16 v[24:25], v4 offset:24576\n"
"v_add_f32 v8, 1.0, v8\n"
"v_mfma_f32_16x16x32_bf16 v[84:87], v[32:35], v[228:231], v[84:87]\n"
"ds_read_b64_tr_b16 v[26:27], v4 offset:25088\n"
"s_waitcnt lgkmcnt(6)\n"
"v_mfma_f32_16x16x32_bf16 v[92:95], v[36:39], v[228:231], v[92:95]\n"
"ds_read_b64_tr_b16 v[28:29], v4 offset:36864\n"
"v_add_f32 v9, 1.0, v9\n"
"v_mfma_f32_16x16x32_bf16 v[88:91], v[36:39], v[224:227], v[88:91]\n"
"ds_read_b64_tr_b16 v[30:31], v4 offset:37376\n"
"s_waitcnt lgkmcnt(6)\n"
"v_mfma_f32_16x16x32_bf16 v[96:99], v[40:43], v[224:227], v[96:99]\n"
"ds_read_b64_tr_b16 v[32:33], v5 offset:24576\n"
"s_setprio 1\n"
"v_rcp_f32 v8, v8\n"
"v_mfma_f32_16x16x32_bf16 v[100:103], v[40:43], v[228:231], v[100:103]\n"
"ds_read_b64_tr_b16 v[34:35], v5 offset:25088\n"
"s_waitcnt lgkmcnt(6)\n"
"v_mfma_f32_16x16x32_bf16 v[108:111], v[44:47], v[228:231], v[108:111]\n"
"ds_read_b64_tr_b16 v[36:37], v5 offset:36864\n"
"v_rcp_f32 v9, v9\n"
"v_mfma_f32_16x16x32_bf16 v[104:107], v[44:47], v[224:227], v[104:107]\n"
"ds_read_b64_tr_b16 v[38:39], v5 offset:37376\n"
"s_waitcnt lgkmcnt(6)\n"
"v_mfma_f32_16x16x32_bf16 v[80:83], v[24:27], v[232:235], v[80:83]\n"
"ds_read_b64_tr_b16 v[40:41], v4 offset:28672\n"
"v_mul_f32 v8, v8, v76\n"
"v_mfma_f32_16x16x32_bf16 v[84:87], v[24:27], v[236:239], v[84:87]\n"
"ds_read_b64_tr_b16 v[42:43], v4 offset:29184\n"
"v_lshrrev_b32 v3, 4, v0\n"
"v_lshlrev_b32 v22, 8, v3\n"
"s_add_u32 m0, s7, 123904\n"
"s_add_u32 s46, s6, 294912\n"
"buffer_load_dwordx4 v2, s[52:55], s46 offen lds\n"
"s_waitcnt lgkmcnt(6)\n"
"v_mfma_f32_16x16x32_bf16 v[92:95], v[28:31], v[236:239], v[92:95]\n"
"ds_read_b64_tr_b16 v[44:45], v4 offset:40960\n"
"v_mul_f32 v9, v9, v77\n"
"v_mfma_f32_16x16x32_bf16 v[88:91], v[28:31], v[232:235], v[88:91]\n"
"ds_read_b64_tr_b16 v[46:47], v4 offset:41472\n"
"v_bfe_u32 v3, v0, 4, 1\n"
"v_lshl_add_u32 v22, v3, 7, v22\n"
"s_waitcnt lgkmcnt(6)\n"
"v_mfma_f32_16x16x32_bf16 v[96:99], v[32:35], v[232:235], v[96:99]\n"
"ds_read_b64_tr_b16 v[24:25], v5 offset:28672\n"
"v_cvt_pk_bf16_f32 v54, v8, v9\n"
"v_mfma_f32_16x16x32_bf16 v[100:103], v[32:35], v[236:239], v[100:103]\n"
"ds_read_b64_tr_b16 v[26:27], v5 offset:29184\n"
"v_and_b32 v3, 15, v0\n"
"v_lshl_add_u32 v22, v3, 3, v22\n"
"s_waitcnt lgkmcnt(6)\n"
"v_mfma_f32_16x16x32_bf16 v[108:111], v[36:39], v[236:239], v[108:111]\n"
"ds_read_b64_tr_b16 v[28:29], v5 offset:40960\n"
"s_setprio 0\n"
"v_exp_f32 v8, -v70\n"
"v_mfma_f32_16x16x32_bf16 v[104:107], v[36:39], v[232:235], v[104:107]\n"
"ds_read_b64_tr_b16 v[30:31], v5 offset:41472\n"
"v_lshrrev_b32 v3, 3, v0\n"
"v_mul_u32_u24 v2, 9216, v3\n"
"s_lshl_b32 s45, s26, 2\n"
"v_and_b32 v3, 2, v3\n"
"v_xor_b32 v3, v3, v0\n"
"v_xor_b32 v3, s45, v3\n"
"v_and_b32 v3, 7, v3\n"
"v_lshl_add_u32 v2, v3, 4, v2\n"
"s_waitcnt lgkmcnt(6)\n"
"v_mfma_f32_16x16x32_bf16 v[80:83], v[40:43], v[240:243], v[80:83]\n"
"ds_read_b64_tr_b16 v[32:33], v4 offset:32768\n"
"v_exp_f32 v9, -v71\n"
"v_mfma_f32_16x16x32_bf16 v[84:87], v[40:43], v[244:247], v[84:87]\n"
"ds_read_b64_tr_b16 v[34:35], v4 offset:33280\n"
"s_waitcnt lgkmcnt(6)\n"
"v_mfma_f32_16x16x32_bf16 v[92:95], v[44:47], v[244:247], v[92:95]\n"
"ds_read_b64_tr_b16 v[36:37], v4 offset:45056\n"
"v_add_f32 v8, 1.0, v8\n"
"v_mfma_f32_16x16x32_bf16 v[88:91], v[44:47], v[240:243], v[88:91]\n"
"ds_read_b64_tr_b16 v[38:39], v4 offset:45568\n"
"s_add_u32 m0, s77, 49152\n"
"s_add_u32 s46, s4, 4096\n"
"buffer_load_dwordx4 v2, s[52:55], s46 offen lds\n"
"s_waitcnt lgkmcnt(6)\n"
"v_mfma_f32_16x16x32_bf16 v[96:99], v[24:27], v[240:243], v[96:99]\n"
"ds_read_b64_tr_b16 v[40:41], v5 offset:32768\n"
"v_add_f32 v9, 1.0, v9\n"
"v_mfma_f32_16x16x32_bf16 v[100:103], v[24:27], v[244:247], v[100:103]\n"
"ds_read_b64_tr_b16 v[42:43], v5 offset:33280\n"
"s_waitcnt lgkmcnt(6)\n"
"v_mfma_f32_16x16x32_bf16 v[108:111], v[28:31], v[244:247], v[108:111]\n"
"ds_read_b64_tr_b16 v[44:45], v5 offset:45056\n"
"v_rcp_f32 v8, v8\n"
"v_mfma_f32_16x16x32_bf16 v[104:107], v[28:31], v[240:243], v[104:107]\n"
"ds_read_b64_tr_b16 v[46:47], v5 offset:45568\n"
"s_waitcnt vmcnt(3)\n"
"s_barrier\n"
"s_waitcnt lgkmcnt(6)\n"
"v_mfma_f32_16x16x32_bf16 v[80:83], v[32:35], v[248:251], v[80:83]\n"
"v_add_u32 v4, 98304, v22\n"
"ds_read_b64_tr_b16 v[24:25], v4\n"
"s_setprio 3\n"
"v_rcp_f32 v9, v9\n"
"v_mfma_f32_16x16x32_bf16 v[84:87], v[32:35], v[252:255], v[84:87]\n"
"v_xor_b32 v5, 128, v4\n"
"ds_read_b64_tr_b16 v[26:27], v5\n"
"s_add_u32 m0, s77, 53248\n"
"s_add_u32 s46, s4, 299008\n"
"buffer_load_dwordx4 v2, s[52:55], s46 offen lds\n"
"s_waitcnt lgkmcnt(6)\n"
"v_mfma_f32_16x16x32_bf16 v[92:95], v[36:39], v[252:255], v[92:95]\n"
"ds_read_b64_tr_b16 v[28:29], v4 offset:12288\n"
"v_mul_f32 v8, v8, v78\n"
"v_mfma_f32_16x16x32_bf16 v[88:91], v[36:39], v[248:251], v[88:91]\n"
"ds_read_b64_tr_b16 v[30:31], v5 offset:12288\n"
"s_waitcnt lgkmcnt(6)\n"
"v_mfma_f32_16x16x32_bf16 v[96:99], v[40:43], v[248:251], v[96:99]\n"
"ds_read_b64_tr_b16 v[32:33], v4 offset:1024\n"
"v_mul_f32 v9, v9, v79\n"
"v_mfma_f32_16x16x32_bf16 v[100:103], v[40:43], v[252:255], v[100:103]\n"
"ds_read_b64_tr_b16 v[34:35], v5 offset:1024\n"
"s_add_u32 m0, s77, 57344\n"
"s_add_u32 s46, s4, 593920\n"
"buffer_load_dwordx4 v2, s[52:55], s46 offen lds\n"
"s_waitcnt lgkmcnt(6)\n"
"v_mfma_f32_16x16x32_bf16 v[108:111], v[44:47], v[252:255], v[108:111]\n"
"ds_read_b64_tr_b16 v[36:37], v4 offset:13312\n"
"v_cvt_pk_bf16_f32 v55, v8, v9\n"
"v_mfma_f32_16x16x32_bf16 v[104:107], v[44:47], v[248:251], v[104:107]\n"
"ds_read_b64_tr_b16 v[38:39], v5 offset:13312\n"
"s_add_u32 s46, s9, 147456\n"
"buffer_store_dwordx4 v[52:55], v1, s[48:51], s46 offen sc1\n"
"s_waitcnt lgkmcnt(6)\n"
"v_mfma_f32_16x16x32_bf16 v[48:51], v[24:27], v[112:115], 0\n"
"ds_read_b64_tr_b16 v[40:41], v4 offset:2048\n"
"v_exp_f32 v8, -v80\n"
"v_mfma_f32_16x16x32_bf16 v[52:55], v[24:27], v[116:119], 0\n"
"ds_read_b64_tr_b16 v[42:43], v5 offset:2048\n"
"v_exp_f32 v9, -v81\n"
"s_waitcnt lgkmcnt(6)\n"
"v_mfma_f32_16x16x32_bf16 v[60:63], v[28:31], v[116:119], 0\n"
"ds_read_b64_tr_b16 v[44:45], v4 offset:14336\n"
"v_add_f32 v8, 1.0, v8\n"
"v_mfma_f32_16x16x32_bf16 v[56:59], v[28:31], v[112:115], 0\n"
"ds_read_b64_tr_b16 v[46:47], v5 offset:14336\n"
"v_add_f32 v9, 1.0, v9\n"
"s_waitcnt lgkmcnt(6)\n"
"v_mfma_f32_16x16x32_bf16 v[48:51], v[32:35], v[120:123], v[48:51]\n"
"ds_read_b64_tr_b16 v[24:25], v4 offset:3072\n"
"s_setprio 2\n"
"v_rcp_f32 v8, v8\n"
"v_mfma_f32_16x16x32_bf16 v[52:55], v[32:35], v[124:127], v[52:55]\n"
"ds_read_b64_tr_b16 v[26:27], v5 offset:3072\n"
"v_rcp_f32 v9, v9\n"
"s_add_u32 m0, s77, 73728\n"
"s_add_u32 s46, s4, 888832\n"
"buffer_load_dwordx4 v2, s[52:55], s46 offen lds\n"
"s_waitcnt lgkmcnt(6)\n"
"v_mfma_f32_16x16x32_bf16 v[60:63], v[36:39], v[124:127], v[60:63]\n"
"ds_read_b64_tr_b16 v[28:29], v4 offset:15360\n"
"v_mul_f32 v8, v8, v88\n"
"v_mfma_f32_16x16x32_bf16 v[56:59], v[36:39], v[120:123], v[56:59]\n"
"ds_read_b64_tr_b16 v[30:31], v5 offset:15360\n"
"v_mul_f32 v9, v9, v89\n"
"s_waitcnt lgkmcnt(6)\n"
"v_mfma_f32_16x16x32_bf16 v[48:51], v[40:43], v[128:131], v[48:51]\n"
"ds_read_b64_tr_b16 v[32:33], v4 offset:4096\n"
"v_cvt_pk_bf16_f32 v80, v8, v9\n"
"v_mfma_f32_16x16x32_bf16 v[52:55], v[40:43], v[132:135], v[52:55]\n"
"ds_read_b64_tr_b16 v[34:35], v5 offset:4096\n"
"v_exp_f32 v8, -v82\n"
"s_add_u32 m0, s77, 77824\n"
"s_add_u32 s46, s4, 1183744\n"
"buffer_load_dwordx4 v2, s[52:55], s46 offen lds\n"
"s_waitcnt lgkmcnt(6)\n"
"v_mfma_f32_16x16x32_bf16 v[60:63], v[44:47], v[132:135], v[60:63]\n"
"ds_read_b64_tr_b16 v[36:37], v4 offset:16384\n"
"v_exp_f32 v9, -v83\n"
"v_mfma_f32_16x16x32_bf16 v[56:59], v[44:47], v[128:131], v[56:59]\n"
"ds_read_b64_tr_b16 v[38:39], v5 offset:16384\n"
"v_add_f32 v8, 1.0, v8\n"
"s_waitcnt lgkmcnt(6)\n"
"v_mfma_f32_16x16x32_bf16 v[48:51], v[24:27], v[136:139], v[48:51]\n"
"ds_read_b64_tr_b16 v[40:41], v4 offset:5120\n"
"v_add_f32 v9, 1.0, v9\n"
"v_mfma_f32_16x16x32_bf16 v[52:55], v[24:27], v[140:143], v[52:55]\n"
"ds_read_b64_tr_b16 v[42:43], v5 offset:5120\n"
"v_rcp_f32 v8, v8\n"
"s_add_u32 m0, s77, 81920\n"
"s_add_u32 s46, s4, 1478656\n"
"buffer_load_dwordx4 v2, s[52:55], s46 offen lds\n"
"s_waitcnt lgkmcnt(6)\n"
"v_mfma_f32_16x16x32_bf16 v[60:63], v[28:31], v[140:143], v[60:63]\n"
"ds_read_b64_tr_b16 v[44:45], v4 offset:17408\n"
"v_rcp_f32 v9, v9\n"
"v_mfma_f32_16x16x32_bf16 v[56:59], v[28:31], v[136:139], v[56:59]\n"
"ds_read_b64_tr_b16 v[46:47], v5 offset:17408\n"
"v_mul_f32 v8, v8, v90\n"
"s_waitcnt lgkmcnt(6)\n"
"v_mfma_f32_16x16x32_bf16 v[48:51], v[32:35], v[144:147], v[48:51]\n"
"ds_read_b64_tr_b16 v[24:25], v4 offset:6144\n"
"s_setprio 1\n"
"v_mul_f32 v9, v9, v91\n"
"v_mfma_f32_16x16x32_bf16 v[52:55], v[32:35], v[148:151], v[52:55]\n"
"ds_read_b64_tr_b16 v[26:27], v5 offset:6144\n"
"v_cvt_pk_bf16_f32 v81, v8, v9\n"
"s_waitcnt lgkmcnt(6)\n"
"v_mfma_f32_16x16x32_bf16 v[60:63], v[36:39], v[148:151], v[60:63]\n"
"ds_read_b64_tr_b16 v[28:29], v4 offset:18432\n"
"v_exp_f32 v8, -v96\n"
"v_mfma_f32_16x16x32_bf16 v[56:59], v[36:39], v[144:147], v[56:59]\n"
"ds_read_b64_tr_b16 v[30:31], v5 offset:18432\n"
"v_exp_f32 v9, -v97\n"
"s_add_u32 m0, s77, 0\n"
"s_add_u32 s46, s4, 1773568\n"
"buffer_load_dwordx4 v2, s[52:55], s46 offen lds\n"
"s_waitcnt lgkmcnt(6)\n"
"v_mfma_f32_16x16x32_bf16 v[48:51], v[40:43], v[152:155], v[48:51]\n"
"ds_read_b64_tr_b16 v[32:33], v4 offset:7168\n"
"v_add_f32 v8, 1.0, v8\n"
"v_mfma_f32_16x16x32_bf16 v[52:55], v[40:43], v[156:159], v[52:55]\n"
"ds_read_b64_tr_b16 v[34:35], v5 offset:7168\n"
"v_add_f32 v9, 1.0, v9\n"
"s_waitcnt lgkmcnt(6)\n"
"v_mfma_f32_16x16x32_bf16 v[60:63], v[44:47], v[156:159], v[60:63]\n"
"ds_read_b64_tr_b16 v[36:37], v4 offset:19456\n"
"v_rcp_f32 v8, v8\n"
"v_mfma_f32_16x16x32_bf16 v[56:59], v[44:47], v[152:155], v[56:59]\n"
"ds_read_b64_tr_b16 v[38:39], v5 offset:19456\n"
"v_rcp_f32 v9, v9\n"
"s_add_u32 m0, s77, 4096\n"
"s_add_u32 s46, s4, 2068480\n"
"buffer_load_dwordx4 v2, s[52:55], s46 offen lds\n"
"s_waitcnt lgkmcnt(6)\n"
"v_mfma_f32_16x16x32_bf16 v[48:51], v[24:27], v[160:163], v[48:51]\n"
"ds_read_b64_tr_b16 v[40:41], v4 offset:8192\n"
"v_mul_f32 v8, v8, v104\n"
"v_mfma_f32_16x16x32_bf16 v[52:55], v[24:27], v[164:167], v[52:55]\n"
"ds_read_b64_tr_b16 v[42:43], v5 offset:8192\n"
"v_mul_f32 v9, v9, v105\n"
"s_waitcnt lgkmcnt(6)\n"
"v_mfma_f32_16x16x32_bf16 v[60:63], v[28:31], v[164:167], v[60:63]\n"
"ds_read_b64_tr_b16 v[44:45], v4 offset:20480\n"
"v_cvt_pk_bf16_f32 v82, v8, v9\n"
"v_mfma_f32_16x16x32_bf16 v[56:59], v[28:31], v[160:163], v[56:59]\n"
"ds_read_b64_tr_b16 v[46:47], v5 offset:20480\n"
"v_exp_f32 v8, -v98\n"
"s_add_u32 m0, s77, 8192\n"
"s_add_u32 s46, s4, 2363392\n"
"buffer_load_dwordx4 v2, s[52:55], s46 offen lds\n"
"s_waitcnt lgkmcnt(6)\n"
"v_mfma_f32_16x16x32_bf16 v[48:51], v[32:35], v[168:171], v[48:51]\n"
"ds_read_b64_tr_b16 v[24:25], v4 offset:9216\n"
"s_setprio 0\n"
"v_exp_f32 v9, -v99\n"
"v_mfma_f32_16x16x32_bf16 v[52:55], v[32:35], v[172:175], v[52:55]\n"
"ds_read_b64_tr_b16 v[26:27], v5 offset:9216\n"
"v_add_f32 v8, 1.0, v8\n"
"s_waitcnt lgkmcnt(6)\n"
"v_mfma_f32_16x16x32_bf16 v[60:63], v[36:39], v[172:175], v[60:63]\n"
"ds_read_b64_tr_b16 v[28:29], v4 offset:21504\n"
"v_add_f32 v9, 1.0, v9\n"
"v_mfma_f32_16x16x32_bf16 v[56:59], v[36:39], v[168:171], v[56:59]\n"
"ds_read_b64_tr_b16 v[30:31], v5 offset:21504\n"
"v_rcp_f32 v8, v8\n"
"s_waitcnt lgkmcnt(6)\n"
"v_mfma_f32_16x16x32_bf16 v[48:51], v[40:43], v[176:179], v[48:51]\n"
"ds_read_b64_tr_b16 v[32:33], v4 offset:10240\n"
"v_rcp_f32 v9, v9\n"
"v_mfma_f32_16x16x32_bf16 v[52:55], v[40:43], v[180:183], v[52:55]\n"
"ds_read_b64_tr_b16 v[34:35], v5 offset:10240\n"
"v_mul_f32 v8, v8, v106\n"
"s_waitcnt lgkmcnt(6)\n"
"v_mfma_f32_16x16x32_bf16 v[60:63], v[44:47], v[180:183], v[60:63]\n"
"ds_read_b64_tr_b16 v[36:37], v4 offset:22528\n"
"v_mul_f32 v9, v9, v107\n"
"v_mfma_f32_16x16x32_bf16 v[56:59], v[44:47], v[176:179], v[56:59]\n"
"ds_read_b64_tr_b16 v[38:39], v5 offset:22528\n"
"v_cvt_pk_bf16_f32 v83, v8, v9\n"
"s_add_u32 s46, s9, 1024\n"
"buffer_store_dwordx4 v[80:83], v1, s[48:51], s46 offen sc1\n"
"s_waitcnt lgkmcnt(6)\n"
"v_mfma_f32_16x16x32_bf16 v[48:51], v[24:27], v[184:187], v[48:51]\n"
"ds_read_b64_tr_b16 v[40:41], v4 offset:11264\n"
"v_exp_f32 v8, -v84\n"
"v_mfma_f32_16x16x32_bf16 v[52:55], v[24:27], v[188:191], v[52:55]\n"
"ds_read_b64_tr_b16 v[42:43], v5 offset:11264\n"
"v_exp_f32 v9, -v85\n"
"s_waitcnt lgkmcnt(6)\n"
"v_mfma_f32_16x16x32_bf16 v[60:63], v[28:31], v[188:191], v[60:63]\n"
"ds_read_b64_tr_b16 v[44:45], v4 offset:23552\n"
"v_add_f32 v8, 1.0, v8\n"
"v_mfma_f32_16x16x32_bf16 v[56:59], v[28:31], v[184:187], v[56:59]\n"
"ds_read_b64_tr_b16 v[46:47], v5 offset:23552\n"
"v_add_f32 v9, 1.0, v9\n"
"s_waitcnt vmcnt(8)\n"
"s_barrier\n"
"s_waitcnt lgkmcnt(6)\n"
"v_mfma_f32_16x16x32_bf16 v[48:51], v[32:35], v[192:195], v[48:51]\n"
"ds_read_b64_tr_b16 v[24:25], v4 offset:24576\n"
"s_setprio 3\n"
"v_rcp_f32 v8, v8\n"
"v_mfma_f32_16x16x32_bf16 v[52:55], v[32:35], v[196:199], v[52:55]\n"
"ds_read_b64_tr_b16 v[26:27], v5 offset:24576\n"
"v_rcp_f32 v9, v9\n"
"s_waitcnt lgkmcnt(6)\n"
"v_mfma_f32_16x16x32_bf16 v[60:63], v[36:39], v[196:199], v[60:63]\n"
"ds_read_b64_tr_b16 v[28:29], v4 offset:36864\n"
"v_mul_f32 v8, v8, v92\n"
"v_mfma_f32_16x16x32_bf16 v[56:59], v[36:39], v[192:195], v[56:59]\n"
"ds_read_b64_tr_b16 v[30:31], v5 offset:36864\n"
"v_mul_f32 v9, v9, v93\n"
"s_waitcnt lgkmcnt(6)\n"
"v_mfma_f32_16x16x32_bf16 v[48:51], v[40:43], v[200:203], v[48:51]\n"
"ds_read_b64_tr_b16 v[32:33], v4 offset:25600\n"
"v_cvt_pk_bf16_f32 v84, v8, v9\n"
"v_mfma_f32_16x16x32_bf16 v[52:55], v[40:43], v[204:207], v[52:55]\n"
"ds_read_b64_tr_b16 v[34:35], v5 offset:25600\n"
"v_exp_f32 v8, -v86\n"
"s_add_u32 m0, s77, 24576\n"
"s_add_u32 s46, s97, 2658304\n"
"buffer_load_dwordx4 v2, s[52:55], s46 offen lds\n"
"s_waitcnt lgkmcnt(6)\n"
"v_mfma_f32_16x16x32_bf16 v[60:63], v[44:47], v[204:207], v[60:63]\n"
"ds_read_b64_tr_b16 v[36:37], v4 offset:37888\n"
"v_exp_f32 v9, -v87\n"
"v_mfma_f32_16x16x32_bf16 v[56:59], v[44:47], v[200:203], v[56:59]\n"
"ds_read_b64_tr_b16 v[38:39], v5 offset:37888\n"
"v_add_f32 v8, 1.0, v8\n"
"s_waitcnt lgkmcnt(6)\n"
"v_mfma_f32_16x16x32_bf16 v[48:51], v[24:27], v[208:211], v[48:51]\n"
"ds_read_b64_tr_b16 v[40:41], v4 offset:26624\n"
"s_setprio 2\n"
"v_add_f32 v9, 1.0, v9\n"
"v_mfma_f32_16x16x32_bf16 v[52:55], v[24:27], v[212:215], v[52:55]\n"
"ds_read_b64_tr_b16 v[42:43], v5 offset:26624\n"
"v_lshrrev_b32 v3, 4, v0\n"
"v_lshlrev_b32 v22, 10, v3\n"
"v_rcp_f32 v8, v8\n"
"s_add_u32 m0, s77, 28672\n"
"s_add_u32 s46, s97, 2953216\n"
"buffer_load_dwordx4 v2, s[52:55], s46 offen lds\n"
"s_waitcnt lgkmcnt(6)\n"
"v_mfma_f32_16x16x32_bf16 v[60:63], v[28:31], v[212:215], v[60:63]\n"
"ds_read_b64_tr_b16 v[44:45], v4 offset:38912\n"
"v_rcp_f32 v9, v9\n"
"v_mfma_f32_16x16x32_bf16 v[56:59], v[28:31], v[208:211], v[56:59]\n"
"ds_read_b64_tr_b16 v[46:47], v5 offset:38912\n"
"v_bfe_u32 v3, v0, 3, 2\n"
"v_lshl_add_u32 v22, v3, 5, v22\n"
"v_mul_f32 v8, v8, v94\n"
"s_waitcnt lgkmcnt(6)\n"
"v_mfma_f32_16x16x32_bf16 v[48:51], v[32:35], v[216:219], v[48:51]\n"
"ds_read_b64_tr_b16 v[24:25], v4 offset:27648\n"
"v_mul_f32 v9, v9, v95\n"
"v_mfma_f32_16x16x32_bf16 v[52:55], v[32:35], v[220:223], v[52:55]\n"
"ds_read_b64_tr_b16 v[26:27], v5 offset:27648\n"
"v_bfe_u32 v3, v0, 2, 2\n"
"v_lshl_add_u32 v22, v3, 7, v22\n"
"v_cvt_pk_bf16_f32 v85, v8, v9\n"
"s_waitcnt lgkmcnt(6)\n"
"v_mfma_f32_16x16x32_bf16 v[60:63], v[36:39], v[220:223], v[60:63]\n"
"ds_read_b64_tr_b16 v[28:29], v4 offset:39936\n"
"v_exp_f32 v8, -v100\n"
"v_mfma_f32_16x16x32_bf16 v[56:59], v[36:39], v[216:219], v[56:59]\n"
"ds_read_b64_tr_b16 v[30:31], v5 offset:39936\n"
"v_and_b32 v3, 3, v0\n"
"v_lshl_add_u32 v22, v3, 3, v22\n"
"v_exp_f32 v9, -v101\n"
"s_add_u32 m0, s77, 32768\n"
"s_add_u32 s46, s97, 3248128\n"
"buffer_load_dwordx4 v2, s[52:55], s46 offen lds\n"
"s_waitcnt lgkmcnt(6)\n"
"v_mfma_f32_16x16x32_bf16 v[48:51], v[40:43], v[224:227], v[48:51]\n"
"ds_read_b64_tr_b16 v[32:33], v4 offset:28672\n"
"s_setprio 1\n"
"v_add_f32 v8, 1.0, v8\n"
"v_mfma_f32_16x16x32_bf16 v[52:55], v[40:43], v[228:231], v[52:55]\n"
"ds_read_b64_tr_b16 v[34:35], v5 offset:28672\n"
"v_add_f32 v9, 1.0, v9\n"
"s_waitcnt lgkmcnt(6)\n"
"v_mfma_f32_16x16x32_bf16 v[60:63], v[44:47], v[228:231], v[60:63]\n"
"ds_read_b64_tr_b16 v[36:37], v4 offset:40960\n"
"v_rcp_f32 v8, v8\n"
"v_mfma_f32_16x16x32_bf16 v[56:59], v[44:47], v[224:227], v[56:59]\n"
"ds_read_b64_tr_b16 v[38:39], v5 offset:40960\n"
"v_rcp_f32 v9, v9\n"
"s_waitcnt lgkmcnt(6)\n"
"v_mfma_f32_16x16x32_bf16 v[48:51], v[24:27], v[232:235], v[48:51]\n"
"ds_read_b64_tr_b16 v[40:41], v4 offset:29696\n"
"v_mul_f32 v8, v8, v108\n"
"v_mfma_f32_16x16x32_bf16 v[52:55], v[24:27], v[236:239], v[52:55]\n"
"ds_read_b64_tr_b16 v[42:43], v5 offset:29696\n"
"v_mul_f32 v9, v9, v109\n"
"s_waitcnt lgkmcnt(6)\n"
"v_mfma_f32_16x16x32_bf16 v[60:63], v[28:31], v[236:239], v[60:63]\n"
"ds_read_b64_tr_b16 v[44:45], v4 offset:41984\n"
"v_cvt_pk_bf16_f32 v86, v8, v9\n"
"v_mfma_f32_16x16x32_bf16 v[56:59], v[28:31], v[232:235], v[56:59]\n"
"ds_read_b64_tr_b16 v[46:47], v5 offset:41984\n"
"v_exp_f32 v8, -v102\n"
"s_waitcnt lgkmcnt(6)\n"
"v_mfma_f32_16x16x32_bf16 v[48:51], v[32:35], v[240:243], v[48:51]\n"
"v_add_u32 v4, 49152, v22\n"
"ds_read_b64_tr_b16 v[24:25], v4\n"
"s_setprio 0\n"
"v_exp_f32 v9, -v103\n"
"v_mfma_f32_16x16x32_bf16 v[52:55], v[32:35], v[244:247], v[52:55]\n"
"ds_read_b64_tr_b16 v[26:27], v4 offset:512\n"
"v_add_f32 v8, 1.0, v8\n"
"s_waitcnt lgkmcnt(6)\n"
"v_mfma_f32_16x16x32_bf16 v[60:63], v[36:39], v[244:247], v[60:63]\n"
"ds_read_b64_tr_b16 v[28:29], v4 offset:12288\n"
"v_add_f32 v9, 1.0, v9\n"
"v_mfma_f32_16x16x32_bf16 v[56:59], v[36:39], v[240:243], v[56:59]\n"
"ds_read_b64_tr_b16 v[30:31], v4 offset:12800\n"
"v_rcp_f32 v8, v8\n"
"s_waitcnt lgkmcnt(6)\n"
"v_mfma_f32_16x16x32_bf16 v[48:51], v[40:43], v[248:251], v[48:51]\n"
"v_xor_b32 v5, 32, v4\n"
"ds_read_b64_tr_b16 v[32:33], v5\n"
"v_rcp_f32 v9, v9\n"
"v_mfma_f32_16x16x32_bf16 v[52:55], v[40:43], v[252:255], v[52:55]\n"
"ds_read_b64_tr_b16 v[34:35], v5 offset:512\n"
"v_mul_f32 v8, v8, v110\n"
"s_waitcnt lgkmcnt(6)\n"
"v_mfma_f32_16x16x32_bf16 v[60:63], v[44:47], v[252:255], v[60:63]\n"
"ds_read_b64_tr_b16 v[36:37], v5 offset:12288\n"
"v_mul_f32 v9, v9, v111\n"
"v_mfma_f32_16x16x32_bf16 v[56:59], v[44:47], v[248:251], v[56:59]\n"
"ds_read_b64_tr_b16 v[38:39], v5 offset:12800\n"
"v_cvt_pk_bf16_f32 v87, v8, v9\n"
"s_add_u32 s46, s9, 148480\n"
"buffer_store_dwordx4 v[84:87], v1, s[48:51], s46 offen sc1\n"
"s_waitcnt vmcnt(1)\n"
"s_barrier\n"
"s_branch .Lsk7_0\n"
".Lst7_0:\n"
"s_branch .Lbk7\n"
".Lsk7_0:\n"
"s_waitcnt lgkmcnt(6)\n"
"v_mfma_f32_16x16x32_bf16 v[80:83], v[24:27], v[112:115], 0\n"
"ds_read_b64_tr_b16 v[40:41], v4 offset:4096\n"
"s_setprio 3\n"
"v_exp_f32 v8, -v48\n"
"v_mfma_f32_16x16x32_bf16 v[84:87], v[24:27], v[116:119], 0\n"
"ds_read_b64_tr_b16 v[42:43], v4 offset:4608\n"
"s_waitcnt lgkmcnt(6)\n"
"v_mfma_f32_16x16x32_bf16 v[92:95], v[28:31], v[116:119], 0\n"
"ds_read_b64_tr_b16 v[44:45], v4 offset:16384\n"
"v_exp_f32 v9, -v49\n"
"v_mfma_f32_16x16x32_bf16 v[88:91], v[28:31], v[112:115], 0\n"
"ds_read_b64_tr_b16 v[46:47], v4 offset:16896\n"
"s_waitcnt lgkmcnt(6)\n"
"v_mfma_f32_16x16x32_bf16 v[96:99], v[32:35], v[112:115], 0\n"
"ds_read_b64_tr_b16 v[24:25], v5 offset:4096\n"
"v_add_f32 v8, 1.0, v8\n"
"v_mfma_f32_16x16x32_bf16 v[100:103], v[32:35], v[116:119], 0\n"
"ds_read_b64_tr_b16 v[26:27], v5 offset:4608\n"
"s_add_u32 m0, s77, 98304\n"
"s_add_u32 s46, s97, 3543040\n"
"buffer_load_dwordx4 v2, s[52:55], s46 offen lds\n"
"s_waitcnt lgkmcnt(6)\n"
"v_mfma_f32_16x16x32_bf16 v[108:111], v[36:39], v[116:119], 0\n"
"ds_read_b64_tr_b16 v[28:29], v5 offset:16384\n"
"v_add_f32 v9, 1.0, v9\n"
"v_mfma_f32_16x16x32_bf16 v[104:107], v[36:39], v[112:115], 0\n"
"ds_read_b64_tr_b16 v[30:31], v5 offset:16896\n"
"s_waitcnt lgkmcnt(6)\n"
"v_mfma_f32_16x16x32_bf16 v[80:83], v[40:43], v[120:123], v[80:83]\n"
"ds_read_b64_tr_b16 v[32:33], v4 offset:8192\n"
"v_rcp_f32 v8, v8\n"
"v_mfma_f32_16x16x32_bf16 v[84:87], v[40:43], v[124:127], v[84:87]\n"
"ds_read_b64_tr_b16 v[34:35], v4 offset:8704\n"
"s_add_u32 m0, s77, 102400\n"
"s_add_u32 s46, s97, 3837952\n"
"buffer_load_dwordx4 v2, s[52:55], s46 offen lds\n"
"s_waitcnt lgkmcnt(6)\n"
"v_mfma_f32_16x16x32_bf16 v[92:95], v[44:47], v[124:127], v[92:95]\n"
"ds_read_b64_tr_b16 v[36:37], v4 offset:20480\n"
"v_rcp_f32 v9, v9\n"
"v_mfma_f32_16x16x32_bf16 v[88:91], v[44:47], v[120:123], v[88:91]\n"
"ds_read_b64_tr_b16 v[38:39], v4 offset:20992\n"
"s_waitcnt lgkmcnt(6)\n"
"v_mfma_f32_16x16x32_bf16 v[96:99], v[24:27], v[120:123], v[96:99]\n"
"ds_read_b64_tr_b16 v[40:41], v5 offset:8192\n"
"v_mul_f32 v8, v8, v56\n"
"v_mfma_f32_16x16x32_bf16 v[100:103], v[24:27], v[124:127], v[100:103]\n"
"ds_read_b64_tr_b16 v[42:43], v5 offset:8704\n"
"s_waitcnt lgkmcnt(6)\n"
"v_mfma_f32_16x16x32_bf16 v[108:111], v[28:31], v[124:127], v[108:111]\n"
"ds_read_b64_tr_b16 v[44:45], v5 offset:20480\n"
"v_mul_f32 v9, v9, v57\n"
"v_mfma_f32_16x16x32_bf16 v[104:107], v[28:31], v[120:123], v[104:107]\n"
"ds_read_b64_tr_b16 v[46:47], v5 offset:20992\n"
"s_add_u32 m0, s77, 106496\n"
"s_add_u32 s46, s97, 4132864\n"
"buffer_load_dwordx4 v2, s[52:55], s46 offen lds\n"
"s_waitcnt lgkmcnt(6)\n"
"v_mfma_f32_16x16x32_bf16 v[80:83], v[32:35], v[128:131], v[80:83]\n"
"ds_read_b64_tr_b16 v[24:25], v4 offset:24576\n"
"v_cvt_pk_bf16_f32 v48, v8, v9\n"
"v_mfma_f32_16x16x32_bf16 v[84:87], v[32:35], v[132:135], v[84:87]\n"
"ds_read_b64_tr_b16 v[26:27], v4 offset:25088\n"
"s_waitcnt lgkmcnt(6)\n"
"v_mfma_f32_16x16x32_bf16 v[92:95], v[36:39], v[132:135], v[92:95]\n"
"ds_read_b64_tr_b16 v[28:29], v4 offset:36864\n"
"v_exp_f32 v8, -v50\n"
"v_mfma_f32_16x16x32_bf16 v[88:91], v[36:39], v[128:131], v[88:91]\n"
"ds_read_b64_tr_b16 v[30:31], v4 offset:37376\n"
"s_add_u32 m0, s77, 122880\n"
"s_add_u32 s46, s97, 4427776\n"
"buffer_load_dwordx4 v2, s[52:55], s46 offen lds\n"
"s_waitcnt lgkmcnt(6)\n"
"v_mfma_f32_16x16x32_bf16 v[96:99], v[40:43], v[128:131], v[96:99]\n"
"ds_read_b64_tr_b16 v[32:33], v5 offset:24576\n"
"v_exp_f32 v9, -v51\n"
"v_mfma_f32_16x16x32_bf16 v[100:103], v[40:43], v[132:135], v[100:103]\n"
"ds_read_b64_tr_b16 v[34:35], v5 offset:25088\n"
"s_waitcnt lgkmcnt(6)\n"
"v_mfma_f32_16x16x32_bf16 v[108:111], v[44:47], v[132:135], v[108:111]\n"
"ds_read_b64_tr_b16 v[36:37], v5 offset:36864\n"
"s_setprio 2\n"
"v_add_f32 v8, 1.0, v8\n"
"v_mfma_f32_16x16x32_bf16 v[104:107], v[44:47], v[128:131], v[104:107]\n"
"ds_read_b64_tr_b16 v[38:39], v5 offset:37376\n"
"s_or_b32 s45, s36, 2\n"
"v_mov_b32 v16, s45\n"
"s_add_u32 m0, s77, 126976\n"
"s_add_u32 s46, s97, 4722688\n"
"buffer_load_dwordx4 v2, s[52:55], s46 offen lds\n"
"s_waitcnt lgkmcnt(6)\n"
"v_mfma_f32_16x16x32_bf16 v[80:83], v[24:27], v[136:139], v[80:83]\n"
"ds_read_b64_tr_b16 v[40:41], v4 offset:28672\n"
"v_add_f32 v9, 1.0, v9\n"
"v_mfma_f32_16x16x32_bf16 v[84:87], v[24:27], v[140:143], v[84:87]\n"
"ds_read_b64_tr_b16 v[42:43], v4 offset:29184\n"
"v_mov_b32 v17, s37\n"
"s_waitcnt lgkmcnt(6)\n"
"v_mfma_f32_16x16x32_bf16 v[92:95], v[28:31], v[140:143], v[92:95]\n"
"ds_read_b64_tr_b16 v[44:45], v4 offset:40960\n"
"v_rcp_f32 v8, v8\n"
"v_mfma_f32_16x16x32_bf16 v[88:91], v[28:31], v[136:139], v[88:91]\n"
"ds_read_b64_tr_b16 v[46:47], v4 offset:41472\n"
"v_lshrrev_b32 v18, 4, v0\n"
"v_lshlrev_b32 v18, 28, v18\n"
"v_lshl_add_u32 v18, v0, 3, v18\n"
"s_mul_i32 s45, s27, 72\n"
"s_mul_i32 s42, s24, 9\n"
"s_add_u32 s45, s45, s42\n"
"v_add_u32 v21, s45, v0\n"
"s_add_u32 m0, s77, 131072\n"
"s_add_u32 s46, s97, 5017600\n"
"buffer_load_dwordx4 v2, s[52:55], s46 offen lds\n"
"s_waitcnt lgkmcnt(6)\n"
"v_mfma_f32_16x16x32_bf16 v[96:99], v[32:35], v[136:139], v[96:99]\n"
"ds_read_b64_tr_b16 v[24:25], v5 offset:28672\n"
"v_rcp_f32 v9, v9\n"
"v_mfma_f32_16x16x32_bf16 v[100:103], v[32:35], v[140:143], v[100:103]\n"
"ds_read_b64_tr_b16 v[26:27], v5 offset:29184\n"
"s_add_u32 s45, s79, 1048576\n"
"s_waitcnt vmcnt(6)\n"
"buffer_store_dwordx2 v[16:17], v18, s[48:51], s45 offen sc1\n"
"v_lshlrev_b32 v21, 7, v21\n"
"s_waitcnt lgkmcnt(6)\n"
"v_mfma_f32_16x16x32_bf16 v[108:111], v[36:39], v[140:143], v[108:111]\n"
"ds_read_b64_tr_b16 v[28:29], v5 offset:40960\n"
"v_mul_f32 v8, v8, v58\n"
"v_mfma_f32_16x16x32_bf16 v[104:107], v[36:39], v[136:139], v[104:107]\n"
"ds_read_b64_tr_b16 v[30:31], v5 offset:41472\n"
"v_sub_u32 v3, 8, v0\n"
"v_and_b32 v3, 0x80000000, v3\n"
"v_or_b32 v21, v21, v3\n"
"s_waitcnt lgkmcnt(6)\n"
"v_mfma_f32_16x16x32_bf16 v[80:83], v[40:43], v[144:147], v[80:83]\n"
"ds_read_b64_tr_b16 v[32:33], v4 offset:32768\n"
"v_mul_f32 v9, v9, v59\n"
"v_mfma_f32_16x16x32_bf16 v[84:87], v[40:43], v[148:151], v[84:87]\n"
"ds_read_b64_tr_b16 v[34:35], v4 offset:33280\n"
"s_mul_i32 s45, s30, 589824\n"
"buffer_load_dword v14, v21, s[56:59], s45 offen\n"
"s_waitcnt lgkmcnt(6)\n"
"v_mfma_f32_16x16x32_bf16 v[92:95], v[44:47], v[148:151], v[92:95]\n"
"ds_read_b64_tr_b16 v[36:37], v4 offset:45056\n"
"v_cvt_pk_bf16_f32 v49, v8, v9\n"
"v_mfma_f32_16x16x32_bf16 v[88:91], v[44:47], v[144:147], v[88:91]\n"
"ds_read_b64_tr_b16 v[38:39], v4 offset:45568\n"
"s_mul_i32 s45, s30, 589824\n"
"s_add_u32 s45, s45, 294912\n"
"buffer_load_dword v20, v21, s[56:59], s45 offen\n"
"s_waitcnt lgkmcnt(6)\n"
"v_mfma_f32_16x16x32_bf16 v[96:99], v[24:27], v[144:147], v[96:99]\n"
"ds_read_b64_tr_b16 v[40:41], v5 offset:32768\n"
"v_exp_f32 v8, -v52\n"
"v_mfma_f32_16x16x32_bf16 v[100:103], v[24:27], v[148:151], v[100:103]\n"
"ds_read_b64_tr_b16 v[42:43], v5 offset:33280\n"
"s_mul_i32 s45, s30, 589824\n"
"s_add_u32 s45, s45, 2359296\n"
"buffer_load_dword v12, v21, s[56:59], s45 offen\n"
"s_waitcnt lgkmcnt(6)\n"
"v_mfma_f32_16x16x32_bf16 v[108:111], v[28:31], v[148:151], v[108:111]\n"
"ds_read_b64_tr_b16 v[44:45], v5 offset:45056\n"
"v_exp_f32 v9, -v53\n"
"v_mfma_f32_16x16x32_bf16 v[104:107], v[28:31], v[144:147], v[104:107]\n"
"ds_read_b64_tr_b16 v[46:47], v5 offset:45568\n"
"s_mul_i32 s45, s30, 589824\n"
"s_add_u32 s45, s45, 2654208\n"
"buffer_load_dword v13, v21, s[56:59], s45 offen\n"
"s_waitcnt lgkmcnt(6)\n"
"v_mfma_f32_16x16x32_bf16 v[80:83], v[32:35], v[152:155], v[80:83]\n"
"v_add_u32 v4, 0, v22\n"
"ds_read_b64_tr_b16 v[24:25], v4\n"
"v_add_f32 v8, 1.0, v8\n"
"v_mfma_f32_16x16x32_bf16 v[84:87], v[32:35], v[156:159], v[84:87]\n"
"ds_read_b64_tr_b16 v[26:27], v4 offset:512\n"
"v_lshrrev_b32 v21, 1, v21\n"
"s_mul_i32 s45, s30, 147456\n"
"s_add_u32 s45, s45, 4718592\n"
"buffer_load_dword v15, v21, s[56:59], s45 offen\n"
"s_waitcnt lgkmcnt(6)\n"
"v_mfma_f32_16x16x32_bf16 v[92:95], v[36:39], v[156:159], v[92:95]\n"
"ds_read_b64_tr_b16 v[28:29], v4 offset:12288\n"
"v_add_f32 v9, 1.0, v9\n"
"v_mfma_f32_16x16x32_bf16 v[88:91], v[36:39], v[152:155], v[88:91]\n"
"ds_read_b64_tr_b16 v[30:31], v4 offset:12800\n"
"s_waitcnt lgkmcnt(6)\n"
"v_mfma_f32_16x16x32_bf16 v[96:99], v[40:43], v[152:155], v[96:99]\n"
"v_xor_b32 v5, 32, v4\n"
"ds_read_b64_tr_b16 v[32:33], v5\n"
"s_setprio 1\n"
"v_rcp_f32 v8, v8\n"
"v_mfma_f32_16x16x32_bf16 v[100:103], v[40:43], v[156:159], v[100:103]\n"
"ds_read_b64_tr_b16 v[34:35], v5 offset:512\n"
"s_waitcnt lgkmcnt(6)\n"
"v_mfma_f32_16x16x32_bf16 v[108:111], v[44:47], v[156:159], v[108:111]\n"
"ds_read_b64_tr_b16 v[36:37], v5 offset:12288\n"
"v_rcp_f32 v9, v9\n"
"v_mfma_f32_16x16x32_bf16 v[104:107], v[44:47], v[152:155], v[104:107]\n"
"ds_read_b64_tr_b16 v[38:39], v5 offset:12800\n"
"s_waitcnt lgkmcnt(6)\n"
"v_mfma_f32_16x16x32_bf16 v[80:83], v[24:27], v[160:163], v[80:83]\n"
"ds_read_b64_tr_b16 v[40:41], v4 offset:4096\n"
"v_mul_f32 v8, v8, v60\n"
"v_mfma_f32_16x16x32_bf16 v[84:87], v[24:27], v[164:167], v[84:87]\n"
"ds_read_b64_tr_b16 v[42:43], v4 offset:4608\n"
"s_waitcnt lgkmcnt(6)\n"
"v_mfma_f32_16x16x32_bf16 v[92:95], v[28:31], v[164:167], v[92:95]\n"
"ds_read_b64_tr_b16 v[44:45], v4 offset:16384\n"
"v_mul_f32 v9, v9, v61\n"
"v_mfma_f32_16x16x32_bf16 v[88:91], v[28:31], v[160:163], v[88:91]\n"
"ds_read_b64_tr_b16 v[46:47], v4 offset:16896\n"
"s_waitcnt lgkmcnt(6)\n"
"v_mfma_f32_16x16x32_bf16 v[96:99], v[32:35], v[160:163], v[96:99]\n"
"ds_read_b64_tr_b16 v[24:25], v5 offset:4096\n"
"v_cvt_pk_bf16_f32 v52, v8, v9\n"
"v_mfma_f32_16x16x32_bf16 v[100:103], v[32:35], v[164:167], v[100:103]\n"
"ds_read_b64_tr_b16 v[26:27], v5 offset:4608\n"
"s_waitcnt lgkmcnt(6)\n"
"v_mfma_f32_16x16x32_bf16 v[108:111], v[36:39], v[164:167], v[108:111]\n"
"ds_read_b64_tr_b16 v[28:29], v5 offset:16384\n"
"v_exp_f32 v8, -v54\n"
"v_mfma_f32_16x16x32_bf16 v[104:107], v[36:39], v[160:163], v[104:107]\n"
"ds_read_b64_tr_b16 v[30:31], v5 offset:16896\n"
"s_waitcnt lgkmcnt(6)\n"
"v_mfma_f32_16x16x32_bf16 v[80:83], v[40:43], v[168:171], v[80:83]\n"
"ds_read_b64_tr_b16 v[32:33], v4 offset:8192\n"
"v_exp_f32 v9, -v55\n"
"v_mfma_f32_16x16x32_bf16 v[84:87], v[40:43], v[172:175], v[84:87]\n"
"ds_read_b64_tr_b16 v[34:35], v4 offset:8704\n"
"s_waitcnt lgkmcnt(6)\n"
"v_mfma_f32_16x16x32_bf16 v[92:95], v[44:47], v[172:175], v[92:95]\n"
"ds_read_b64_tr_b16 v[36:37], v4 offset:20480\n"
"v_add_f32 v8, 1.0, v8\n"
"v_mfma_f32_16x16x32_bf16 v[88:91], v[44:47], v[168:171], v[88:91]\n"
"ds_read_b64_tr_b16 v[38:39], v4 offset:20992\n"
"s_waitcnt lgkmcnt(6)\n"
"v_mfma_f32_16x16x32_bf16 v[96:99], v[24:27], v[168:171], v[96:99]\n"
"ds_read_b64_tr_b16 v[40:41], v5 offset:8192\n"
"v_add_f32 v9, 1.0, v9\n"
"v_mfma_f32_16x16x32_bf16 v[100:103], v[24:27], v[172:175], v[100:103]\n"
"ds_read_b64_tr_b16 v[42:43], v5 offset:8704\n"
"s_waitcnt lgkmcnt(6)\n"
"v_mfma_f32_16x16x32_bf16 v[108:111], v[28:31], v[172:175], v[108:111]\n"
"ds_read_b64_tr_b16 v[44:45], v5 offset:20480\n"
"v_rcp_f32 v8, v8\n"
"v_mfma_f32_16x16x32_bf16 v[104:107], v[28:31], v[168:171], v[104:107]\n"
"ds_read_b64_tr_b16 v[46:47], v5 offset:20992\n"
"s_waitcnt lgkmcnt(6)\n"
"v_mfma_f32_16x16x32_bf16 v[80:83], v[32:35], v[176:179], v[80:83]\n"
"ds_read_b64_tr_b16 v[24:25], v4 offset:24576\n"
"v_rcp_f32 v9, v9\n"
"v_mfma_f32_16x16x32_bf16 v[84:87], v[32:35], v[180:183], v[84:87]\n"
"ds_read_b64_tr_b16 v[26:27], v4 offset:25088\n"
"s_waitcnt lgkmcnt(6)\n"
"v_mfma_f32_16x16x32_bf16 v[92:95], v[36:39], v[180:183], v[92:95]\n"
"ds_read_b64_tr_b16 v[28:29], v4 offset:36864\n"
"s_setprio 0\n"
"v_mul_f32 v8, v8, v62\n"
"v_mfma_f32_16x16x32_bf16 v[88:91], v[36:39], v[176:179], v[88:91]\n"
"ds_read_b64_tr_b16 v[30:31], v4 offset:37376\n"
"s_waitcnt lgkmcnt(6)\n"
"v_mfma_f32_16x16x32_bf16 v[96:99], v[40:43], v[176:179], v[96:99]\n"
"ds_read_b64_tr_b16 v[32:33], v5 offset:24576\n"
"v_mul_f32 v9, v9, v63\n"
"v_mfma_f32_16x16x32_bf16 v[100:103], v[40:43], v[180:183], v[100:103]\n"
"ds_read_b64_tr_b16 v[34:35], v5 offset:25088\n"
"s_waitcnt lgkmcnt(6)\n"
"v_mfma_f32_16x16x32_bf16 v[108:111], v[44:47], v[180:183], v[108:111]\n"
"ds_read_b64_tr_b16 v[36:37], v5 offset:36864\n"
"v_cvt_pk_bf16_f32 v53, v8, v9\n"
"v_mfma_f32_16x16x32_bf16 v[104:107], v[44:47], v[176:179], v[104:107]\n"
"ds_read_b64_tr_b16 v[38:39], v5 offset:37376\n"
"s_waitcnt lgkmcnt(6)\n"
"v_mfma_f32_16x16x32_bf16 v[80:83], v[24:27], v[184:187], v[80:83]\n"
"ds_read_b64_tr_b16 v[40:41], v4 offset:28672\n"
"v_mfma_f32_16x16x32_bf16 v[84:87], v[24:27], v[188:191], v[84:87]\n"
"ds_read_b64_tr_b16 v[42:43], v4 offset:29184\n"
"s_waitcnt lgkmcnt(6)\n"
"v_mfma_f32_16x16x32_bf16 v[92:95], v[28:31], v[188:191], v[92:95]\n"
"ds_read_b64_tr_b16 v[44:45], v4 offset:40960\n"
"v_mfma_f32_16x16x32_bf16 v[88:91], v[28:31], v[184:187], v[88:91]\n"
"ds_read_b64_tr_b16 v[46:47], v4 offset:41472\n"
"s_waitcnt lgkmcnt(6)\n"
"v_mfma_f32_16x16x32_bf16 v[96:99], v[32:35], v[184:187], v[96:99]\n"
"ds_read_b64_tr_b16 v[24:25], v5 offset:28672\n"
"v_mfma_f32_16x16x32_bf16 v[100:103], v[32:35], v[188:191], v[100:103]\n"
"ds_read_b64_tr_b16 v[26:27], v5 offset:29184\n"
"s_waitcnt lgkmcnt(6)\n"
"v_mfma_f32_16x16x32_bf16 v[108:111], v[36:39], v[188:191], v[108:111]\n"
"ds_read_b64_tr_b16 v[28:29], v5 offset:40960\n"
"v_mfma_f32_16x16x32_bf16 v[104:107], v[36:39], v[184:187], v[104:107]\n"
"ds_read_b64_tr_b16 v[30:31], v5 offset:41472\n"
"s_waitcnt lgkmcnt(6)\n"
"v_mfma_f32_16x16x32_bf16 v[80:83], v[40:43], v[192:195], v[80:83]\n"
"ds_read_b64_tr_b16 v[32:33], v4 offset:32768\n"
"v_mfma_f32_16x16x32_bf16 v[84:87], v[40:43], v[196:199], v[84:87]\n"
"ds_read_b64_tr_b16 v[34:35], v4 offset:33280\n"
"s_waitcnt lgkmcnt(6)\n"
"v_mfma_f32_16x16x32_bf16 v[92:95], v[44:47], v[196:199], v[92:95]\n"
"ds_read_b64_tr_b16 v[36:37], v4 offset:45056\n"
"v_mfma_f32_16x16x32_bf16 v[88:91], v[44:47], v[192:195], v[88:91]\n"
"ds_read_b64_tr_b16 v[38:39], v4 offset:45568\n"
"s_waitcnt lgkmcnt(6)\n"
"v_mfma_f32_16x16x32_bf16 v[96:99], v[24:27], v[192:195], v[96:99]\n"
"ds_read_b64_tr_b16 v[40:41], v5 offset:32768\n"
"v_mfma_f32_16x16x32_bf16 v[100:103], v[24:27], v[196:199], v[100:103]\n"
"ds_read_b64_tr_b16 v[42:43], v5 offset:33280\n"
"s_waitcnt lgkmcnt(6)\n"
"v_mfma_f32_16x16x32_bf16 v[108:111], v[28:31], v[196:199], v[108:111]\n"
"ds_read_b64_tr_b16 v[44:45], v5 offset:45056\n"
"v_mfma_f32_16x16x32_bf16 v[104:107], v[28:31], v[192:195], v[104:107]\n"
"ds_read_b64_tr_b16 v[46:47], v5 offset:45568\n"
"s_waitcnt vmcnt(6)\n"
"s_barrier\n"
"s_waitcnt lgkmcnt(6)\n"
"v_mfma_f32_16x16x32_bf16 v[80:83], v[32:35], v[200:203], v[80:83]\n"
"v_add_u32 v4, 98304, v22\n"
"ds_read_b64_tr_b16 v[24:25], v4\n"
"s_setprio 3\n"
"v_mfma_f32_16x16x32_bf16 v[84:87], v[32:35], v[204:207], v[84:87]\n"
"ds_read_b64_tr_b16 v[26:27], v4 offset:512\n"
"s_waitcnt lgkmcnt(6)\n"
"v_mfma_f32_16x16x32_bf16 v[92:95], v[36:39], v[204:207], v[92:95]\n"
"ds_read_b64_tr_b16 v[28:29], v4 offset:12288\n"
"v_mfma_f32_16x16x32_bf16 v[88:91], v[36:39], v[200:203], v[88:91]\n"
"ds_read_b64_tr_b16 v[30:31], v4 offset:12800\n"
"s_waitcnt lgkmcnt(6)\n"
"v_mfma_f32_16x16x32_bf16 v[96:99], v[40:43], v[200:203], v[96:99]\n"
"v_xor_b32 v5, 32, v4\n"
"ds_read_b64_tr_b16 v[32:33], v5\n"
"v_mfma_f32_16x16x32_bf16 v[100:103], v[40:43], v[204:207], v[100:103]\n"
"ds_read_b64_tr_b16 v[34:35], v5 offset:512\n"
"s_waitcnt lgkmcnt(6)\n"
"v_mfma_f32_16x16x32_bf16 v[108:111], v[44:47], v[204:207], v[108:111]\n"
"ds_read_b64_tr_b16 v[36:37], v5 offset:12288\n"
"v_mfma_f32_16x16x32_bf16 v[104:107], v[44:47], v[200:203], v[104:107]\n"
"ds_read_b64_tr_b16 v[38:39], v5 offset:12800\n"
"s_waitcnt lgkmcnt(6)\n"
"v_mfma_f32_16x16x32_bf16 v[80:83], v[24:27], v[208:211], v[80:83]\n"
"ds_read_b64_tr_b16 v[40:41], v4 offset:4096\n"
"v_mfma_f32_16x16x32_bf16 v[84:87], v[24:27], v[212:215], v[84:87]\n"
"ds_read_b64_tr_b16 v[42:43], v4 offset:4608\n"
"s_waitcnt lgkmcnt(6)\n"
"v_mfma_f32_16x16x32_bf16 v[92:95], v[28:31], v[212:215], v[92:95]\n"
"ds_read_b64_tr_b16 v[44:45], v4 offset:16384\n"
"v_mfma_f32_16x16x32_bf16 v[88:91], v[28:31], v[208:211], v[88:91]\n"
"ds_read_b64_tr_b16 v[46:47], v4 offset:16896\n"
"s_waitcnt lgkmcnt(6)\n"
"v_mfma_f32_16x16x32_bf16 v[96:99], v[32:35], v[208:211], v[96:99]\n"
"ds_read_b64_tr_b16 v[24:25], v5 offset:4096\n"
"v_mfma_f32_16x16x32_bf16 v[100:103], v[32:35], v[212:215], v[100:103]\n"
"ds_read_b64_tr_b16 v[26:27], v5 offset:4608\n"
"s_waitcnt lgkmcnt(6)\n"
"v_mfma_f32_16x16x32_bf16 v[108:111], v[36:39], v[212:215], v[108:111]\n"
"ds_read_b64_tr_b16 v[28:29], v5 offset:16384\n"
"v_mfma_f32_16x16x32_bf16 v[104:107], v[36:39], v[208:211], v[104:107]\n"
"ds_read_b64_tr_b16 v[30:31], v5 offset:16896\n"
"s_waitcnt lgkmcnt(6)\n"
"v_mfma_f32_16x16x32_bf16 v[80:83], v[40:43], v[216:219], v[80:83]\n"
"ds_read_b64_tr_b16 v[32:33], v4 offset:8192\n"
"v_mfma_f32_16x16x32_bf16 v[84:87], v[40:43], v[220:223], v[84:87]\n"
"ds_read_b64_tr_b16 v[34:35], v4 offset:8704\n"
"s_waitcnt lgkmcnt(6)\n"
"v_mfma_f32_16x16x32_bf16 v[92:95], v[44:47], v[220:223], v[92:95]\n"
"ds_read_b64_tr_b16 v[36:37], v4 offset:20480\n"
"v_mfma_f32_16x16x32_bf16 v[88:91], v[44:47], v[216:219], v[88:91]\n"
"ds_read_b64_tr_b16 v[38:39], v4 offset:20992\n"
"s_waitcnt lgkmcnt(6)\n"
"v_mfma_f32_16x16x32_bf16 v[96:99], v[24:27], v[216:219], v[96:99]\n"
"ds_read_b64_tr_b16 v[40:41], v5 offset:8192\n"
"v_mfma_f32_16x16x32_bf16 v[100:103], v[24:27], v[220:223], v[100:103]\n"
"ds_read_b64_tr_b16 v[42:43], v5 offset:8704\n"
"s_waitcnt lgkmcnt(6)\n"
"v_mfma_f32_16x16x32_bf16 v[108:111], v[28:31], v[220:223], v[108:111]\n"
"ds_read_b64_tr_b16 v[44:45], v5 offset:20480\n"
"v_mfma_f32_16x16x32_bf16 v[104:107], v[28:31], v[216:219], v[104:107]\n"
"ds_read_b64_tr_b16 v[46:47], v5 offset:20992\n"
"s_waitcnt lgkmcnt(6)\n"
"v_mfma_f32_16x16x32_bf16 v[80:83], v[32:35], v[224:227], v[80:83]\n"
"ds_read_b64_tr_b16 v[24:25], v4 offset:24576\n"
"v_mfma_f32_16x16x32_bf16 v[84:87], v[32:35], v[228:231], v[84:87]\n"
"ds_read_b64_tr_b16 v[26:27], v4 offset:25088\n"
"s_waitcnt lgkmcnt(6)\n"
"v_mfma_f32_16x16x32_bf16 v[92:95], v[36:39], v[228:231], v[92:95]\n"
"ds_read_b64_tr_b16 v[28:29], v4 offset:36864\n"
"s_setprio 2\n"
"v_mfma_f32_16x16x32_bf16 v[88:91], v[36:39], v[224:227], v[88:91]\n"
"ds_read_b64_tr_b16 v[30:31], v4 offset:37376\n"
"s_waitcnt lgkmcnt(6)\n"
"v_mfma_f32_16x16x32_bf16 v[96:99], v[40:43], v[224:227], v[96:99]\n"
"ds_read_b64_tr_b16 v[32:33], v5 offset:24576\n"
"v_mfma_f32_16x16x32_bf16 v[100:103], v[40:43], v[228:231], v[100:103]\n"
"ds_read_b64_tr_b16 v[34:35], v5 offset:25088\n"
"s_waitcnt lgkmcnt(6)\n"
"v_mfma_f32_16x16x32_bf16 v[108:111], v[44:47], v[228:231], v[108:111]\n"
"ds_read_b64_tr_b16 v[36:37], v5 offset:36864\n"
"v_mfma_f32_16x16x32_bf16 v[104:107], v[44:47], v[224:227], v[104:107]\n"
"ds_read_b64_tr_b16 v[38:39], v5 offset:37376\n"
"s_waitcnt lgkmcnt(6)\n"
"v_mfma_f32_16x16x32_bf16 v[80:83], v[24:27], v[232:235], v[80:83]\n"
"ds_read_b64_tr_b16 v[40:41], v4 offset:28672\n"
"v_mfma_f32_16x16x32_bf16 v[84:87], v[24:27], v[236:239], v[84:87]\n"
"ds_read_b64_tr_b16 v[42:43], v4 offset:29184\n"
"s_add_u32 s46, s10, 0\n"
"v_lshlrev_b32 v3, 3, v0\n"
"buffer_store_dwordx2 v[48:49], v3, s[48:51], s46 offen sc1\n"
"s_waitcnt lgkmcnt(6)\n"
"v_mfma_f32_16x16x32_bf16 v[92:95], v[28:31], v[236:239], v[92:95]\n"
"ds_read_b64_tr_b16 v[44:45], v4 offset:40960\n"
"v_mfma_f32_16x16x32_bf16 v[88:91], v[28:31], v[232:235], v[88:91]\n"
"ds_read_b64_tr_b16 v[46:47], v4 offset:41472\n"
"s_waitcnt lgkmcnt(6)\n"
"v_mfma_f32_16x16x32_bf16 v[96:99], v[32:35], v[232:235], v[96:99]\n"
"ds_read_b64_tr_b16 v[24:25], v5 offset:28672\n"
"v_mfma_f32_16x16x32_bf16 v[100:103], v[32:35], v[236:239], v[100:103]\n"
"ds_read_b64_tr_b16 v[26:27], v5 offset:29184\n"
"s_mov_b64 exec, 0xf\n"
"buffer_load_dwordx2 v[10:11], v19, s[48:51], 0 offen sc1\n"
"s_mov_b64 exec, -1\n"
"s_waitcnt lgkmcnt(6)\n"
"v_mfma_f32_16x16x32_bf16 v[108:111], v[36:39], v[236:239], v[108:111]\n"
"ds_read_b64_tr_b16 v[28:29], v5 offset:40960\n"
"v_mfma_f32_16x16x32_bf16 v[104:107], v[36:39], v[232:235], v[104:107]\n"
"ds_read_b64_tr_b16 v[30:31], v5 offset:41472\n"
"s_waitcnt lgkmcnt(6)\n"
"v_mfma_f32_16x16x32_bf16 v[80:83], v[40:43], v[240:243], v[80:83]\n"
"ds_read_b64_tr_b16 v[32:33], v4 offset:32768\n"
"v_mfma_f32_16x16x32_bf16 v[84:87], v[40:43], v[244:247], v[84:87]\n"
"ds_read_b64_tr_b16 v[34:35], v4 offset:33280\n"
"s_add_u32 s46, s10, 147456\n"
"v_lshlrev_b32 v3, 3, v0\n"
"buffer_store_dwordx2 v[52:53], v3, s[48:51], s46 offen sc1\n"
"s_waitcnt lgkmcnt(6)\n"
"v_mfma_f32_16x16x32_bf16 v[92:95], v[44:47], v[244:247], v[92:95]\n"
"ds_read_b64_tr_b16 v[36:37], v4 offset:45056\n"
"v_mfma_f32_16x16x32_bf16 v[88:91], v[44:47], v[240:243], v[88:91]\n"
"ds_read_b64_tr_b16 v[38:39], v4 offset:45568\n"
"s_waitcnt lgkmcnt(6)\n"
"v_mfma_f32_16x16x32_bf16 v[96:99], v[24:27], v[240:243], v[96:99]\n"
"ds_read_b64_tr_b16 v[40:41], v5 offset:32768\n"
"v_mfma_f32_16x16x32_bf16 v[100:103], v[24:27], v[244:247], v[100:103]\n"
"ds_read_b64_tr_b16 v[42:43], v5 offset:33280\n"
"s_waitcnt lgkmcnt(6)\n"
"v_mfma_f32_16x16x32_bf16 v[108:111], v[28:31], v[244:247], v[108:111]\n"
"ds_read_b64_tr_b16 v[44:45], v5 offset:45056\n"
"v_mfma_f32_16x16x32_bf16 v[104:107], v[28:31], v[240:243], v[104:107]\n"
"ds_read_b64_tr_b16 v[46:47], v5 offset:45568\n"
"s_waitcnt lgkmcnt(6)\n"
"v_mfma_f32_16x16x32_bf16 v[80:83], v[32:35], v[248:251], v[80:83]\n"
"s_mov_b32 s44, 49152\n"
"v_xad_u32 v4, 64, v22, s44\n"
"ds_read_b64_tr_b16 v[24:25], v4\n"
"v_mfma_f32_16x16x32_bf16 v[84:87], v[32:35], v[252:255], v[84:87]\n"
"ds_read_b64_tr_b16 v[26:27], v4 offset:512\n"
"s_waitcnt lgkmcnt(6)\n"
"v_mfma_f32_16x16x32_bf16 v[92:95], v[36:39], v[252:255], v[92:95]\n"
"ds_read_b64_tr_b16 v[28:29], v4 offset:12288\n"
"v_mfma_f32_16x16x32_bf16 v[88:91], v[36:39], v[248:251], v[88:91]\n"
"ds_read_b64_tr_b16 v[30:31], v4 offset:12800\n"
"s_waitcnt lgkmcnt(6)\n"
"v_mfma_f32_16x16x32_bf16 v[96:99], v[40:43], v[248:251], v[96:99]\n"
"v_xor_b32 v5, 32, v4\n"
"ds_read_b64_tr_b16 v[32:33], v5\n"
"s_setprio 1\n"
"v_mfma_f32_16x16x32_bf16 v[100:103], v[40:43], v[252:255], v[100:103]\n"
"ds_read_b64_tr_b16 v[34:35], v5 offset:512\n"
"s_waitcnt lgkmcnt(6)\n"
"v_mfma_f32_16x16x32_bf16 v[108:111], v[44:47], v[252:255], v[108:111]\n"
"ds_read_b64_tr_b16 v[36:37], v5 offset:12288\n"
"v_mfma_f32_16x16x32_bf16 v[104:107], v[44:47], v[248:251], v[104:107]\n"
"ds_read_b64_tr_b16 v[38:39], v5 offset:12800\n"
"s_waitcnt lgkmcnt(6)\n"
"v_mfma_f32_16x16x32_bf16 v[48:51], v[24:27], v[112:115], 0\n"
"ds_read_b64_tr_b16 v[40:41], v4 offset:4096\n"
"v_exp_f32 v8, -v80\n"
"v_mfma_f32_16x16x32_bf16 v[52:55], v[24:27], v[116:119], 0\n"
"ds_read_b64_tr_b16 v[42:43], v4 offset:4608\n"
"v_exp_f32 v9, -v81\n"
"s_waitcnt lgkmcnt(6)\n"
"v_mfma_f32_16x16x32_bf16 v[60:63], v[28:31], v[116:119], 0\n"
"ds_read_b64_tr_b16 v[44:45], v4 offset:16384\n"
"v_add_f32 v8, 1.0, v8\n"
"v_mfma_f32_16x16x32_bf16 v[56:59], v[28:31], v[112:115], 0\n"
"ds_read_b64_tr_b16 v[46:47], v4 offset:16896\n"
"v_add_f32 v9, 1.0, v9\n"
"s_waitcnt lgkmcnt(6)\n"
"v_mfma_f32_16x16x32_bf16 v[64:67], v[32:35], v[112:115], 0\n"
"ds_read_b64_tr_b16 v[24:25], v5 offset:4096\n"
"v_rcp_f32 v8, v8\n"
"v_mfma_f32_16x16x32_bf16 v[68:71], v[32:35], v[116:119], 0\n"
"ds_read_b64_tr_b16 v[26:27], v5 offset:4608\n"
"v_rcp_f32 v9, v9\n"
"s_or_b32 s40, s36, 2\n"
"s_waitcnt vmcnt(1)\n"
"s_mov_b32 s44, 0x40000\n"
"s_mov_b64 exec, 0xf\n"
".Lp4:\n"
"v_cmp_le_u64 vcc, s[40:41], v[10:11]\n"
"v_cmp_ge_u64 s[42:43], s[38:39], v[10:11]\n"
"s_nop 1\n"
"s_and_b64 s[42:43], s[42:43], vcc\n"
"s_cmp_eq_u64 s[42:43], exec\n"
"s_cbranch_scc1 .Ld4\n"
"s_sub_u32 s44, s44, 1\n"
"s_cbranch_scc1 .Ld4\n"
"s_sleep 1\n"
"buffer_load_dwordx2 v[10:11], v19, s[48:51], 0 offen sc1\n"
"s_waitcnt vmcnt(0)\n"
"s_branch .Lp4\n"
".Ld4:\n"
"s_mov_b64 exec, -1\n"
"s_waitcnt lgkmcnt(6)\n"
"v_mfma_f32_16x16x32_bf16 v[76:79], v[36:39], v[116:119], 0\n"
"ds_read_b64_tr_b16 v[28:29], v5 offset:16384\n"
"v_mul_f32 v8, v8, v88\n"
"v_mfma_f32_16x16x32_bf16 v[72:75], v[36:39], v[112:115], 0\n"
"ds_read_b64_tr_b16 v[30:31], v5 offset:16896\n"
"v_mul_f32 v9, v9, v89\n"
"s_waitcnt lgkmcnt(6)\n"
"v_mfma_f32_16x16x32_bf16 v[48:51], v[40:43], v[120:123], v[48:51]\n"
"ds_read_b64_tr_b16 v[32:33], v4 offset:8192\n"
"v_cvt_pk_bf16_f32 v80, v8, v9\n"
"v_mfma_f32_16x16x32_bf16 v[52:55], v[40:43], v[124:127], v[52:55]\n"
"ds_read_b64_tr_b16 v[34:35], v4 offset:8704\n"
"v_exp_f32 v8, -v82\n"
"s_waitcnt lgkmcnt(6)\n"
"v_mfma_f32_16x16x32_bf16 v[60:63], v[44:47], v[124:127], v[60:63]\n"
"ds_read_b64_tr_b16 v[36:37], v4 offset:20480\n"
"v_exp_f32 v9, -v83\n"
"v_mfma_f32_16x16x32_bf16 v[56:59], v[44:47], v[120:123], v[56:59]\n"
"ds_read_b64_tr_b16 v[38:39], v4 offset:20992\n"
"v_add_f32 v8, 1.0, v8\n"
"s_waitcnt lgkmcnt(6)\n"
"v_mfma_f32_16x16x32_bf16 v[64:67], v[24:27], v[120:123], v[64:67]\n"
"ds_read_b64_tr_b16 v[40:41], v5 offset:8192\n"
"v_add_f32 v9, 1.0, v9\n"
"v_mfma_f32_16x16x32_bf16 v[68:71], v[24:27], v[124:127], v[68:71]\n"
"ds_read_b64_tr_b16 v[42:43], v5 offset:8704\n"
"v_rcp_f32 v8, v8\n"
"s_waitcnt lgkmcnt(6)\n"
"v_mfma_f32_16x16x32_bf16 v[76:79], v[28:31], v[124:127], v[76:79]\n"
"ds_read_b64_tr_b16 v[44:45], v5 offset:20480\n"
"v_rcp_f32 v9, v9\n"
"v_mfma_f32_16x16x32_bf16 v[72:75], v[28:31], v[120:123], v[72:75]\n"
"ds_read_b64_tr_b16 v[46:47], v5 offset:20992\n"
"v_mul_f32 v8, v8, v90\n"
"s_waitcnt lgkmcnt(6)\n"
"v_mfma_f32_16x16x32_bf16 v[48:51], v[32:35], v[128:131], v[48:51]\n"
"ds_read_b64_tr_b16 v[24:25], v4 offset:24576\n"
"v_mul_f32 v9, v9, v91\n"
"v_mfma_f32_16x16x32_bf16 v[52:55], v[32:35], v[132:135], v[52:55]\n"
"ds_read_b64_tr_b16 v[26:27], v4 offset:25088\n"
"v_cvt_pk_bf16_f32 v81, v8, v9\n"
"s_or_b32 s45, s36, 3\n"
"v_mov_b32 v16, s45\n"
"buffer_load_dwordx4 v[112:115], v1, s[48:51], s19 offen sc1\n"
"s_waitcnt lgkmcnt(6)\n"
"v_mfma_f32_16x16x32_bf16 v[60:63], v[36:39], v[132:135], v[60:63]\n"
"ds_read_b64_tr_b16 v[28:29], v4 offset:36864\n"
"v_exp_f32 v8, -v96\n"
"v_mfma_f32_16x16x32_bf16 v[56:59], v[36:39], v[128:131], v[56:59]\n"
"ds_read_b64_tr_b16 v[30:31], v4 offset:37376\n"
"v_exp_f32 v9, -v97\n"
"v_mov_b32 v17, s37\n"
"s_waitcnt lgkmcnt(6)\n"
"v_mfma_f32_16x16x32_bf16 v[64:67], v[40:43], v[128:131], v[64:67]\n"
"ds_read_b64_tr_b16 v[32:33], v5 offset:24576\n"
"v_add_f32 v8, 1.0, v8\n"
"v_mfma_f32_16x16x32_bf16 v[68:71], v[40:43], v[132:135], v[68:71]\n"
"ds_read_b64_tr_b16 v[34:35], v5 offset:25088\n"
"v_add_f32 v9, 1.0, v9\n"
"v_lshrrev_b32 v18, 4, v0\n"
"v_lshlrev_b32 v18, 28, v18\n"
"v_lshl_add_u32 v18, v0, 3, v18\n"
"s_waitcnt lgkmcnt(6)\n"
"v_mfma_f32_16x16x32_bf16 v[76:79], v[44:47], v[132:135], v[76:79]\n"
"ds_read_b64_tr_b16 v[36:37], v5 offset:36864\n"
"s_setprio 0\n"
"v_rcp_f32 v8, v8\n"
"v_mfma_f32_16x16x32_bf16 v[72:75], v[44:47], v[128:131], v[72:75]\n"
"ds_read_b64_tr_b16 v[38:39], v5 offset:37376\n"
"v_rcp_f32 v9, v9\n"
"s_add_u32 s45, s79, 1048576\n"
"s_waitcnt vmcnt(1)\n"
"buffer_store_dwordx2 v[16:17], v18, s[48:51], s45 offen sc1\n"
"s_add_u32 s46, s19, s81\n"
"buffer_load_dwordx4 v[116:119], v1, s[48:51], s46 offen sc1\n"
"s_waitcnt lgkmcnt(6)\n"
"v_mfma_f32_16x16x32_bf16 v[48:51], v[24:27], v[136:139], v[48:51]\n"
"ds_read_b64_tr_b16 v[40:41], v4 offset:28672\n"
"v_mul_f32 v8, v8, v104\n"
"v_mfma_f32_16x16x32_bf16 v[52:55], v[24:27], v[140:143], v[52:55]\n"
"ds_read_b64_tr_b16 v[42:43], v4 offset:29184\n"
"v_mul_f32 v9, v9, v105\n"
"s_waitcnt lgkmcnt(6)\n"
"v_mfma_f32_16x16x32_bf16 v[60:63], v[28:31], v[140:143], v[60:63]\n"
"ds_read_b64_tr_b16 v[44:45], v4 offset:40960\n"
"v_cvt_pk_bf16_f32 v82, v8, v9\n"
"v_mfma_f32_16x16x32_bf16 v[56:59], v[28:31], v[136:139], v[56:59]\n"
"ds_read_b64_tr_b16 v[46:47], v4 offset:41472\n"
"v_exp_f32 v8, -v98\n"
"buffer_load_dwordx4 v[120:123], v1, s[48:51], s20 offen sc1\n"
"s_waitcnt lgkmcnt(6)\n"
"v_mfma_f32_16x16x32_bf16 v[64:67], v[32:35], v[136:139], v[64:67]\n"
"s_branch .Lsk8_0\n"
".Lst8_0:\n"
"s_branch .Lbk8\n"
".Lsk8_0:\n"
"ds_read_b64_tr_b16 v[24:25], v5 offset:28672\n"
"v_exp_f32 v9, -v99\n"
"v_mfma_f32_16x16x32_bf16 v[68:71], v[32:35], v[140:143], v[68:71]\n"
"ds_read_b64_tr_b16 v[26:27], v5 offset:29184\n"
"v_add_f32 v8, 1.0, v8\n"
"s_waitcnt lgkmcnt(6)\n"
"v_mfma_f32_16x16x32_bf16 v[76:79], v[36:39], v[140:143], v[76:79]\n"
"ds_read_b64_tr_b16 v[28:29], v5 offset:40960\n"
"v_add_f32 v9, 1.0, v9\n"
"v_mfma_f32_16x16x32_bf16 v[72:75], v[36:39], v[136:139], v[72:75]\n"
"ds_read_b64_tr_b16 v[30:31], v5 offset:41472\n"
"v_rcp_f32 v8, v8\n"
"s_add_u32 s46, s20, s81\n"
"buffer_load_dwordx4 v[124:127], v1, s[48:51], s46 offen sc1\n"
"s_waitcnt lgkmcnt(6)\n"
"v_mfma_f32_16x16x32_bf16 v[48:51], v[40:43], v[144:147], v[48:51]\n"
"ds_read_b64_tr_b16 v[32:33], v4 offset:32768\n"
"v_rcp_f32 v9, v9\n"
"v_mfma_f32_16x16x32_bf16 v[52:55], v[40:43], v[148:151], v[52:55]\n"
"ds_read_b64_tr_b16 v[34:35], v4 offset:33280\n"
"v_mul_f32 v8, v8, v106\n"
"s_waitcnt lgkmcnt(6)\n"
"v_mfma_f32_16x16x32_bf16 v[60:63], v[44:47], v[148:151], v[60:63]\n"
"ds_read_b64_tr_b16 v[36:37], v4 offset:45056\n"
"v_mul_f32 v9, v9, v107\n"
"v_mfma_f32_16x16x32_bf16 v[56:59], v[44:47], v[144:147], v[56:59]\n"
"ds_read_b64_tr_b16 v[38:39], v4 offset:45568\n"
"v_cvt_pk_bf16_f32 v83, v8, v9\n"
"s_add_u32 s46, s9, 65536\n"
"buffer_store_dwordx4 v[80:83], v1, s[48:51], s46 offen sc1\n"
"s_waitcnt lgkmcnt(6)\n"
"v_mfma_f32_16x16x32_bf16 v[64:67], v[24:27], v[144:147], v[64:67]\n"
"ds_read_b64_tr_b16 v[40:41], v5 offset:32768\n"
"v_exp_f32 v8, -v84\n"
"v_mfma_f32_16x16x32_bf16 v[68:71], v[24:27], v[148:151], v[68:71]\n"
"ds_read_b64_tr_b16 v[42:43], v5 offset:33280\n"
"v_exp_f32 v9, -v85\n"
"s_waitcnt lgkmcnt(6)\n"
"v_mfma_f32_16x16x32_bf16 v[76:79], v[28:31], v[148:151], v[76:79]\n"
"ds_read_b64_tr_b16 v[44:45], v5 offset:45056\n"
"v_add_f32 v8, 1.0, v8\n"
"v_mfma_f32_16x16x32_bf16 v[72:75], v[28:31], v[144:147], v[72:75]\n"
"ds_read_b64_tr_b16 v[46:47], v5 offset:45568\n"
"v_add_f32 v9, 1.0, v9\n"
"buffer_load_dwordx4 v[128:131], v1, s[48:51], s21 offen sc1\n"
"s_waitcnt lgkmcnt(6)\n"
"v_mfma_f32_16x16x32_bf16 v[48:51], v[32:35], v[152:155], v[48:51]\n"
"s_mov_b32 s44, 0\n"
"v_xad_u32 v4, 64, v22, s44\n"
"ds_read_b64_tr_b16 v[24:25], v4\n"
"v_rcp_f32 v8, v8\n"
"v_mfma_f32_16x16x32_bf16 v[52:55], v[32:35], v[156:159], v[52:55]\n"
"ds_read_b64_tr_b16 v[26:27], v4 offset:512\n"
"v_rcp_f32 v9, v9\n"
"s_waitcnt lgkmcnt(6)\n"
"v_mfma_f32_16x16x32_bf16 v[60:63], v[36:39], v[156:159], v[60:63]\n"
"ds_read_b64_tr_b16 v[28:29], v4 offset:12288\n"
"v_mul_f32 v8, v8, v92\n"
"v_mfma_f32_16x16x32_bf16 v[56:59], v[36:39], v[152:155], v[56:59]\n"
"ds_read_b64_tr_b16 v[30:31], v4 offset:12800\n"
"v_mul_f32 v9, v9, v93\n"
"s_add_u32 s46, s21, s81\n"
"buffer_load_dwordx4 v[132:135], v1, s[48:51], s46 offen sc1\n"
"s_waitcnt lgkmcnt(6)\n"
"v_mfma_f32_16x16x32_bf16 v[64:67], v[40:43], v[152:155], v[64:67]\n"
"v_xor_b32 v5, 32, v4\n"
"ds_read_b64_tr_b16 v[32:33], v5\n"
"v_cvt_pk_bf16_f32 v84, v8, v9\n"
"v_mfma_f32_16x16x32_bf16 v[68:71], v[40:43], v[156:159], v[68:71]\n"
"ds_read_b64_tr_b16 v[34:35], v5 offset:512\n"
"v_exp_f32 v8, -v86\n"
"s_waitcnt lgkmcnt(6)\n"
"v_mfma_f32_16x16x32_bf16 v[76:79], v[44:47], v[156:159], v[76:79]\n"
"ds_read_b64_tr_b16 v[36:37], v5 offset:12288\n"
"v_exp_f32 v9, -v87\n"
"v_mfma_f32_16x16x32_bf16 v[72:75], v[44:47], v[152:155], v[72:75]\n"
"ds_read_b64_tr_b16 v[38:39], v5 offset:12800\n"
"v_add_f32 v8, 1.0, v8\n"
"s_barrier\n"
"s_waitcnt lgkmcnt(6)\n"
"v_mfma_f32_16x16x32_bf16 v[48:51], v[24:27], v[160:163], v[48:51]\n"
"ds_read_b64_tr_b16 v[40:41], v4 offset:4096\n"
"s_setprio 3\n"
"v_add_f32 v9, 1.0, v9\n"
"v_mfma_f32_16x16x32_bf16 v[52:55], v[24:27], v[164:167], v[52:55]\n"
"ds_read_b64_tr_b16 v[42:43], v4 offset:4608\n"
"v_rcp_f32 v8, v8\n"
"buffer_load_dwordx4 v[136:139], v1, s[48:51], s22 offen sc1\n"
"v_lshrrev_b32 v3, 3, v0\n"
"v_mul_u32_u24 v2, 1152, v3\n"
"v_and_b32 v3, 6, v3\n"
"v_xor_b32 v3, v3, v0\n"
"v_and_b32 v3, 7, v3\n"
"v_lshl_add_u32 v2, v3, 4, v2\n"
"s_waitcnt lgkmcnt(6)\n"
"v_mfma_f32_16x16x32_bf16 v[60:63], v[28:31], v[164:167], v[60:63]\n"
"ds_read_b64_tr_b16 v[44:45], v4 offset:16384\n"
"v_rcp_f32 v9, v9\n"
"v_mfma_f32_16x16x32_bf16 v[56:59], v[28:31], v[160:163], v[56:59]\n"
"ds_read_b64_tr_b16 v[46:47], v4 offset:16896\n"
"v_mul_f32 v8, v8, v94\n"
"s_waitcnt lgkmcnt(6)\n"
"v_mfma_f32_16x16x32_bf16 v[64:67], v[32:35], v[160:163], v[64:67]\n"
"ds_read_b64_tr_b16 v[24:25], v5 offset:4096\n"
"v_mul_f32 v9, v9, v95\n"
"v_mfma_f32_16x16x32_bf16 v[68:71], v[32:35], v[164:167], v[68:71]\n"
"ds_read_b64_tr_b16 v[26:27], v5 offset:4608\n"
"v_cvt_pk_bf16_f32 v85, v8, v9\n"
"s_add_u32 m0, s77, 49152\n"
"s_add_u32 s46, s11, 0\n"
"buffer_load_dwordx4 v2, s[56:59], s46 offen lds\n"
"s_waitcnt lgkmcnt(6)\n"
"v_mfma_f32_16x16x32_bf16 v[76:79], v[36:39], v[164:167], v[76:79]\n"
"ds_read_b64_tr_b16 v[28:29], v5 offset:16384\n"
"v_exp_f32 v8, -v100\n"
"v_mfma_f32_16x16x32_bf16 v[72:75], v[36:39], v[160:163], v[72:75]\n"
"ds_read_b64_tr_b16 v[30:31], v5 offset:16896\n"
"v_exp_f32 v9, -v101\n"
"s_waitcnt lgkmcnt(6)\n"
"v_mfma_f32_16x16x32_bf16 v[48:51], v[40:43], v[168:171], v[48:51]\n"
"ds_read_b64_tr_b16 v[32:33], v4 offset:8192\n"
"v_add_f32 v8, 1.0, v8\n"
"v_mfma_f32_16x16x32_bf16 v[52:55], v[40:43], v[172:175], v[52:55]\n"
"ds_read_b64_tr_b16 v[34:35], v4 offset:8704\n"
"v_add_f32 v9, 1.0, v9\n"
"s_add_u32 m0, s77, 53248\n"
"s_add_u32 s46, s12, 0\n"
"buffer_load_dwordx4 v2, s[56:59], s46 offen lds\n"
"s_waitcnt lgkmcnt(6)\n"
"v_mfma_f32_16x16x32_bf16 v[60:63], v[44:47], v[172:175], v[60:63]\n"
"ds_read_b64_tr_b16 v[36:37], v4 offset:20480\n"
"v_rcp_f32 v8, v8\n"
"v_mfma_f32_16x16x32_bf16 v[56:59], v[44:47], v[168:171], v[56:59]\n"
"ds_read_b64_tr_b16 v[38:39], v4 offset:20992\n"
"v_rcp_f32 v9, v9\n"
"s_waitcnt lgkmcnt(6)\n"
"v_mfma_f32_16x16x32_bf16 v[64:67], v[24:27], v[168:171], v[64:67]\n"
"ds_read_b64_tr_b16 v[40:41], v5 offset:8192\n"
"s_setprio 2\n"
"v_mul_f32 v8, v8, v108\n"
"v_mfma_f32_16x16x32_bf16 v[68:71], v[24:27], v[172:175], v[68:71]\n"
"ds_read_b64_tr_b16 v[42:43], v5 offset:8704\n"
"v_mul_f32 v9, v9, v109\n"
"s_add_u32 m0, s77, 57344\n"
"s_add_u32 s46, s13, 0\n"
"buffer_load_dwordx4 v2, s[56:59], s46 offen lds\n"
"s_waitcnt lgkmcnt(6)\n"
"v_mfma_f32_16x16x32_bf16 v[76:79], v[28:31], v[172:175], v[76:79]\n"
"ds_read_b64_tr_b16 v[44:45], v5 offset:20480\n"
"v_cvt_pk_bf16_f32 v86, v8, v9\n"
"v_mfma_f32_16x16x32_bf16 v[72:75], v[28:31], v[168:171], v[72:75]\n"
"ds_read_b64_tr_b16 v[46:47], v5 offset:20992\n"
"v_exp_f32 v8, -v102\n"
"s_waitcnt lgkmcnt(6)\n"
"v_mfma_f32_16x16x32_bf16 v[48:51], v[32:35], v[176:179], v[48:51]\n"
"ds_read_b64_tr_b16 v[24:25], v4 offset:24576\n"
"v_exp_f32 v9, -v103\n"
"v_mfma_f32_16x16x32_bf16 v[52:55], v[32:35], v[180:183], v[52:55]\n"
"ds_read_b64_tr_b16 v[26:27], v4 offset:25088\n"
"v_add_f32 v8, 1.0, v8\n"
"s_add_u32 m0, s77, 73728\n"
"s_add_u32 s46, s14, 0\n"
"buffer_load_dwordx4 v2, s[56:59], s46 offen lds\n"
"s_waitcnt lgkmcnt(6)\n"
"v_mfma_f32_16x16x32_bf16 v[60:63], v[36:39], v[180:183], v[60:63]\n"
"ds_read_b64_tr_b16 v[28:29], v4 offset:36864\n"
"v_add_f32 v9, 1.0, v9\n"
"v_mfma_f32_16x16x32_bf16 v[56:59], v[36:39], v[176:179], v[56:59]\n"
"ds_read_b64_tr_b16 v[30:31], v4 offset:37376\n"
"v_rcp_f32 v8, v8\n"
"s_waitcnt lgkmcnt(6)\n"
"v_mfma_f32_16x16x32_bf16 v[64:67], v[40:43], v[176:179], v[64:67]\n"
"ds_read_b64_tr_b16 v[32:33], v5 offset:24576\n"
"v_rcp_f32 v9, v9\n"
"v_mfma_f32_16x16x32_bf16 v[68:71], v[40:43], v[180:183], v[68:71]\n"
"ds_read_b64_tr_b16 v[34:35], v5 offset:25088\n"
"v_mul_f32 v8, v8, v110\n"
"s_add_u32 m0, s77, 77824\n"
"s_add_u32 s46, s11, 36864\n"
"buffer_load_dwordx4 v2, s[56:59], s46 offen lds\n"
"s_waitcnt lgkmcnt(6)\n"
"v_mfma_f32_16x16x32_bf16 v[76:79], v[44:47], v[180:183], v[76:79]\n"
"ds_read_b64_tr_b16 v[36:37], v5 offset:36864\n"
"v_mul_f32 v9, v9, v111\n"
"v_mfma_f32_16x16x32_bf16 v[72:75], v[44:47], v[176:179], v[72:75]\n"
"ds_read_b64_tr_b16 v[38:39], v5 offset:37376\n"
"v_cvt_pk_bf16_f32 v87, v8, v9\n"
"s_add_u32 s46, s9, 212992\n"
"buffer_store_dwordx4 v[84:87], v1, s[48:51], s46 offen sc1\n"
"s_waitcnt lgkmcnt(6)\n"
"v_mfma_f32_16x16x32_bf16 v[48:51], v[24:27], v[184:187], v[48:51]\n"
"ds_read_b64_tr_b16 v[40:41], v4 offset:28672\n"
"s_setprio 1\n"
"v_mfma_f32_16x16x32_bf16 v[52:55], v[24:27], v[188:191], v[52:55]\n"
"ds_read_b64_tr_b16 v[42:43], v4 offset:29184\n"
"s_mov_b64 exec, 0xf\n"
"buffer_load_dwordx2 v[10:11], v19, s[48:51], 0 offen sc1\n"
"s_mov_b64 exec, -1\n"
"s_waitcnt lgkmcnt(6)\n"
"v_mfma_f32_16x16x32_bf16 v[60:63], v[28:31], v[188:191], v[60:63]\n"
"ds_read_b64_tr_b16 v[44:45], v4 offset:40960\n"
"v_mfma_f32_16x16x32_bf16 v[56:59], v[28:31], v[184:187], v[56:59]\n"
"ds_read_b64_tr_b16 v[46:47], v4 offset:41472\n"
"s_waitcnt lgkmcnt(6)\n"
"v_mfma_f32_16x16x32_bf16 v[64:67], v[32:35], v[184:187], v[64:67]\n"
"ds_read_b64_tr_b16 v[24:25], v5 offset:28672\n"
"v_mfma_f32_16x16x32_bf16 v[68:71], v[32:35], v[188:191], v[68:71]\n"
"ds_read_b64_tr_b16 v[26:27], v5 offset:29184\n"
"s_waitcnt lgkmcnt(6)\n"
"v_mfma_f32_16x16x32_bf16 v[76:79], v[36:39], v[188:191], v[76:79]\n"
"ds_read_b64_tr_b16 v[28:29], v5 offset:40960\n"
"v_mfma_f32_16x16x32_bf16 v[72:75], v[36:39], v[184:187], v[72:75]\n"
"ds_read_b64_tr_b16 v[30:31], v5 offset:41472\n"
"s_add_u32 m0, s77, 81920\n"
"s_add_u32 s46, s12, 36864\n"
"buffer_load_dwordx4 v2, s[56:59], s46 offen lds\n"
"s_waitcnt lgkmcnt(6)\n"
"v_mfma_f32_16x16x32_bf16 v[48:51], v[40:43], v[192:195], v[48:51]\n"
"ds_read_b64_tr_b16 v[32:33], v4 offset:32768\n"
"v_mfma_f32_16x16x32_bf16 v[52:55], v[40:43], v[196:199], v[52:55]\n"
"ds_read_b64_tr_b16 v[34:35], v4 offset:33280\n"
"s_waitcnt lgkmcnt(6)\n"
"v_mfma_f32_16x16x32_bf16 v[60:63], v[44:47], v[196:199], v[60:63]\n"
"ds_read_b64_tr_b16 v[36:37], v4 offset:45056\n"
"v_mfma_f32_16x16x32_bf16 v[56:59], v[44:47], v[192:195], v[56:59]\n"
"ds_read_b64_tr_b16 v[38:39], v4 offset:45568\n"
"s_add_u32 s46, s22, s81\n"
"buffer_load_dwordx4 v[140:143], v1, s[48:51], s46 offen sc1\n"
"s_waitcnt lgkmcnt(6)\n"
"v_mfma_f32_16x16x32_bf16 v[64:67], v[24:27], v[192:195], v[64:67]\n"
"ds_read_b64_tr_b16 v[40:41], v5 offset:32768\n"
"s_setprio 0\n"
"v_mfma_f32_16x16x32_bf16 v[68:71], v[24:27], v[196:199], v[68:71]\n"
"ds_read_b64_tr_b16 v[42:43], v5 offset:33280\n"
"s_waitcnt lgkmcnt(6)\n"
"v_mfma_f32_16x16x32_bf16 v[76:79], v[28:31], v[196:199], v[76:79]\n"
"ds_read_b64_tr_b16 v[44:45], v5 offset:45056\n"
"v_mfma_f32_16x16x32_bf16 v[72:75], v[28:31], v[192:195], v[72:75]\n"
"ds_read_b64_tr_b16 v[46:47], v5 offset:45568\n"
"buffer_load_dwordx4 v[144:147], v1, s[48:51], s19 offen offset:1024 sc1\n"
"s_waitcnt lgkmcnt(6)\n"
"v_mfma_f32_16x16x32_bf16 v[48:51], v[32:35], v[200:203], v[48:51]\n"
"s_mov_b32 s44, 98304\n"
"v_xad_u32 v4, 64, v22, s44\n"
"ds_read_b64_tr_b16 v[24:25], v4\n"
"v_mfma_f32_16x16x32_bf16 v[52:55], v[32:35], v[204:207], v[52:55]\n"
"ds_read_b64_tr_b16 v[26:27], v4 offset:512\n"
"s_waitcnt lgkmcnt(6)\n"
"v_mfma_f32_16x16x32_bf16 v[60:63], v[36:39], v[204:207], v[60:63]\n"
"ds_read_b64_tr_b16 v[28:29], v4 offset:12288\n"
"v_mfma_f32_16x16x32_bf16 v[56:59], v[36:39], v[200:203], v[56:59]\n"
"ds_read_b64_tr_b16 v[30:31], v4 offset:12800\n"
"s_add_u32 s46, s19, s81\n"
"buffer_load_dwordx4 v[148:151], v1, s[48:51], s46 offen offset:1024 sc1\n"
"s_waitcnt lgkmcnt(6)\n"
"v_mfma_f32_16x16x32_bf16 v[64:67], v[40:43], v[200:203], v[64:67]\n"
"v_xor_b32 v5, 32, v4\n"
"ds_read_b64_tr_b16 v[32:33], v5\n"
"v_mfma_f32_16x16x32_bf16 v[68:71], v[40:43], v[204:207], v[68:71]\n"
"ds_read_b64_tr_b16 v[34:35], v5 offset:512\n"
"s_waitcnt lgkmcnt(6)\n"
"v_mfma_f32_16x16x32_bf16 v[76:79], v[44:47], v[204:207], v[76:79]\n"
"ds_read_b64_tr_b16 v[36:37], v5 offset:12288\n"
"v_mfma_f32_16x16x32_bf16 v[72:75], v[44:47], v[200:203], v[72:75]\n"
"ds_read_b64_tr_b16 v[38:39], v5 offset:12800\n"
"buffer_load_dwordx4 v[152:155], v1, s[48:51], s20 offen offset:1024 sc1\n"
"s_barrier\n"
"s_waitcnt lgkmcnt(6)\n"
"v_mfma_f32_16x16x32_bf16 v[48:51], v[24:27], v[208:211], v[48:51]\n"
"ds_read_b64_tr_b16 v[40:41], v4 offset:4096\n"
"s_setprio 3\n"
"v_mfma_f32_16x16x32_bf16 v[52:55], v[24:27], v[212:215], v[52:55]\n"
"ds_read_b64_tr_b16 v[42:43], v4 offset:4608\n"
"s_or_b32 s40, s36, 3\n"
"s_waitcnt vmcnt(5)\n"
"s_mov_b32 s44, 0x40000\n"
"s_mov_b64 exec, 0xf\n"
".Lp5:\n"
"v_cmp_le_u64 vcc, s[40:41], v[10:11]\n"
"v_cmp_ge_u64 s[42:43], s[38:39], v[10:11]\n"
"s_nop 1\n"
"s_and_b64 s[42:43], s[42:43], vcc\n"
"s_cmp_eq_u64 s[42:43], exec\n"
"s_cbranch_scc1 .Ld5\n"
"s_sub_u32 s44, s44, 1\n"
"s_cbranch_scc1 .Ld5\n"
"s_sleep 1\n"
"buffer_load_dwordx2 v[10:11], v19, s[48:51], 0 offen sc1\n"
"s_waitcnt vmcnt(0)\n"
"s_branch .Lp5\n"
".Ld5:\n"
"s_mov_b64 exec, -1\n"
"s_waitcnt lgkmcnt(6)\n"
"v_mfma_f32_16x16x32_bf16 v[60:63], v[28:31], v[212:215], v[60:63]\n"
"ds_read_b64_tr_b16 v[44:45], v4 offset:16384\n"
"v_mfma_f32_16x16x32_bf16 v[56:59], v[28:31], v[208:211], v[56:59]\n"
"ds_read_b64_tr_b16 v[46:47], v4 offset:16896\n"
"s_waitcnt lgkmcnt(6)\n"
"v_mfma_f32_16x16x32_bf16 v[64:67], v[32:35], v[208:211], v[64:67]\n"
"ds_read_b64_tr_b16 v[24:25], v5 offset:4096\n"
"v_mfma_f32_16x16x32_bf16 v[68:71], v[32:35], v[212:215], v[68:71]\n"
"ds_read_b64_tr_b16 v[26:27], v5 offset:4608\n"
"s_add_u32 s46, s20, s81\n"
"buffer_load_dwordx4 v[156:159], v1, s[48:51], s46 offen offset:1024 sc1\n"
"s_waitcnt lgkmcnt(6)\n"
"v_mfma_f32_16x16x32_bf16 v[76:79], v[36:39], v[212:215], v[76:79]\n"
"ds_read_b64_tr_b16 v[28:29], v5 offset:16384\n"
"v_mfma_f32_16x16x32_bf16 v[72:75], v[36:39], v[208:211], v[72:75]\n"
"ds_read_b64_tr_b16 v[30:31], v5 offset:16896\n"
"s_waitcnt lgkmcnt(6)\n"
"v_mfma_f32_16x16x32_bf16 v[48:51], v[40:43], v[216:219], v[48:51]\n"
"ds_read_b64_tr_b16 v[32:33], v4 offset:8192\n"
"v_mfma_f32_16x16x32_bf16 v[52:55], v[40:43], v[220:223], v[52:55]\n"
"ds_read_b64_tr_b16 v[34:35], v4 offset:8704\n"
"buffer_load_dwordx4 v[160:163], v1, s[48:51], s21 offen offset:1024 sc1\n"
"s_waitcnt lgkmcnt(6)\n"
"v_mfma_f32_16x16x32_bf16 v[60:63], v[44:47], v[220:223], v[60:63]\n"
"ds_read_b64_tr_b16 v[36:37], v4 offset:20480\n"
"s_setprio 2\n"
"v_mfma_f32_16x16x32_bf16 v[56:59], v[44:47], v[216:219], v[56:59]\n"
"ds_read_b64_tr_b16 v[38:39], v4 offset:20992\n"
"s_or_b32 s45, s36, 4\n"
"v_mov_b32 v16, s45\n"
"s_waitcnt lgkmcnt(6)\n"
"v_mfma_f32_16x16x32_bf16 v[64:67], v[24:27], v[216:219], v[64:67]\n"
"ds_read_b64_tr_b16 v[40:41], v5 offset:8192\n"
"v_mfma_f32_16x16x32_bf16 v[68:71], v[24:27], v[220:223], v[68:71]\n"
"ds_read_b64_tr_b16 v[42:43], v5 offset:8704\n"
"v_mov_b32 v17, s37\n"
"s_add_u32 s46, s21, s81\n"
"buffer_load_dwordx4 v[164:167], v1, s[48:51], s46 offen offset:1024 sc1\n"
"s_waitcnt lgkmcnt(6)\n"
"v_mfma_f32_16x16x32_bf16 v[76:79], v[28:31], v[220:223], v[76:79]\n"
"ds_read_b64_tr_b16 v[44:45], v5 offset:20480\n"
"v_mfma_f32_16x16x32_bf16 v[72:75], v[28:31], v[216:219], v[72:75]\n"
"ds_read_b64_tr_b16 v[46:47], v5 offset:20992\n"
"v_lshrrev_b32 v18, 4, v0\n"
"v_lshlrev_b32 v18, 28, v18\n"
"v_lshl_add_u32 v18, v0, 3, v18\n"
"s_waitcnt lgkmcnt(6)\n"
"v_mfma_f32_16x16x32_bf16 v[48:51], v[32:35], v[224:227], v[48:51]\n"
"ds_read_b64_tr_b16 v[24:25], v4 offset:24576\n"
"v_mfma_f32_16x16x32_bf16 v[52:55], v[32:35], v[228:231], v[52:55]\n"
"ds_read_b64_tr_b16 v[26:27], v4 offset:25088\n"
"s_add_u32 s45, s79, 1048576\n"
"buffer_store_dwordx2 v[16:17], v18, s[48:51], s45 offen sc1\n"
"s_add_u32 m0, s77, 0\n"
"s_add_u32 s46, s13, 36864\n"
"buffer_load_dwordx4 v2, s[56:59], s46 offen lds\n"
"s_waitcnt lgkmcnt(6)\n"
"v_mfma_f32_16x16x32_bf16 v[60:63], v[36:39], v[228:231], v[60:63]\n"
"ds_read_b64_tr_b16 v[28:29], v4 offset:36864\n"
"v_mfma_f32_16x16x32_bf16 v[56:59], v[36:39], v[224:227], v[56:59]\n"
"ds_read_b64_tr_b16 v[30:31], v4 offset:37376\n"
"s_waitcnt lgkmcnt(6)\n"
"v_mfma_f32_16x16x32_bf16 v[64:67], v[40:43], v[224:227], v[64:67]\n"
"ds_read_b64_tr_b16 v[32:33], v5 offset:24576\n"
"s_setprio 1\n"
"v_mfma_f32_16x16x32_bf16 v[68:71], v[40:43], v[228:231], v[68:71]\n"
"ds_read_b64_tr_b16 v[34:35], v5 offset:25088\n"
"s_add_u32 m0, s77, 4096\n"
"s_add_u32 s46, s14, 36864\n"
"buffer_load_dwordx4 v2, s[56:59], s46 offen lds\n"
"s_waitcnt lgkmcnt(6)\n"
"v_mfma_f32_16x16x32_bf16 v[76:79], v[44:47], v[228:231], v[76:79]\n"
"ds_read_b64_tr_b16 v[36:37], v5 offset:36864\n"
"v_mfma_f32_16x16x32_bf16 v[72:75], v[44:47], v[224:227], v[72:75]\n"
"ds_read_b64_tr_b16 v[38:39], v5 offset:37376\n"
"s_waitcnt lgkmcnt(6)\n"
"v_mfma_f32_16x16x32_bf16 v[48:51], v[24:27], v[232:235], v[48:51]\n"
"ds_read_b64_tr_b16 v[40:41], v4 offset:28672\n"
"v_mfma_f32_16x16x32_bf16 v[52:55], v[24:27], v[236:239], v[52:55]\n"
"ds_read_b64_tr_b16 v[42:43], v4 offset:29184\n"
"v_lshrrev_b32 v3, 2, v0\n"
"v_lshlrev_b32 v22, 7, v3\n"
"s_waitcnt lgkmcnt(6)\n"
"v_mfma_f32_16x16x32_bf16 v[60:63], v[28:31], v[236:239], v[60:63]\n"
"ds_read_b64_tr_b16 v[44:45], v4 offset:40960\n"
"v_mfma_f32_16x16x32_bf16 v[56:59], v[28:31], v[232:235], v[56:59]\n"
"ds_read_b64_tr_b16 v[46:47], v4 offset:41472\n"
"v_bfe_u32 v3, v0, 3, 2\n"
"v_lshl_add_u32 v22, v3, 5, v22\n"
"s_add_u32 m0, s77, 8192\n"
"s_add_u32 s46, s15, 0\n"
"buffer_load_dwordx4 v2, s[56:59], s46 offen lds\n"
"s_waitcnt lgkmcnt(6)\n"
"v_mfma_f32_16x16x32_bf16 v[64:67], v[32:35], v[232:235], v[64:67]\n"
"ds_read_b64_tr_b16 v[24:25], v5 offset:28672\n"
"v_mfma_f32_16x16x32_bf16 v[68:71], v[32:35], v[236:239], v[68:71]\n"
"ds_read_b64_tr_b16 v[26:27], v5 offset:29184\n"
"v_and_b32 v3, 3, v0\n"
"v_lshl_add_u32 v22, v3, 3, v22\n"
"s_waitcnt lgkmcnt(6)\n"
"v_mfma_f32_16x16x32_bf16 v[76:79], v[36:39], v[236:239], v[76:79]\n"
"ds_read_b64_tr_b16 v[28:29], v5 offset:40960\n"
"s_setprio 0\n"
"v_mfma_f32_16x16x32_bf16 v[72:75], v[36:39], v[232:235], v[72:75]\n"
"ds_read_b64_tr_b16 v[30:31], v5 offset:41472\n"
"v_add_u32 v22, s78, v22\n"
"s_add_u32 m0, s77, 24576\n"
"s_add_u32 s46, s15, 36864\n"
"buffer_load_dwordx4 v2, s[56:59], s46 offen lds\n"
"s_waitcnt lgkmcnt(6)\n"
"v_mfma_f32_16x16x32_bf16 v[48:51], v[40:43], v[240:243], v[48:51]\n"
"ds_read_b64_tr_b16 v[32:33], v4 offset:32768\n"
"v_mfma_f32_16x16x32_bf16 v[52:55], v[40:43], v[244:247], v[52:55]\n"
"ds_read_b64_tr_b16 v[34:35], v4 offset:33280\n"
"s_waitcnt lgkmcnt(6)\n"
"v_mfma_f32_16x16x32_bf16 v[60:63], v[44:47], v[244:247], v[60:63]\n"
"ds_read_b64_tr_b16 v[36:37], v4 offset:45056\n"
"v_mfma_f32_16x16x32_bf16 v[56:59], v[44:47], v[240:243], v[56:59]\n"
"ds_read_b64_tr_b16 v[38:39], v4 offset:45568\n"
"s_add_u32 m0, s77, 28672\n"
"s_add_u32 s46, s11, 2359296\n"
"buffer_load_dwordx4 v2, s[56:59], s46 offen lds\n"
"s_waitcnt lgkmcnt(6)\n"
"v_mfma_f32_16x16x32_bf16 v[64:67], v[24:27], v[240:243], v[64:67]\n"
"ds_read_b64_tr_b16 v[40:41], v5 offset:32768\n"
"v_mfma_f32_16x16x32_bf16 v[68:71], v[24:27], v[244:247], v[68:71]\n"
"ds_read_b64_tr_b16 v[42:43], v5 offset:33280\n"
"s_waitcnt lgkmcnt(6)\n"
"v_mfma_f32_16x16x32_bf16 v[76:79], v[28:31], v[244:247], v[76:79]\n"
"ds_read_b64_tr_b16 v[44:45], v5 offset:45056\n"
"v_mfma_f32_16x16x32_bf16 v[72:75], v[28:31], v[240:243], v[72:75]\n"
"ds_read_b64_tr_b16 v[46:47], v5 offset:45568\n"
"s_add_u32 m0, s77, 32768\n"
"s_add_u32 s46, s12, 2359296\n"
"buffer_load_dwordx4 v2, s[56:59], s46 offen lds\n"
"s_waitcnt vmcnt(14)\n"
"s_barrier\n"
"s_waitcnt lgkmcnt(6)\n"
"v_mfma_f32_16x16x32_bf16 v[48:51], v[32:35], v[248:251], v[48:51]\n"
"v_add_u32 v4, 49152, v22\n"
"ds_read_b64_tr_b16 v[24:25], v4\n"
"s_setprio 3\n"
"v_mfma_f32_16x16x32_bf16 v[52:55], v[32:35], v[252:255], v[52:55]\n"
"ds_read_b64_tr_b16 v[26:27], v4 offset:2048\n"
"s_waitcnt lgkmcnt(6)\n"
"v_mfma_f32_16x16x32_bf16 v[60:63], v[36:39], v[252:255], v[60:63]\n"
"v_xor_b32 v5, 32, v4\n"
"ds_read_b64_tr_b16 v[28:29], v5\n"
"v_mfma_f32_16x16x32_bf16 v[56:59], v[36:39], v[248:251], v[56:59]\n"
"ds_read_b64_tr_b16 v[30:31], v5 offset:2048\n"
"buffer_load_dwordx4 v[168:171], v1, s[48:51], s22 offen offset:1024 sc1\n"
"s_waitcnt lgkmcnt(6)\n"
"v_mfma_f32_16x16x32_bf16 v[64:67], v[40:43], v[248:251], v[64:67]\n"
"v_xor_b32 v6, 64, v4\n"
"ds_read_b64_tr_b16 v[32:33], v6\n"
"v_mfma_f32_16x16x32_bf16 v[68:71], v[40:43], v[252:255], v[68:71]\n"
"ds_read_b64_tr_b16 v[34:35], v6 offset:2048\n"
"s_waitcnt lgkmcnt(6)\n"
"v_mfma_f32_16x16x32_bf16 v[76:79], v[44:47], v[252:255], v[76:79]\n"
"v_xor_b32 v7, 96, v4\n"
"ds_read_b64_tr_b16 v[36:37], v7\n"
"v_mfma_f32_16x16x32_bf16 v[72:75], v[44:47], v[248:251], v[72:75]\n"
"ds_read_b64_tr_b16 v[38:39], v7 offset:2048\n"
"s_add_u32 s46, s22, s81\n"
"buffer_load_dwordx4 v[172:175], v1, s[48:51], s46 offen offset:1024 sc1\n"
"s_waitcnt lgkmcnt(6)\n"
"v_mfma_f32_16x16x32_bf16 v[80:83], v[24:27], v[112:115], 0\n"
"ds_read_b64_tr_b16 v[40:41], v4 offset:4096\n"
"v_exp_f32 v8, -v48\n"
"v_mfma_f32_16x16x32_bf16 v[84:87], v[24:27], v[116:119], 0\n"
"ds_read_b64_tr_b16 v[42:43], v4 offset:6144\n"
"v_exp_f32 v9, -v49\n"
"s_mov_b64 exec, 0xf\n"
"buffer_load_dwordx2 v[10:11], v19, s[48:51], 0 offen sc1\n"
"s_mov_b64 exec, -1\n"
"s_waitcnt lgkmcnt(6)\n"
"v_mfma_f32_16x16x32_bf16 v[92:95], v[28:31], v[116:119], 0\n"
"ds_read_b64_tr_b16 v[44:45], v5 offset:4096\n"
"v_add_f32 v8, 1.0, v8\n"
"v_mfma_f32_16x16x32_bf16 v[88:91], v[28:31], v[112:115], 0\n"
"ds_read_b64_tr_b16 v[46:47], v5 offset:6144\n"
"v_add_f32 v9, 1.0, v9\n"
"s_waitcnt lgkmcnt(6)\n"
"v_mfma_f32_16x16x32_bf16 v[96:99], v[32:35], v[112:115], 0\n"
"ds_read_b64_tr_b16 v[24:25], v6 offset:4096\n"
"s_setprio 2\n"
"v_rcp_f32 v8, v8\n"
"v_mfma_f32_16x16x32_bf16 v[100:103], v[32:35], v[116:119], 0\n"
"ds_read_b64_tr_b16 v[26:27], v6 offset:6144\n"
"v_rcp_f32 v9, v9\n"
"s_add_u32 s46, s23, 0\n"
"v_lshlrev_b32 v3, 3, v0\n"
"buffer_load_dwordx2 v[176:177], v3, s[48:51], s46 offen sc1\n"
"buffer_load_dwordx2 v[178:179], v3, s[48:51], s46 offen offset:512 sc1\n"
"s_waitcnt lgkmcnt(6)\n"
"v_mfma_f32_16x16x32_bf16 v[108:111], v[36:39], v[116:119], 0\n"
"ds_read_b64_tr_b16 v[28:29], v7 offset:4096\n"
"v_mul_f32 v8, v8, v56\n"
"v_mfma_f32_16x16x32_bf16 v[104:107], v[36:39], v[112:115], 0\n"
"ds_read_b64_tr_b16 v[30:31], v7 offset:6144\n"
"v_mul_f32 v9, v9, v57\n"
"s_waitcnt lgkmcnt(6)\n"
"v_mfma_f32_16x16x32_bf16 v[80:83], v[40:43], v[120:123], v[80:83]\n"
"ds_read_b64_tr_b16 v[32:33], v4 offset:8192\n"
"v_cvt_pk_bf16_f32 v48, v8, v9\n"
"v_mfma_f32_16x16x32_bf16 v[84:87], v[40:43], v[124:127], v[84:87]\n"
"ds_read_b64_tr_b16 v[34:35], v4 offset:10240\n"
"v_exp_f32 v8, -v50\n"
"s_add_u32 s46, s23, 0\n"
"s_add_u32 s46, s46, s81\n"
"v_lshlrev_b32 v3, 3, v0\n"
"buffer_load_dwordx2 v[180:181], v3, s[48:51], s46 offen sc1\n"
"buffer_load_dwordx2 v[182:183], v3, s[48:51], s46 offen offset:512 sc1\n"
"s_waitcnt lgkmcnt(6)\n"
"v_mfma_f32_16x16x32_bf16 v[92:95], v[44:47], v[124:127], v[92:95]\n"
"ds_read_b64_tr_b16 v[36:37], v5 offset:8192\n"
"v_exp_f32 v9, -v51\n"
"v_mfma_f32_16x16x32_bf16 v[88:91], v[44:47], v[120:123], v[88:91]\n"
"ds_read_b64_tr_b16 v[38:39], v5 offset:10240\n"
"v_add_f32 v8, 1.0, v8\n"
"s_waitcnt lgkmcnt(6)\n"
"v_mfma_f32_16x16x32_bf16 v[96:99], v[24:27], v[120:123], v[96:99]\n"
"ds_read_b64_tr_b16 v[40:41], v6 offset:8192\n"
"v_add_f32 v9, 1.0, v9\n"
"v_mfma_f32_16x16x32_bf16 v[100:103], v[24:27], v[124:127], v[100:103]\n"
"ds_read_b64_tr_b16 v[42:43], v6 offset:10240\n"
"v_rcp_f32 v8, v8\n"
"s_add_u32 s46, s23, 1024\n"
"v_lshlrev_b32 v3, 3, v0\n"
"buffer_load_dwordx2 v[184:185], v3, s[48:51], s46 offen sc1\n"
"buffer_load_dwordx2 v[186:187], v3, s[48:51], s46 offen offset:512 sc1\n"
"s_waitcnt lgkmcnt(6)\n"
"v_mfma_f32_16x16x32_bf16 v[108:111], v[28:31], v[124:127], v[108:111]\n"
"ds_read_b64_tr_b16 v[44:45], v7 offset:8192\n"
"v_rcp_f32 v9, v9\n"
"v_mfma_f32_16x16x32_bf16 v[104:107], v[28:31], v[120:123], v[104:107]\n"
"ds_read_b64_tr_b16 v[46:47], v7 offset:10240\n"
"v_mul_f32 v8, v8, v58\n"
"s_waitcnt lgkmcnt(6)\n"
"v_mfma_f32_16x16x32_bf16 v[80:83], v[32:35], v[128:131], v[80:83]\n"
"ds_read_b64_tr_b16 v[24:25], v4 offset:24576\n"
"s_setprio 1\n"
"v_mul_f32 v9, v9, v59\n"
"v_mfma_f32_16x16x32_bf16 v[84:87], v[32:35], v[132:135], v[84:87]\n"
"ds_read_b64_tr_b16 v[26:27], v4 offset:26624\n"
"v_cvt_pk_bf16_f32 v49, v8, v9\n"
"s_add_u32 s46, s23, 1024\n"
"s_add_u32 s46, s46, s81\n"
"v_lshlrev_b32 v3, 3, v0\n"
"buffer_load_dwordx2 v[188:189], v3, s[48:51], s46 offen sc1\n"
"buffer_load_dwordx2 v[190:191], v3, s[48:51], s46 offen offset:512 sc1\n"
"s_waitcnt lgkmcnt(6)\n"
"v_mfma_f32_16x16x32_bf16 v[92:95], v[36:39], v[132:135], v[92:95]\n"
"ds_read_b64_tr_b16 v[28:29], v5 offset:24576\n"
"v_exp_f32 v8, -v64\n"
"v_mfma_f32_16x16x32_bf16 v[88:91], v[36:39], v[128:131], v[88:91]\n"
"ds_read_b64_tr_b16 v[30:31], v5 offset:26624\n"
"v_exp_f32 v9, -v65\n"
"s_waitcnt lgkmcnt(6)\n"
"v_mfma_f32_16x16x32_bf16 v[96:99], v[40:43], v[128:131], v[96:99]\n"
"ds_read_b64_tr_b16 v[32:33], v6 offset:24576\n"
"v_add_f32 v8, 1.0, v8\n"
"v_mfma_f32_16x16x32_bf16 v[100:103], v[40:43], v[132:135], v[100:103]\n"
"ds_read_b64_tr_b16 v[34:35], v6 offset:26624\n"
"v_add_f32 v9, 1.0, v9\n"
"s_add_u32 m0, s77, 98304\n"
"s_add_u32 s46, s11, 128\n"
"buffer_load_dwordx4 v2, s[56:59], s46 offen lds\n"
"s_waitcnt lgkmcnt(6)\n"
"v_mfma_f32_16x16x32_bf16 v[108:111], v[44:47], v[132:135], v[108:111]\n"
"ds_read_b64_tr_b16 v[36:37], v7 offset:24576\n"
"v_rcp_f32 v8, v8\n"
"v_mfma_f32_16x16x32_bf16 v[104:107], v[44:47], v[128:131], v[104:107]\n"
"ds_read_b64_tr_b16 v[38:39], v7 offset:26624\n"
"v_rcp_f32 v9, v9\n"
"s_waitcnt lgkmcnt(6)\n"
"v_mfma_f32_16x16x32_bf16 v[80:83], v[24:27], v[136:139], v[80:83]\n"
"ds_read_b64_tr_b16 v[40:41], v4 offset:28672\n"
"v_mul_f32 v8, v8, v72\n"
"s_waitcnt vmcnt(25)\n"
"v_mfma_f32_16x16x32_bf16 v[84:87], v[24:27], v[140:143], v[84:87]\n"
"ds_read_b64_tr_b16 v[42:43], v4 offset:30720\n"
"v_mul_f32 v9, v9, v73\n"
"s_or_b32 s40, s36, 4\n"
"s_waitcnt vmcnt(9)\n"
"s_mov_b32 s44, 0x40000\n"
"s_mov_b64 exec, 0xf\n"
".Lp6:\n"
"v_cmp_le_u64 vcc, s[40:41], v[10:11]\n"
"v_cmp_ge_u64 s[42:43], s[38:39], v[10:11]\n"
"s_nop 1\n"
"s_and_b64 s[42:43], s[42:43], vcc\n"
"s_cmp_eq_u64 s[42:43], exec\n"
"s_cbranch_scc1 .Ld6\n"
"s_sub_u32 s44, s44, 1\n"
"s_cbranch_scc1 .Ld6\n"
"s_sleep 1\n"
"buffer_load_dwordx2 v[10:11], v19, s[48:51], 0 offen sc1\n"
"s_waitcnt vmcnt(0)\n"
"s_branch .Lp6\n"
".Ld6:\n"
"s_mov_b64 exec, -1\n"
"s_add_u32 m0, s77, 102400\n"
"s_add_u32 s46, s12, 128\n"
"buffer_load_dwordx4 v2, s[56:59], s46 offen lds\n"
"s_waitcnt lgkmcnt(6)\n"
"v_mfma_f32_16x16x32_bf16 v[92:95], v[28:31], v[140:143], v[92:95]\n"
"ds_read_b64_tr_b16 v[44:45], v5 offset:28672\n"
"v_cvt_pk_bf16_f32 v50, v8, v9\n"
"v_mfma_f32_16x16x32_bf16 v[88:91], v[28:31], v[136:139], v[88:91]\n"
"ds_read_b64_tr_b16 v[46:47], v5 offset:30720\n"
"v_exp_f32 v8, -v66\n"
"s_waitcnt lgkmcnt(6)\n"
"v_mfma_f32_16x16x32_bf16 v[96:99], v[32:35], v[136:139], v[96:99]\n"
"ds_read_b64_tr_b16 v[24:25], v6 offset:28672\n"
"s_setprio 0\n"
"v_exp_f32 v9, -v67\n"
"v_mfma_f32_16x16x32_bf16 v[100:103], v[32:35], v[140:143], v[100:103]\n"
"ds_read_b64_tr_b16 v[26:27], v6 offset:30720\n"
"v_add_f32 v8, 1.0, v8\n"
"s_waitcnt lgkmcnt(6)\n"
"v_mfma_f32_16x16x32_bf16 v[108:111], v[36:39], v[140:143], v[108:111]\n"
"ds_read_b64_tr_b16 v[28:29], v7 offset:28672\n"
"v_add_f32 v9, 1.0, v9\n"
"v_mfma_f32_16x16x32_bf16 v[104:107], v[36:39], v[136:139], v[104:107]\n"
"ds_read_b64_tr_b16 v[30:31], v7 offset:30720\n"
"v_rcp_f32 v8, v8\n"
"s_add_u32 s46, s19, 65536\n"
"s_branch .Lsk9_0\n"
".Lst9_0:\n"
"s_branch .Lbk9\n"
".Lsk9_0:\n"
"buffer_load_dwordx4 v[192:195], v1, s[48:51], s46 offen sc1\n"
"s_waitcnt lgkmcnt(6)\n"
"v_mfma_f32_16x16x32_bf16 v[80:83], v[40:43], v[144:147], v[80:83]\n"
"ds_read_b64_tr_b16 v[32:33], v4 offset:32768\n"
"v_rcp_f32 v9, v9\n"
"v_mfma_f32_16x16x32_bf16 v[84:87], v[40:43], v[148:151], v[84:87]\n"
"ds_read_b64_tr_b16 v[34:35], v4 offset:34816\n"
"v_mul_f32 v8, v8, v74\n"
"s_waitcnt lgkmcnt(6)\n"
"v_mfma_f32_16x16x32_bf16 v[92:95], v[44:47], v[148:151], v[92:95]\n"
"ds_read_b64_tr_b16 v[36:37], v5 offset:32768\n"
"v_mul_f32 v9, v9, v75\n"
"v_mfma_f32_16x16x32_bf16 v[88:91], v[44:47], v[144:147], v[88:91]\n"
"ds_read_b64_tr_b16 v[38:39], v5 offset:34816\n"
"v_cvt_pk_bf16_f32 v51, v8, v9\n"
"s_add_u32 s46, s9, 66560\n"
"buffer_store_dwordx4 v[48:51], v1, s[48:51], s46 offen sc1\n"
"s_add_u32 s46, s19, 65536\n"
"s_add_u32 s46, s46, s81\n"
"buffer_load_dwordx4 v[196:199], v1, s[48:51], s46 offen sc1\n"
"s_waitcnt lgkmcnt(6)\n"
"v_mfma_f32_16x16x32_bf16 v[96:99], v[24:27], v[144:147], v[96:99]\n"
"ds_read_b64_tr_b16 v[40:41], v6 offset:32768\n"
"v_exp_f32 v8, -v52\n"
"v_mfma_f32_16x16x32_bf16 v[100:103], v[24:27], v[148:151], v[100:103]\n"
"ds_read_b64_tr_b16 v[42:43], v6 offset:34816\n"
"v_exp_f32 v9, -v53\n"
"s_waitcnt lgkmcnt(6)\n"
"v_mfma_f32_16x16x32_bf16 v[108:111], v[28:31], v[148:151], v[108:111]\n"
"ds_read_b64_tr_b16 v[44:45], v7 offset:32768\n"
"v_add_f32 v8, 1.0, v8\n"
"v_mfma_f32_16x16x32_bf16 v[104:107], v[28:31], v[144:147], v[104:107]\n"
"ds_read_b64_tr_b16 v[46:47], v7 offset:34816\n"
"v_add_f32 v9, 1.0, v9\n"
"s_barrier\n"
"s_waitcnt lgkmcnt(6)\n"
"v_mfma_f32_16x16x32_bf16 v[80:83], v[32:35], v[152:155], v[80:83]\n"
"v_add_u32 v4, 0, v22\n"
"ds_read_b64_tr_b16 v[24:25], v4\n"
"s_setprio 3\n"
"v_rcp_f32 v8, v8\n"
"v_mfma_f32_16x16x32_bf16 v[84:87], v[32:35], v[156:159], v[84:87]\n"
"ds_read_b64_tr_b16 v[26:27], v4 offset:2048\n"
"v_rcp_f32 v9, v9\n"
"s_waitcnt lgkmcnt(6)\n"
"v_mfma_f32_16x16x32_bf16 v[92:95], v[36:39], v[156:159], v[92:95]\n"
"v_xor_b32 v5, 32, v4\n"
"ds_read_b64_tr_b16 v[28:29], v5\n"
"v_mul_f32 v8, v8, v60\n"
"v_mfma_f32_16x16x32_bf16 v[88:91], v[36:39], v[152:155], v[88:91]\n"
"ds_read_b64_tr_b16 v[30:31], v5 offset:2048\n"
"v_mul_f32 v9, v9, v61\n"
"s_add_u32 s46, s20, 65536\n"
"buffer_load_dwordx4 v[200:203], v1, s[48:51], s46 offen sc1\n"
"s_waitcnt lgkmcnt(6)\n"
"v_mfma_f32_16x16x32_bf16 v[96:99], v[40:43], v[152:155], v[96:99]\n"
"v_xor_b32 v6, 64, v4\n"
"ds_read_b64_tr_b16 v[32:33], v6\n"
"v_cvt_pk_bf16_f32 v52, v8, v9\n"
"v_mfma_f32_16x16x32_bf16 v[100:103], v[40:43], v[156:159], v[100:103]\n"
"ds_read_b64_tr_b16 v[34:35], v6 offset:2048\n"
"v_exp_f32 v8, -v54\n"
"s_waitcnt lgkmcnt(6)\n"
"v_mfma_f32_16x16x32_bf16 v[108:111], v[44:47], v[156:159], v[108:111]\n"
"v_xor_b32 v7, 96, v4\n"
"ds_read_b64_tr_b16 v[36:37], v7\n"
"v_exp_f32 v9, -v55\n"
"v_mfma_f32_16x16x32_bf16 v[104:107], v[44:47], v[152:155], v[104:107]\n"
"ds_read_b64_tr_b16 v[38:39], v7 offset:2048\n"
"v_add_f32 v8, 1.0, v8\n"
"s_add_u32 s46, s20, 65536\n"
"s_add_u32 s46, s46, s81\n"
"buffer_load_dwordx4 v[204:207], v1, s[48:51], s46 offen sc1\n"
"s_waitcnt lgkmcnt(6)\n"
"v_mfma_f32_16x16x32_bf16 v[80:83], v[24:27], v[160:163], v[80:83]\n"
"ds_read_b64_tr_b16 v[40:41], v4 offset:4096\n"
"v_add_f32 v9, 1.0, v9\n"
"v_mfma_f32_16x16x32_bf16 v[84:87], v[24:27], v[164:167], v[84:87]\n"
"ds_read_b64_tr_b16 v[42:43], v4 offset:6144\n"
"v_rcp_f32 v8, v8\n"
"s_waitcnt lgkmcnt(6)\n"
"v_mfma_f32_16x16x32_bf16 v[92:95], v[28:31], v[164:167], v[92:95]\n"
"ds_read_b64_tr_b16 v[44:45], v5 offset:4096\n"
"v_rcp_f32 v9, v9\n"
"v_mfma_f32_16x16x32_bf16 v[88:91], v[28:31], v[160:163], v[88:91]\n"
"ds_read_b64_tr_b16 v[46:47], v5 offset:6144\n"
"v_mul_f32 v8, v8, v62\n"
"s_waitcnt lgkmcnt(6)\n"
"v_mfma_f32_16x16x32_bf16 v[96:99], v[32:35], v[160:163], v[96:99]\n"
"ds_read_b64_tr_b16 v[24:25], v6 offset:4096\n"
"s_setprio 2\n"
"v_mul_f32 v9, v9, v63\n"
"v_mfma_f32_16x16x32_bf16 v[100:103], v[32:35], v[164:167], v[100:103]\n"
"ds_read_b64_tr_b16 v[26:27], v6 offset:6144\n"
"v_cvt_pk_bf16_f32 v53, v8, v9\n"
"s_add_u32 m0, s77, 106496\n"
"s_add_u32 s46, s13, 128\n"
"buffer_load_dwordx4 v2, s[56:59], s46 offen lds\n"
"s_waitcnt lgkmcnt(6)\n"
"v_mfma_f32_16x16x32_bf16 v[108:111], v[36:39], v[164:167], v[108:111]\n"
"ds_read_b64_tr_b16 v[28:29], v7 offset:4096\n"
"v_exp_f32 v8, -v68\n"
"v_mfma_f32_16x16x32_bf16 v[104:107], v[36:39], v[160:163], v[104:107]\n"
"ds_read_b64_tr_b16 v[30:31], v7 offset:6144\n"
"v_exp_f32 v9, -v69\n"
"s_waitcnt lgkmcnt(6)\n"
"v_mfma_f32_16x16x32_bf16 v[80:83], v[40:43], v[168:171], v[80:83]\n"
"ds_read_b64_tr_b16 v[32:33], v4 offset:8192\n"
"v_add_f32 v8, 1.0, v8\n"
"v_mfma_f32_16x16x32_bf16 v[84:87], v[40:43], v[172:175], v[84:87]\n"
"ds_read_b64_tr_b16 v[34:35], v4 offset:10240\n"
"v_add_f32 v9, 1.0, v9\n"
"s_add_u32 m0, s77, 122880\n"
"s_add_u32 s46, s14, 128\n"
"buffer_load_dwordx4 v2, s[56:59], s46 offen lds\n"
"s_waitcnt lgkmcnt(6)\n"
"v_mfma_f32_16x16x32_bf16 v[92:95], v[44:47], v[172:175], v[92:95]\n"
"ds_read_b64_tr_b16 v[36:37], v5 offset:8192\n"
"v_rcp_f32 v8, v8\n"
"v_mfma_f32_16x16x32_bf16 v[88:91], v[44:47], v[168:171], v[88:91]\n"
"ds_read_b64_tr_b16 v[38:39], v5 offset:10240\n"
"v_rcp_f32 v9, v9\n"
"s_waitcnt lgkmcnt(6)\n"
"v_mfma_f32_16x16x32_bf16 v[96:99], v[24:27], v[168:171], v[96:99]\n"
"ds_read_b64_tr_b16 v[40:41], v6 offset:8192\n"
"v_mul_f32 v8, v8, v76\n"
"v_mfma_f32_16x16x32_bf16 v[100:103], v[24:27], v[172:175], v[100:103]\n"
"ds_read_b64_tr_b16 v[42:43], v6 offset:10240\n"
"v_mul_f32 v9, v9, v77\n"
"s_add_u32 m0, s77, 126976\n"
"s_add_u32 s46, s11, 36992\n"
"buffer_load_dwordx4 v2, s[56:59], s46 offen lds\n"
"s_waitcnt lgkmcnt(6)\n"
"v_mfma_f32_16x16x32_bf16 v[108:111], v[28:31], v[172:175], v[108:111]\n"
"ds_read_b64_tr_b16 v[44:45], v7 offset:8192\n"
"v_cvt_pk_bf16_f32 v54, v8, v9\n"
"v_mfma_f32_16x16x32_bf16 v[104:107], v[28:31], v[168:171], v[104:107]\n"
"ds_read_b64_tr_b16 v[46:47], v7 offset:10240\n"
"v_exp_f32 v8, -v70\n"
"s_waitcnt vmcnt(16) lgkmcnt(6)\n"
"v_mfma_f32_16x16x32_bf16 v[80:83], v[32:35], v[176:179], v[80:83]\n"
"ds_read_b64_tr_b16 v[24:25], v4 offset:24576\n"
"s_setprio 1\n"
"v_exp_f32 v9, -v71\n"
"s_waitcnt vmcnt(14)\n"
"v_mfma_f32_16x16x32_bf16 v[84:87], v[32:35], v[180:183], v[84:87]\n"
"ds_read_b64_tr_b16 v[26:27], v4 offset:26624\n"
"v_add_f32 v8, 1.0, v8\n"
"s_add_u32 m0, s77, 131072\n"
"s_add_u32 s46, s12, 36992\n"
"buffer_load_dwordx4 v2, s[56:59], s46 offen lds\n"
"s_waitcnt lgkmcnt(6)\n"
"v_mfma_f32_16x16x32_bf16 v[92:95], v[36:39], v[180:183], v[92:95]\n"
"ds_read_b64_tr_b16 v[28:29], v5 offset:24576\n"
"v_add_f32 v9, 1.0, v9\n"
"v_mfma_f32_16x16x32_bf16 v[88:91], v[36:39], v[176:179], v[88:91]\n"
"ds_read_b64_tr_b16 v[30:31], v5 offset:26624\n"
"v_rcp_f32 v8, v8\n"
"s_waitcnt lgkmcnt(6)\n"
"v_mfma_f32_16x16x32_bf16 v[96:99], v[40:43], v[176:179], v[96:99]\n"
"ds_read_b64_tr_b16 v[32:33], v6 offset:24576\n"
"v_rcp_f32 v9, v9\n"
"v_mfma_f32_16x16x32_bf16 v[100:103], v[40:43], v[180:183], v[100:103]\n"
"ds_read_b64_tr_b16 v[34:35], v6 offset:26624\n"
"v_mul_f32 v8, v8, v78\n"
"s_add_u32 m0, s77, 49152\n"
"s_add_u32 s46, s13, 36992\n"
"buffer_load_dwordx4 v2, s[56:59], s46 offen lds\n"
"s_waitcnt lgkmcnt(6)\n"
"v_mfma_f32_16x16x32_bf16 v[108:111], v[44:47], v[180:183], v[108:111]\n"
"ds_read_b64_tr_b16 v[36:37], v7 offset:24576\n"
"v_mul_f32 v9, v9, v79\n"
"v_mfma_f32_16x16x32_bf16 v[104:107], v[44:47], v[176:179], v[104:107]\n"
"ds_read_b64_tr_b16 v[38:39], v7 offset:26624\n"
"v_cvt_pk_bf16_f32 v55, v8, v9\n"
"s_add_u32 s46, s9, 214016\n"
"buffer_store_dwordx4 v[52:55], v1, s[48:51], s46 offen sc1\n"
"s_waitcnt vmcnt(15) lgkmcnt(6)\n"
"v_mfma_f32_16x16x32_bf16 v[80:83], v[24:27], v[184:187], v[80:83]\n"
"ds_read_b64_tr_b16 v[40:41], v4 offset:28672\n"
"s_waitcnt vmcnt(13)\n"
"v_mfma_f32_16x16x32_bf16 v[84:87], v[24:27], v[188:191], v[84:87]\n"
"ds_read_b64_tr_b16 v[42:43], v4 offset:30720\n"
"s_waitcnt lgkmcnt(6)\n"
"v_mfma_f32_16x16x32_bf16 v[92:95], v[28:31], v[188:191], v[92:95]\n"
"ds_read_b64_tr_b16 v[44:45], v5 offset:28672\n"
"v_mfma_f32_16x16x32_bf16 v[88:91], v[28:31], v[184:187], v[88:91]\n"
"ds_read_b64_tr_b16 v[46:47], v5 offset:30720\n"
"s_waitcnt lgkmcnt(6)\n"
"v_mfma_f32_16x16x32_bf16 v[96:99], v[32:35], v[184:187], v[96:99]\n"
"ds_read_b64_tr_b16 v[24:25], v6 offset:28672\n"
"s_setprio 0\n"
"v_mfma_f32_16x16x32_bf16 v[100:103], v[32:35], v[188:191], v[100:103]\n"
"ds_read_b64_tr_b16 v[26:27], v6 offset:30720\n"
"s_waitcnt lgkmcnt(6)\n"
"v_mfma_f32_16x16x32_bf16 v[108:111], v[36:39], v[188:191], v[108:111]\n"
"ds_read_b64_tr_b16 v[28:29], v7 offset:28672\n"
"v_mfma_f32_16x16x32_bf16 v[104:107], v[36:39], v[184:187], v[104:107]\n"
"ds_read_b64_tr_b16 v[30:31], v7 offset:30720\n"
"s_add_u32 m0, s77, 53248\n"
"s_add_u32 s46, s14, 36992\n"
"buffer_load_dwordx4 v2, s[56:59], s46 offen lds\n"
"s_waitcnt vmcnt(11) lgkmcnt(6)\n"
"v_mfma_f32_16x16x32_bf16 v[80:83], v[40:43], v[192:195], v[80:83]\n"
"ds_read_b64_tr_b16 v[32:33], v4 offset:32768\n"
"s_waitcnt vmcnt(9)\n"
"v_mfma_f32_16x16x32_bf16 v[84:87], v[40:43], v[196:199], v[84:87]\n"
"ds_read_b64_tr_b16 v[34:35], v4 offset:34816\n"
"s_waitcnt lgkmcnt(6)\n"
"v_mfma_f32_16x16x32_bf16 v[92:95], v[44:47], v[196:199], v[92:95]\n"
"ds_read_b64_tr_b16 v[36:37], v5 offset:32768\n"
"v_mfma_f32_16x16x32_bf16 v[88:91], v[44:47], v[192:195], v[88:91]\n"
"ds_read_b64_tr_b16 v[38:39], v5 offset:34816\n"
"s_add_u32 m0, s77, 57344\n"
"s_add_u32 s46, s15, 128\n"
"buffer_load_dwordx4 v2, s[56:59], s46 offen lds\n"
"s_waitcnt lgkmcnt(6)\n"
"v_mfma_f32_16x16x32_bf16 v[96:99], v[24:27], v[192:195], v[96:99]\n"
"ds_read_b64_tr_b16 v[40:41], v6 offset:32768\n"
"v_mfma_f32_16x16x32_bf16 v[100:103], v[24:27], v[196:199], v[100:103]\n"
"ds_read_b64_tr_b16 v[42:43], v6 offset:34816\n"
"s_waitcnt lgkmcnt(6)\n"
"v_mfma_f32_16x16x32_bf16 v[108:111], v[28:31], v[196:199], v[108:111]\n"
"ds_read_b64_tr_b16 v[44:45], v7 offset:32768\n"
"v_mfma_f32_16x16x32_bf16 v[104:107], v[28:31], v[192:195], v[104:107]\n"
"ds_read_b64_tr_b16 v[46:47], v7 offset:34816\n"
"s_add_u32 s46, s21, 65536\n"
"buffer_load_dwordx4 v[208:211], v1, s[48:51], s46 offen sc1\n"
"s_waitcnt vmcnt(5)\n"
"s_barrier\n"
"s_waitcnt lgkmcnt(6)\n"
"v_mfma_f32_16x16x32_bf16 v[80:83], v[32:35], v[200:203], v[80:83]\n"
"v_add_u32 v4, 98304, v22\n"
"ds_read_b64_tr_b16 v[24:25], v4\n"
"s_setprio 3\n"
"v_mfma_f32_16x16x32_bf16 v[84:87], v[32:35], v[204:207], v[84:87]\n"
"ds_read_b64_tr_b16 v[26:27], v4 offset:2048\n"
"s_waitcnt lgkmcnt(6)\n"
"v_mfma_f32_16x16x32_bf16 v[92:95], v[36:39], v[204:207], v[92:95]\n"
"v_xor_b32 v5, 32, v4\n"
"ds_read_b64_tr_b16 v[28:29], v5\n"
"v_mfma_f32_16x16x32_bf16 v[88:91], v[36:39], v[200:203], v[88:91]\n"
"ds_read_b64_tr_b16 v[30:31], v5 offset:2048\n"
"s_add_u32 m0, s77, 73728\n"
"s_add_u32 s46, s15, 36992\n"
"buffer_load_dwordx4 v2, s[56:59], s46 offen lds\n"
"s_waitcnt lgkmcnt(6)\n"
"v_mfma_f32_16x16x32_bf16 v[96:99], v[40:43], v[200:203], v[96:99]\n"
"v_xor_b32 v6, 64, v4\n"
"ds_read_b64_tr_b16 v[32:33], v6\n"
"v_mfma_f32_16x16x32_bf16 v[100:103], v[40:43], v[204:207], v[100:103]\n"
"ds_read_b64_tr_b16 v[34:35], v6 offset:2048\n"
"s_waitcnt lgkmcnt(6)\n"
"v_mfma_f32_16x16x32_bf16 v[108:111], v[44:47], v[204:207], v[108:111]\n"
"v_xor_b32 v7, 96, v4\n"
"ds_read_b64_tr_b16 v[36:37], v7\n"
"v_mfma_f32_16x16x32_bf16 v[104:107], v[44:47], v[200:203], v[104:107]\n"
"ds_read_b64_tr_b16 v[38:39], v7 offset:2048\n"
"s_add_u32 m0, s77, 77824\n"
"s_add_u32 s46, s11, 2359424\n"
"buffer_load_dwordx4 v2, s[56:59], s46 offen lds\n"
"s_waitcnt lgkmcnt(6)\n"
"v_mfma_f32_16x16x32_bf16 v[48:51], v[24:27], v[112:115], 0\n"
"ds_read_b64_tr_b16 v[40:41], v4 offset:4096\n"
"v_mfma_f32_16x16x32_bf16 v[52:55], v[24:27], v[116:119], 0\n"
"ds_read_b64_tr_b16 v[42:43], v4 offset:6144\n"
"s_waitcnt lgkmcnt(6)\n"
"v_mfma_f32_16x16x32_bf16 v[60:63], v[28:31], v[116:119], 0\n"
"ds_read_b64_tr_b16 v[44:45], v5 offset:4096\n"
"v_mfma_f32_16x16x32_bf16 v[56:59], v[28:31], v[112:115], 0\n"
"ds_read_b64_tr_b16 v[46:47], v5 offset:6144\n"
"s_add_u32 m0, s77, 81920\n"
"s_add_u32 s46, s12, 2359424\n"
"buffer_load_dwordx4 v2, s[56:59], s46 offen lds\n"
"s_waitcnt lgkmcnt(6)\n"
"v_mfma_f32_16x16x32_bf16 v[64:67], v[32:35], v[112:115], 0\n"
"ds_read_b64_tr_b16 v[24:25], v6 offset:4096\n"
"s_setprio 2\n"
"v_mfma_f32_16x16x32_bf16 v[68:71], v[32:35], v[116:119], 0\n"
"ds_read_b64_tr_b16 v[26:27], v6 offset:6144\n"
"s_waitcnt lgkmcnt(6)\n"
"v_mfma_f32_16x16x32_bf16 v[76:79], v[36:39], v[116:119], 0\n"
"ds_read_b64_tr_b16 v[28:29], v7 offset:4096\n"
"v_mfma_f32_16x16x32_bf16 v[72:75], v[36:39], v[112:115], 0\n"
"ds_read_b64_tr_b16 v[30:31], v7 offset:6144\n"
"s_waitcnt lgkmcnt(6)\n"
"v_mfma_f32_16x16x32_bf16 v[48:51], v[40:43], v[120:123], v[48:51]\n"
"ds_read_b64_tr_b16 v[32:33], v4 offset:8192\n"
"v_mfma_f32_16x16x32_bf16 v[52:55], v[40:43], v[124:127], v[52:55]\n"
"ds_read_b64_tr_b16 v[34:35], v4 offset:10240\n"
"s_add_u32 s46, s21, 65536\n"
"s_add_u32 s46, s46, s81\n"
"buffer_load_dwordx4 v[212:215], v1, s[48:51], s46 offen sc1\n"
"s_waitcnt lgkmcnt(6)\n"
"v_mfma_f32_16x16x32_bf16 v[60:63], v[44:47], v[124:127], v[60:63]\n"
"ds_read_b64_tr_b16 v[36:37], v5 offset:8192\n"
"v_mfma_f32_16x16x32_bf16 v[56:59], v[44:47], v[120:123], v[56:59]\n"
"ds_read_b64_tr_b16 v[38:39], v5 offset:10240\n"
"s_or_b32 s45, s36, 5\n"
"v_mov_b32 v16, s45\n"
"s_waitcnt lgkmcnt(6)\n"
"v_mfma_f32_16x16x32_bf16 v[64:67], v[24:27], v[120:123], v[64:67]\n"
"ds_read_b64_tr_b16 v[40:41], v6 offset:8192\n"
"v_mfma_f32_16x16x32_bf16 v[68:71], v[24:27], v[124:127], v[68:71]\n"
"ds_read_b64_tr_b16 v[42:43], v6 offset:10240\n"
"v_mov_b32 v17, s37\n"
"s_add_u32 m0, s77, 0\n"
"s_add_u32 s46, s13, 2359296\n"
"buffer_load_dwordx4 v2, s[56:59], s46 offen lds\n"
"s_waitcnt lgkmcnt(6)\n"
"v_mfma_f32_16x16x32_bf16 v[76:79], v[28:31], v[124:127], v[76:79]\n"
"ds_read_b64_tr_b16 v[44:45], v7 offset:8192\n"
"v_mfma_f32_16x16x32_bf16 v[72:75], v[28:31], v[120:123], v[72:75]\n"
"ds_read_b64_tr_b16 v[46:47], v7 offset:10240\n"
"v_lshrrev_b32 v18, 4, v0\n"
"v_lshlrev_b32 v18, 28, v18\n"
"v_lshl_add_u32 v18, v0, 3, v18\n"
"s_waitcnt lgkmcnt(6)\n"
"v_mfma_f32_16x16x32_bf16 v[48:51], v[32:35], v[128:131], v[48:51]\n"
"ds_read_b64_tr_b16 v[24:25], v4 offset:24576\n"
"s_setprio 1\n"
"v_mfma_f32_16x16x32_bf16 v[52:55], v[32:35], v[132:135], v[52:55]\n"
"ds_read_b64_tr_b16 v[26:27], v4 offset:26624\n"
"s_add_u32 s45, s79, 1048576\n"
"s_waitcnt vmcnt(8)\n"
"buffer_store_dwordx2 v[16:17], v18, s[48:51], s45 offen sc1\n"
"s_add_u32 m0, s77, 4096\n"
"s_add_u32 s46, s14, 2359296\n"
"buffer_load_dwordx4 v2, s[56:59], s46 offen lds\n"
"s_waitcnt lgkmcnt(6)\n"
"v_mfma_f32_16x16x32_bf16 v[60:63], v[36:39], v[132:135], v[60:63]\n"
"ds_read_b64_tr_b16 v[28:29], v5 offset:24576\n"
"v_mfma_f32_16x16x32_bf16 v[56:59], v[36:39], v[128:131], v[56:59]\n"
"ds_read_b64_tr_b16 v[30:31], v5 offset:26624\n"
"s_waitcnt lgkmcnt(6)\n"
"v_mfma_f32_16x16x32_bf16 v[64:67], v[40:43], v[128:131], v[64:67]\n"
"ds_read_b64_tr_b16 v[32:33], v6 offset:24576\n"
"v_mfma_f32_16x16x32_bf16 v[68:71], v[40:43], v[132:135], v[68:71]\n"
"ds_read_b64_tr_b16 v[34:35], v6 offset:26624\n"
"s_add_u32 m0, s77, 8192\n"
"s_add_u32 s46, s11, 2396160\n"
"buffer_load_dwordx4 v2, s[56:59], s46 offen lds\n"
"s_waitcnt lgkmcnt(6)\n"
"v_mfma_f32_16x16x32_bf16 v[76:79], v[44:47], v[132:135], v[76:79]\n"
"ds_read_b64_tr_b16 v[36:37], v7 offset:24576\n"
"v_mfma_f32_16x16x32_bf16 v[72:75], v[44:47], v[128:131], v[72:75]\n"
"ds_read_b64_tr_b16 v[38:39], v7 offset:26624\n"
"s_waitcnt lgkmcnt(6)\n"
"v_mfma_f32_16x16x32_bf16 v[48:51], v[24:27], v[136:139], v[48:51]\n"
"ds_read_b64_tr_b16 v[40:41], v4 offset:28672\n"
"v_mfma_f32_16x16x32_bf16 v[52:55], v[24:27], v[140:143], v[52:55]\n"
"ds_read_b64_tr_b16 v[42:43], v4 offset:30720\n"
"s_add_u32 s46, s22, 65536\n"
"buffer_load_dwordx4 v[216:219], v1, s[48:51], s46 offen sc1\n"
"s_waitcnt lgkmcnt(6)\n"
"v_mfma_f32_16x16x32_bf16 v[60:63], v[28:31], v[140:143], v[60:63]\n"
"ds_read_b64_tr_b16 v[44:45], v5 offset:28672\n"
"v_mfma_f32_16x16x32_bf16 v[56:59], v[28:31], v[136:139], v[56:59]\n"
"ds_read_b64_tr_b16 v[46:47], v5 offset:30720\n"
"s_waitcnt lgkmcnt(6)\n"
"v_mfma_f32_16x16x32_bf16 v[64:67], v[32:35], v[136:139], v[64:67]\n"
"ds_read_b64_tr_b16 v[24:25], v6 offset:28672\n"
"s_setprio 0\n"
"v_mfma_f32_16x16x32_bf16 v[68:71], v[32:35], v[140:143], v[68:71]\n"
"ds_read_b64_tr_b16 v[26:27], v6 offset:30720\n"
"s_add_u32 s46, s22, 65536\n"
"s_add_u32 s46, s46, s81\n"
"buffer_load_dwordx4 v[220:223], v1, s[48:51], s46 offen sc1\n"
"s_waitcnt lgkmcnt(6)\n"
"v_mfma_f32_16x16x32_bf16 v[76:79], v[36:39], v[140:143], v[76:79]\n"
"ds_read_b64_tr_b16 v[28:29], v7 offset:28672\n"
"v_mfma_f32_16x16x32_bf16 v[72:75], v[36:39], v[136:139], v[72:75]\n"
"ds_read_b64_tr_b16 v[30:31], v7 offset:30720\n"
"s_waitcnt lgkmcnt(6)\n"
"v_mfma_f32_16x16x32_bf16 v[48:51], v[40:43], v[144:147], v[48:51]\n"
"ds_read_b64_tr_b16 v[32:33], v4 offset:32768\n"
"v_mfma_f32_16x16x32_bf16 v[52:55], v[40:43], v[148:151], v[52:55]\n"
"ds_read_b64_tr_b16 v[34:35], v4 offset:34816\n"
"s_waitcnt lgkmcnt(6)\n"
"v_mfma_f32_16x16x32_bf16 v[60:63], v[44:47], v[148:151], v[60:63]\n"
"ds_read_b64_tr_b16 v[36:37], v5 offset:32768\n"
"v_mfma_f32_16x16x32_bf16 v[56:59], v[44:47], v[144:147], v[56:59]\n"
"ds_read_b64_tr_b16 v[38:39], v5 offset:34816\n"
"s_waitcnt lgkmcnt(6)\n"
"v_mfma_f32_16x16x32_bf16 v[64:67], v[24:27], v[144:147], v[64:67]\n"
"ds_read_b64_tr_b16 v[40:41], v6 offset:32768\n"
"v_mfma_f32_16x16x32_bf16 v[68:71], v[24:27], v[148:151], v[68:71]\n"
"ds_read_b64_tr_b16 v[42:43], v6 offset:34816\n"
"s_waitcnt lgkmcnt(6)\n"
"v_mfma_f32_16x16x32_bf16 v[76:79], v[28:31], v[148:151], v[76:79]\n"
"ds_read_b64_tr_b16 v[44:45], v7 offset:32768\n"
"v_mfma_f32_16x16x32_bf16 v[72:75], v[28:31], v[144:147], v[72:75]\n"
"ds_read_b64_tr_b16 v[46:47], v7 offset:34816\n"
"s_waitcnt vmcnt(7)\n"
"s_barrier\n"
"s_waitcnt lgkmcnt(6)\n"
"v_mfma_f32_16x16x32_bf16 v[48:51], v[32:35], v[152:155], v[48:51]\n"
"v_add_u32 v4, 49152, v22\n"
"ds_read_b64_tr_b16 v[24:25], v4\n"
"s_setprio 3\n"
"v_mfma_f32_16x16x32_bf16 v[52:55], v[32:35], v[156:159], v[52:55]\n"
"ds_read_b64_tr_b16 v[26:27], v4 offset:2048\n"
"s_mov_b64 exec, 0xf\n"
"buffer_load_dwordx2 v[10:11], v19, s[48:51], 0 offen sc1\n"
"s_mov_b64 exec, -1\n"
"s_add_u32 m0, s77, 24576\n"
"s_add_u32 s46, s12, 2396160\n"
"buffer_load_dwordx4 v2, s[56:59], s46 offen lds\n"
"s_waitcnt lgkmcnt(6)\n"
"v_mfma_f32_16x16x32_bf16 v[60:63], v[36:39], v[156:159], v[60:63]\n"
"v_xor_b32 v5, 32, v4\n"
"ds_read_b64_tr_b16 v[28:29], v5\n"
"v_mfma_f32_16x16x32_bf16 v[56:59], v[36:39], v[152:155], v[56:59]\n"
"ds_read_b64_tr_b16 v[30:31], v5 offset:2048\n"
"s_waitcnt lgkmcnt(6)\n"
"v_mfma_f32_16x16x32_bf16 v[64:67], v[40:43], v[152:155], v[64:67]\n"
"v_xor_b32 v6, 64, v4\n"
"ds_read_b64_tr_b16 v[32:33], v6\n"
"v_mfma_f32_16x16x32_bf16 v[68:71], v[40:43], v[156:159], v[68:71]\n"
"ds_read_b64_tr_b16 v[34:35], v6 offset:2048\n"
"s_add_u32 m0, s77, 28672\n"
"s_add_u32 s46, s13, 2396160\n"
"buffer_load_dwordx4 v2, s[56:59], s46 offen lds\n"
"s_waitcnt lgkmcnt(6)\n"
"v_mfma_f32_16x16x32_bf16 v[76:79], v[44:47], v[156:159], v[76:79]\n"
"v_xor_b32 v7, 96, v4\n"
"ds_read_b64_tr_b16 v[36:37], v7\n"
"v_mfma_f32_16x16x32_bf16 v[72:75], v[44:47], v[152:155], v[72:75]\n"
"ds_read_b64_tr_b16 v[38:39], v7 offset:2048\n"
"s_waitcnt lgkmcnt(6)\n"
"v_mfma_f32_16x16x32_bf16 v[48:51], v[24:27], v[160:163], v[48:51]\n"
"ds_read_b64_tr_b16 v[40:41], v4 offset:4096\n"
"v_mfma_f32_16x16x32_bf16 v[52:55], v[24:27], v[164:167], v[52:55]\n"
"ds_read_b64_tr_b16 v[42:43], v4 offset:6144\n"
"s_add_u32 m0, s77, 32768\n"
"s_add_u32 s46, s14, 2396160\n"
"buffer_load_dwordx4 v2, s[56:59], s46 offen lds\n"
"s_waitcnt lgkmcnt(6)\n"
"v_mfma_f32_16x16x32_bf16 v[60:63], v[28:31], v[164:167], v[60:63]\n"
"ds_read_b64_tr_b16 v[44:45], v5 offset:4096\n"
"v_mfma_f32_16x16x32_bf16 v[56:59], v[28:31], v[160:163], v[56:59]\n"
"ds_read_b64_tr_b16 v[46:47], v5 offset:6144\n"
"s_waitcnt lgkmcnt(6)\n"
"v_mfma_f32_16x16x32_bf16 v[64:67], v[32:35], v[160:163], v[64:67]\n"
"ds_read_b64_tr_b16 v[24:25], v6 offset:4096\n"
"s_setprio 2\n"
"v_mfma_f32_16x16x32_bf16 v[68:71], v[32:35], v[164:167], v[68:71]\n"
"ds_read_b64_tr_b16 v[26:27], v6 offset:6144\n"
"s_waitcnt lgkmcnt(6)\n"
"v_mfma_f32_16x16x32_bf16 v[76:79], v[36:39], v[164:167], v[76:79]\n"
"ds_read_b64_tr_b16 v[28:29], v7 offset:4096\n"
"v_mfma_f32_16x16x32_bf16 v[72:75], v[36:39], v[160:163], v[72:75]\n"
"ds_read_b64_tr_b16 v[30:31], v7 offset:6144\n"
"s_add_u32 m0, s77, 98304\n"
"s_add_u32 s46, s13, 2359424\n"
"buffer_load_dwordx4 v2, s[56:59], s46 offen lds\n"
"s_waitcnt lgkmcnt(6)\n"
"v_mfma_f32_16x16x32_bf16 v[48:51], v[40:43], v[168:171], v[48:51]\n"
"ds_read_b64_tr_b16 v[32:33], v4 offset:8192\n"
"v_mfma_f32_16x16x32_bf16 v[52:55], v[40:43], v[172:175], v[52:55]\n"
"ds_read_b64_tr_b16 v[34:35], v4 offset:10240\n"
"s_waitcnt lgkmcnt(6)\n"
"v_mfma_f32_16x16x32_bf16 v[60:63], v[44:47], v[172:175], v[60:63]\n"
"ds_read_b64_tr_b16 v[36:37], v5 offset:8192\n"
"v_mfma_f32_16x16x32_bf16 v[56:59], v[44:47], v[168:171], v[56:59]\n"
"ds_read_b64_tr_b16 v[38:39], v5 offset:10240\n"
"s_add_u32 m0, s77, 102400\n"
"s_add_u32 s46, s14, 2359424\n"
"buffer_load_dwordx4 v2, s[56:59], s46 offen lds\n"
"s_waitcnt lgkmcnt(6)\n"
"v_mfma_f32_16x16x32_bf16 v[64:67], v[24:27], v[168:171], v[64:67]\n"
"ds_read_b64_tr_b16 v[40:41], v6 offset:8192\n"
"v_mfma_f32_16x16x32_bf16 v[68:71], v[24:27], v[172:175], v[68:71]\n"
"ds_read_b64_tr_b16 v[42:43], v6 offset:10240\n"
"s_waitcnt lgkmcnt(6)\n"
"v_mfma_f32_16x16x32_bf16 v[76:79], v[28:31], v[172:175], v[76:79]\n"
"ds_read_b64_tr_b16 v[44:45], v7 offset:8192\n"
"v_mfma_f32_16x16x32_bf16 v[72:75], v[28:31], v[168:171], v[72:75]\n"
"ds_read_b64_tr_b16 v[46:47], v7 offset:10240\n"
"s_add_u32 m0, s77, 106496\n"
"s_add_u32 s46, s11, 2396288\n"
"buffer_load_dwordx4 v2, s[56:59], s46 offen lds\n"
"s_waitcnt lgkmcnt(6)\n"
"v_mfma_f32_16x16x32_bf16 v[48:51], v[32:35], v[176:179], v[48:51]\n"
"ds_read_b64_tr_b16 v[24:25], v4 offset:24576\n"
"s_setprio 1\n"
"v_mfma_f32_16x16x32_bf16 v[52:55], v[32:35], v[180:183], v[52:55]\n"
"ds_read_b64_tr_b16 v[26:27], v4 offset:26624\n"
"s_or_b32 s40, s36, 5\n"
"s_waitcnt vmcnt(6)\n"
"s_mov_b32 s44, 0x40000\n"
"s_mov_b64 exec, 0xf\n"
".Lp7:\n"
"v_cmp_le_u64 vcc, s[40:41], v[10:11]\n"
"v_cmp_ge_u64 s[42:43], s[38:39], v[10:11]\n"
"s_nop 1\n"
"s_and_b64 s[42:43], s[42:43], vcc\n"
"s_cmp_eq_u64 s[42:43], exec\n"
"s_cbranch_scc1 .Ld7\n"
"s_sub_u32 s44, s44, 1\n"
"s_cbranch_scc1 .Ld7\n"
"s_sleep 1\n"
"buffer_load_dwordx2 v[10:11], v19, s[48:51], 0 offen sc1\n"
"s_waitcnt vmcnt(0)\n"
"s_branch .Lp7\n"
".Ld7:\n"
"s_mov_b64 exec, -1\n"
"s_waitcnt lgkmcnt(6)\n"
"v_mfma_f32_16x16x32_bf16 v[60:63], v[36:39], v[180:183], v[60:63]\n"
"ds_read_b64_tr_b16 v[28:29], v5 offset:24576\n"
"v_mfma_f32_16x16x32_bf16 v[56:59], v[36:39], v[176:179], v[56:59]\n"
"ds_read_b64_tr_b16 v[30:31], v5 offset:26624\n"
"s_add_u32 s46, s19, 66560\n"
"buffer_load_dwordx4 v[224:227], v1, s[48:51], s46 offen sc1\n"
"s_waitcnt lgkmcnt(6)\n"
"v_mfma_f32_16x16x32_bf16 v[64:67], v[40:43], v[176:179], v[64:67]\n"
"ds_read_b64_tr_b16 v[32:33], v6 offset:24576\n"
"v_mfma_f32_16x16x32_bf16 v[68:71], v[40:43], v[180:183], v[68:71]\n"
"ds_read_b64_tr_b16 v[34:35], v6 offset:26624\n"
"s_waitcnt lgkmcnt(6)\n"
"v_mfma_f32_16x16x32_bf16 v[76:79], v[44:47], v[180:183], v[76:79]\n"
"ds_read_b64_tr_b16 v[36:37], v7 offset:24576\n"
"v_mfma_f32_16x16x32_bf16 v[72:75], v[44:47], v[176:179], v[72:75]\n"
"ds_read_b64_tr_b16 v[38:39], v7 offset:26624\n"
"s_add_u32 s46, s19, 66560\n"
"s_add_u32 s46, s46, s81\n"
"buffer_load_dwordx4 v[228:231], v1, s[48:51], s46 offen sc1\n"
"s_waitcnt lgkmcnt(6)\n"
"v_mfma_f32_16x16x32_bf16 v[48:51], v[24:27], v[184:187], v[48:51]\n"
"ds_read_b64_tr_b16 v[40:41], v4 offset:28672\n"
"v_mfma_f32_16x16x32_bf16 v[52:55], v[24:27], v[188:191], v[52:55]\n"
"ds_read_b64_tr_b16 v[42:43], v4 offset:30720\n"
"s_waitcnt lgkmcnt(6)\n"
"v_mfma_f32_16x16x32_bf16 v[60:63], v[28:31], v[188:191], v[60:63]\n"
"ds_read_b64_tr_b16 v[44:45], v5 offset:28672\n"
"v_mfma_f32_16x16x32_bf16 v[56:59], v[28:31], v[184:187], v[56:59]\n"
"ds_read_b64_tr_b16 v[46:47], v5 offset:30720\n"
"s_add_u32 s46, s20, 66560\n"
"buffer_load_dwordx4 v[232:235], v1, s[48:51], s46 offen sc1\n"
"s_waitcnt lgkmcnt(6)\n"
"v_mfma_f32_16x16x32_bf16 v[64:67], v[32:35], v[184:187], v[64:67]\n"
"ds_read_b64_tr_b16 v[24:25], v6 offset:28672\n"
"s_setprio 0\n"
"v_mfma_f32_16x16x32_bf16 v[68:71], v[32:35], v[188:191], v[68:71]\n"
"ds_read_b64_tr_b16 v[26:27], v6 offset:30720\n"
"s_waitcnt lgkmcnt(6)\n"
"v_mfma_f32_16x16x32_bf16 v[76:79], v[36:39], v[188:191], v[76:79]\n"
"ds_read_b64_tr_b16 v[28:29], v7 offset:28672\n"
"v_mfma_f32_16x16x32_bf16 v[72:75], v[36:39], v[184:187], v[72:75]\n"
"ds_read_b64_tr_b16 v[30:31], v7 offset:30720\n"
"s_waitcnt lgkmcnt(6)\n"
"v_mfma_f32_16x16x32_bf16 v[48:51], v[40:43], v[192:195], v[48:51]\n"
"ds_read_b64_tr_b16 v[32:33], v4 offset:32768\n"
"v_mfma_f32_16x16x32_bf16 v[52:55], v[40:43], v[196:199], v[52:55]\n"
"ds_read_b64_tr_b16 v[34:35], v4 offset:34816\n"
"s_add_u32 s46, s20, 66560\n"
"s_add_u32 s46, s46, s81\n"
"buffer_load_dwordx4 v[236:239], v1, s[48:51], s46 offen sc1\n"
"s_waitcnt lgkmcnt(6)\n"
"v_mfma_f32_16x16x32_bf16 v[60:63], v[44:47], v[196:199], v[60:63]\n"
"ds_read_b64_tr_b16 v[36:37], v5 offset:32768\n"
"v_mfma_f32_16x16x32_bf16 v[56:59], v[44:47], v[192:195], v[56:59]\n"
"ds_read_b64_tr_b16 v[38:39], v5 offset:34816\n"
"s_waitcnt lgkmcnt(6)\n"
"v_mfma_f32_16x16x32_bf16 v[64:67], v[24:27], v[192:195], v[64:67]\n"
"ds_read_b64_tr_b16 v[40:41], v6 offset:32768\n"
"v_mfma_f32_16x16x32_bf16 v[68:71], v[24:27], v[196:199], v[68:71]\n"
"ds_read_b64_tr_b16 v[42:43], v6 offset:34816\n"
"s_add_u32 s46, s21, 66560\n"
"buffer_load_dwordx4 v[240:243], v1, s[48:51], s46 offen sc1\n"
"s_waitcnt lgkmcnt(6)\n"
"v_mfma_f32_16x16x32_bf16 v[76:79], v[28:31], v[196:199], v[76:79]\n"
"ds_read_b64_tr_b16 v[44:45], v7 offset:32768\n"
"v_mfma_f32_16x16x32_bf16 v[72:75], v[28:31], v[192:195], v[72:75]\n"
"ds_read_b64_tr_b16 v[46:47], v7 offset:34816\n"
"s_waitcnt vmcnt(8)\n"
"s_barrier\n"
"s_waitcnt lgkmcnt(6)\n"
"v_mfma_f32_16x16x32_bf16 v[48:51], v[32:35], v[200:203], v[48:51]\n"
"v_add_u32 v4, 0, v22\n"
"ds_read_b64_tr_b16 v[24:25], v4\n"
"s_setprio 3\n"
"v_mfma_f32_16x16x32_bf16 v[52:55], v[32:35], v[204:207], v[52:55]\n"
"ds_read_b64_tr_b16 v[26:27], v4 offset:2048\n"
"s_add_u32 s46, s21, 66560\n"
"s_add_u32 s46, s46, s81\n"
"buffer_load_dwordx4 v[244:247], v1, s[48:51], s46 offen sc1\n"
"s_waitcnt lgkmcnt(6)\n"
"v_mfma_f32_16x16x32_bf16 v[60:63], v[36:39], v[204:207], v[60:63]\n"
"v_xor_b32 v5, 32, v4\n"
"ds_read_b64_tr_b16 v[28:29], v5\n"
"v_mfma_f32_16x16x32_bf16 v[56:59], v[36:39], v[200:203], v[56:59]\n"
"ds_read_b64_tr_b16 v[30:31], v5 offset:2048\n"
"s_waitcnt lgkmcnt(6)\n"
"v_mfma_f32_16x16x32_bf16 v[64:67], v[40:43], v[200:203], v[64:67]\n"
"v_xor_b32 v6, 64, v4\n"
"ds_read_b64_tr_b16 v[32:33], v6\n"
"v_mfma_f32_16x16x32_bf16 v[68:71], v[40:43], v[204:207], v[68:71]\n"
"s_branch .Lsk10_0\n"
".Lst10_0:\n"
"s_branch .Lbk10\n"
".Lsk10_0:\n"
"ds_read_b64_tr_b16 v[34:35], v6 offset:2048\n"
"s_add_u32 s46, s22, 66560\n"
"buffer_load_dwordx4 v[248:251], v1, s[48:51], s46 offen sc1\n"
"s_waitcnt lgkmcnt(6)\n"
"v_mfma_f32_16x16x32_bf16 v[76:79], v[44:47], v[204:207], v[76:79]\n"
"v_xor_b32 v7, 96, v4\n"
"ds_read_b64_tr_b16 v[36:37], v7\n"
"v_mfma_f32_16x16x32_bf16 v[72:75], v[44:47], v[200:203], v[72:75]\n"
"ds_read_b64_tr_b16 v[38:39], v7 offset:2048\n"
"s_waitcnt lgkmcnt(6)\n"
"v_mfma_f32_16x16x32_bf16 v[80:83], v[24:27], v[208:211], v[80:83]\n"
"ds_read_b64_tr_b16 v[40:41], v4 offset:4096\n"
"v_mfma_f32_16x16x32_bf16 v[84:87], v[24:27], v[212:215], v[84:87]\n"
"ds_read_b64_tr_b16 v[42:43], v4 offset:6144\n"
"s_add_u32 s46, s22, 66560\n"
"s_add_u32 s46, s46, s81\n"
"buffer_load_dwordx4 v[252:255], v1, s[48:51], s46 offen sc1\n"
"s_waitcnt lgkmcnt(6)\n"
"v_mfma_f32_16x16x32_bf16 v[92:95], v[28:31], v[212:215], v[92:95]\n"
"ds_read_b64_tr_b16 v[44:45], v5 offset:4096\n"
"v_mfma_f32_16x16x32_bf16 v[88:91], v[28:31], v[208:211], v[88:91]\n"
"ds_read_b64_tr_b16 v[46:47], v5 offset:6144\n"
"s_waitcnt lgkmcnt(6)\n"
"v_mfma_f32_16x16x32_bf16 v[96:99], v[32:35], v[208:211], v[96:99]\n"
"ds_read_b64_tr_b16 v[24:25], v6 offset:4096\n"
"s_setprio 2\n"
"v_mfma_f32_16x16x32_bf16 v[100:103], v[32:35], v[212:215], v[100:103]\n"
"ds_read_b64_tr_b16 v[26:27], v6 offset:6144\n"
"s_add_u32 m0, s77, 122880\n"
"s_add_u32 s46, s12, 2396288\n"
"buffer_load_dwordx4 v2, s[56:59], s46 offen lds\n"
"s_waitcnt lgkmcnt(6)\n"
"v_mfma_f32_16x16x32_bf16 v[108:111], v[36:39], v[212:215], v[108:111]\n"
"ds_read_b64_tr_b16 v[28:29], v7 offset:4096\n"
"v_mfma_f32_16x16x32_bf16 v[104:107], v[36:39], v[208:211], v[104:107]\n"
"ds_read_b64_tr_b16 v[30:31], v7 offset:6144\n"
"s_waitcnt lgkmcnt(6)\n"
"v_mfma_f32_16x16x32_bf16 v[80:83], v[40:43], v[216:219], v[80:83]\n"
"ds_read_b64_tr_b16 v[32:33], v4 offset:8192\n"
"v_bfe_u32 v3, v0, 3, 1\n"
"v_mul_u32_u24 v21, 36864, v3\n"
"v_mfma_f32_16x16x32_bf16 v[84:87], v[40:43], v[220:223], v[84:87]\n"
"ds_read_b64_tr_b16 v[34:35], v4 offset:10240\n"
"s_waitcnt lgkmcnt(6)\n"
"v_mfma_f32_16x16x32_bf16 v[92:95], v[44:47], v[220:223], v[92:95]\n"
"ds_read_b64_tr_b16 v[36:37], v5 offset:8192\n"
"v_bfe_u32 v3, v0, 4, 1\n"
"v_lshl_add_u32 v21, v3, 8, v21\n"
"v_mfma_f32_16x16x32_bf16 v[88:91], v[44:47], v[216:219], v[88:91]\n"
"ds_read_b64_tr_b16 v[38:39], v5 offset:10240\n"
"s_add_u32 m0, s77, 126976\n"
"s_add_u32 s46, s13, 2396288\n"
"buffer_load_dwordx4 v2, s[56:59], s46 offen lds\n"
"s_waitcnt lgkmcnt(6)\n"
"v_mfma_f32_16x16x32_bf16 v[96:99], v[24:27], v[216:219], v[96:99]\n"
"ds_read_b64_tr_b16 v[40:41], v6 offset:8192\n"
"v_lshrrev_b32 v3, 5, v0\n"
"v_lshl_add_u32 v21, v3, 7, v21\n"
"v_mfma_f32_16x16x32_bf16 v[100:103], v[24:27], v[220:223], v[100:103]\n"
"ds_read_b64_tr_b16 v[42:43], v6 offset:10240\n"
"s_waitcnt lgkmcnt(6)\n"
"v_mfma_f32_16x16x32_bf16 v[108:111], v[28:31], v[220:223], v[108:111]\n"
"ds_read_b64_tr_b16 v[44:45], v7 offset:8192\n"
"v_and_b32 v3, 7, v0\n"
"v_lshl_add_u32 v21, v3, 4, v21\n"
"v_mfma_f32_16x16x32_bf16 v[104:107], v[28:31], v[216:219], v[104:107]\n"
"ds_read_b64_tr_b16 v[46:47], v7 offset:10240\n"
"s_add_u32 m0, s77, 131072\n"
"s_add_u32 s46, s14, 2396288\n"
"buffer_load_dwordx4 v2, s[56:59], s46 offen lds\n"
"s_waitcnt vmcnt(10) lgkmcnt(6)\n"
"v_mfma_f32_16x16x32_bf16 v[80:83], v[32:35], v[224:227], v[80:83]\n"
"ds_read_b64_tr_b16 v[24:25], v4 offset:24576\n"
"s_setprio 1\n"
"s_waitcnt vmcnt(9)\n"
"v_mfma_f32_16x16x32_bf16 v[84:87], v[32:35], v[228:231], v[84:87]\n"
"ds_read_b64_tr_b16 v[26:27], v4 offset:26624\n"
"v_lshrrev_b32 v3, 1, v0\n"
"v_mul_u32_u24 v2, 1152, v3\n"
"v_and_b32 v3, 1, v0\n"
"v_lshl_add_u32 v2, v3, 4, v2\n"
"s_waitcnt lgkmcnt(6)\n"
"v_mfma_f32_16x16x32_bf16 v[92:95], v[36:39], v[228:231], v[92:95]\n"
"ds_read_b64_tr_b16 v[28:29], v5 offset:24576\n"
"v_mfma_f32_16x16x32_bf16 v[88:91], v[36:39], v[224:227], v[88:91]\n"
"ds_read_b64_tr_b16 v[30:31], v5 offset:26624\n"
"s_add_u32 m0, s77, 49152\n"
"s_add_u32 s46, s16, 0\n"
"buffer_load_dwordx4 v2, s[56:59], s46 offen lds\n"
"s_waitcnt lgkmcnt(6)\n"
"v_mfma_f32_16x16x32_bf16 v[96:99], v[40:43], v[224:227], v[96:99]\n"
"ds_read_b64_tr_b16 v[32:33], v6 offset:24576\n"
"v_mfma_f32_16x16x32_bf16 v[100:103], v[40:43], v[228:231], v[100:103]\n"
"ds_read_b64_tr_b16 v[34:35], v6 offset:26624\n"
"s_waitcnt lgkmcnt(6)\n"
"v_mfma_f32_16x16x32_bf16 v[108:111], v[44:47], v[228:231], v[108:111]\n"
"ds_read_b64_tr_b16 v[36:37], v7 offset:24576\n"
"v_mfma_f32_16x16x32_bf16 v[104:107], v[44:47], v[224:227], v[104:107]\n"
"ds_read_b64_tr_b16 v[38:39], v7 offset:26624\n"
"s_waitcnt vmcnt(9) lgkmcnt(6)\n"
"v_mfma_f32_16x16x32_bf16 v[80:83], v[24:27], v[232:235], v[80:83]\n"
"ds_read_b64_tr_b16 v[40:41], v4 offset:28672\n"
"s_waitcnt vmcnt(8)\n"
"v_mfma_f32_16x16x32_bf16 v[84:87], v[24:27], v[236:239], v[84:87]\n"
"ds_read_b64_tr_b16 v[42:43], v4 offset:30720\n"
"s_waitcnt lgkmcnt(6)\n"
"v_mfma_f32_16x16x32_bf16 v[92:95], v[28:31], v[236:239], v[92:95]\n"
"ds_read_b64_tr_b16 v[44:45], v5 offset:28672\n"
"v_mfma_f32_16x16x32_bf16 v[88:91], v[28:31], v[232:235], v[88:91]\n"
"ds_read_b64_tr_b16 v[46:47], v5 offset:30720\n"
"s_waitcnt lgkmcnt(6)\n"
"v_mfma_f32_16x16x32_bf16 v[96:99], v[32:35], v[232:235], v[96:99]\n"
"ds_read_b64_tr_b16 v[24:25], v6 offset:28672\n"
"s_setprio 0\n"
"v_mfma_f32_16x16x32_bf16 v[100:103], v[32:35], v[236:239], v[100:103]\n"
"ds_read_b64_tr_b16 v[26:27], v6 offset:30720\n"
"s_waitcnt lgkmcnt(6)\n"
"v_mfma_f32_16x16x32_bf16 v[108:111], v[36:39], v[236:239], v[108:111]\n"
"ds_read_b64_tr_b16 v[28:29], v7 offset:28672\n"
"v_mfma_f32_16x16x32_bf16 v[104:107], v[36:39], v[232:235], v[104:107]\n"
"ds_read_b64_tr_b16 v[30:31], v7 offset:30720\n"
"s_add_u32 m0, s77, 53248\n"
"s_add_u32 s46, s16, 36864\n"
"buffer_load_dwordx4 v2, s[56:59], s46 offen lds\n"
"s_waitcnt vmcnt(8) lgkmcnt(6)\n"
"v_mfma_f32_16x16x32_bf16 v[80:83], v[40:43], v[240:243], v[80:83]\n"
"ds_read_b64_tr_b16 v[32:33], v4 offset:32768\n"
"s_waitcnt vmcnt(7)\n"
"v_mfma_f32_16x16x32_bf16 v[84:87], v[40:43], v[244:247], v[84:87]\n"
"ds_read_b64_tr_b16 v[34:35], v4 offset:34816\n"
"s_waitcnt lgkmcnt(6)\n"
"v_mfma_f32_16x16x32_bf16 v[92:95], v[44:47], v[244:247], v[92:95]\n"
"ds_read_b64_tr_b16 v[36:37], v5 offset:32768\n"
"v_mfma_f32_16x16x32_bf16 v[88:91], v[44:47], v[240:243], v[88:91]\n"
"ds_read_b64_tr_b16 v[38:39], v5 offset:34816\n"
"s_waitcnt lgkmcnt(6)\n"
"v_mfma_f32_16x16x32_bf16 v[96:99], v[24:27], v[240:243], v[96:99]\n"
"ds_read_b64_tr_b16 v[40:41], v6 offset:32768\n"
"v_mfma_f32_16x16x32_bf16 v[100:103], v[24:27], v[244:247], v[100:103]\n"
"ds_read_b64_tr_b16 v[42:43], v6 offset:34816\n"
"s_waitcnt lgkmcnt(6)\n"
"v_mfma_f32_16x16x32_bf16 v[108:111], v[28:31], v[244:247], v[108:111]\n"
"ds_read_b64_tr_b16 v[44:45], v7 offset:32768\n"
"v_mfma_f32_16x16x32_bf16 v[104:107], v[28:31], v[240:243], v[104:107]\n"
"ds_read_b64_tr_b16 v[46:47], v7 offset:34816\n"
"s_waitcnt vmcnt(2)\n"
"s_barrier\n"
"s_waitcnt lgkmcnt(6)\n"
"v_mfma_f32_16x16x32_bf16 v[80:83], v[32:35], v[248:251], v[80:83]\n"
"v_add_u32 v4, 98304, v22\n"
"ds_read_b64_tr_b16 v[24:25], v4\n"
"s_setprio 3\n"
"v_mfma_f32_16x16x32_bf16 v[84:87], v[32:35], v[252:255], v[84:87]\n"
"ds_read_b64_tr_b16 v[26:27], v4 offset:2048\n"
"s_add_u32 m0, s18, 57344\n"
"s_add_u32 s46, s17, 0\n"
"buffer_load_dwordx4 v2, s[56:59], s46 offen lds\n"
"s_waitcnt lgkmcnt(6)\n"
"v_mfma_f32_16x16x32_bf16 v[92:95], v[36:39], v[252:255], v[92:95]\n"
"v_xor_b32 v5, 32, v4\n"
"ds_read_b64_tr_b16 v[28:29], v5\n"
"v_mfma_f32_16x16x32_bf16 v[88:91], v[36:39], v[248:251], v[88:91]\n"
"ds_read_b64_tr_b16 v[30:31], v5 offset:2048\n"
"s_cmp_eq_u32 s25, 0\n"
"s_cselect_b32 s45, 0, 147456\n"
"s_mul_i32 s46, s26, 4096\n"
"s_add_u32 s46, s46, s45\n"
"v_add_u32 v20, s46, v1\n"
"s_waitcnt lgkmcnt(6)\n"
"v_mfma_f32_16x16x32_bf16 v[96:99], v[40:43], v[248:251], v[96:99]\n"
"ds_read_b64_tr_b16 v[32:33], v4 offset:4096\n"
"v_mfma_f32_16x16x32_bf16 v[100:103], v[40:43], v[252:255], v[100:103]\n"
"ds_read_b64_tr_b16 v[34:35], v4 offset:6144\n"
"ds_write_b128 v20, v[84:87]\n"
"s_waitcnt lgkmcnt(7)\n"
"v_mfma_f32_16x16x32_bf16 v[108:111], v[44:47], v[252:255], v[108:111]\n"
"ds_read_b64_tr_b16 v[36:37], v5 offset:4096\n"
"s_setprio 2\n"
"v_mfma_f32_16x16x32_bf16 v[104:107], v[44:47], v[248:251], v[104:107]\n"
"ds_read_b64_tr_b16 v[38:39], v5 offset:6144\n"
"s_waitcnt lgkmcnt(7)\n"
"v_mfma_f32_16x16x32_bf16 v[48:51], v[24:27], v[208:211], v[48:51]\n"
"ds_read_b64_tr_b16 v[40:41], v4 offset:8192\n"
"v_mfma_f32_16x16x32_bf16 v[52:55], v[24:27], v[212:215], v[52:55]\n"
"ds_read_b64_tr_b16 v[42:43], v4 offset:10240\n"
"ds_write_b128 v20, v[92:95] offset:1024\n"
"s_waitcnt lgkmcnt(8)\n"
"v_mfma_f32_16x16x32_bf16 v[60:63], v[28:31], v[212:215], v[60:63]\n"
"ds_read_b64_tr_b16 v[44:45], v5 offset:8192\n"
"v_mfma_f32_16x16x32_bf16 v[56:59], v[28:31], v[208:211], v[56:59]\n"
"ds_read_b64_tr_b16 v[46:47], v5 offset:10240\n"
"s_waitcnt lgkmcnt(8)\n"
"v_mfma_f32_16x16x32_bf16 v[48:51], v[32:35], v[216:219], v[48:51]\n"
"ds_read_b64_tr_b16 v[24:25], v4 offset:24576\n"
"s_setprio 1\n"
"v_mfma_f32_16x16x32_bf16 v[52:55], v[32:35], v[220:223], v[52:55]\n"
"ds_read_b64_tr_b16 v[26:27], v4 offset:26624\n"
"s_add_u32 m0, s77, 73728\n"
"s_add_u32 s46, s16, 2359296\n"
"buffer_load_dwordx4 v2, s[56:59], s46 offen lds\n"
"ds_write_b128 v20, v[100:103] offset:2048\n"
"s_waitcnt lgkmcnt(8)\n"
"v_mfma_f32_16x16x32_bf16 v[60:63], v[36:39], v[220:223], v[60:63]\n"
"ds_read_b64_tr_b16 v[28:29], v5 offset:24576\n"
"v_mfma_f32_16x16x32_bf16 v[56:59], v[36:39], v[216:219], v[56:59]\n"
"ds_read_b64_tr_b16 v[30:31], v5 offset:26624\n"
"s_waitcnt lgkmcnt(8)\n"
"v_mfma_f32_16x16x32_bf16 v[48:51], v[40:43], v[224:227], v[48:51]\n"
"ds_read_b64_tr_b16 v[32:33], v4 offset:28672\n"
"v_mfma_f32_16x16x32_bf16 v[52:55], v[40:43], v[228:231], v[52:55]\n"
"ds_read_b64_tr_b16 v[34:35], v4 offset:30720\n"
"ds_write_b128 v20, v[108:111] offset:3072\n"
"s_waitcnt lgkmcnt(8)\n"
"v_mfma_f32_16x16x32_bf16 v[60:63], v[44:47], v[228:231], v[60:63]\n"
"ds_read_b64_tr_b16 v[36:37], v5 offset:28672\n"
"s_setprio 0\n"
"v_mfma_f32_16x16x32_bf16 v[56:59], v[44:47], v[224:227], v[56:59]\n"
"ds_read_b64_tr_b16 v[38:39], v5 offset:30720\n"
"s_waitcnt lgkmcnt(8)\n"
"v_mfma_f32_16x16x32_bf16 v[48:51], v[24:27], v[232:235], v[48:51]\n"
"ds_read_b64_tr_b16 v[40:41], v4 offset:32768\n"
"v_mfma_f32_16x16x32_bf16 v[52:55], v[24:27], v[236:239], v[52:55]\n"
"ds_read_b64_tr_b16 v[42:43], v4 offset:34816\n"
"s_waitcnt lgkmcnt(7)\n"
"v_mfma_f32_16x16x32_bf16 v[60:63], v[28:31], v[236:239], v[60:63]\n"
"ds_read_b64_tr_b16 v[44:45], v5 offset:32768\n"
"v_mfma_f32_16x16x32_bf16 v[56:59], v[28:31], v[232:235], v[56:59]\n"
"ds_read_b64_tr_b16 v[46:47], v5 offset:34816\n"
"s_add_u32 m0, s77, 77824\n"
"s_add_u32 s46, s16, 2396160\n"
"buffer_load_dwordx4 v2, s[56:59], s46 offen lds\n"
"s_cmp_eq_u32 s25, 1\n"
"s_cselect_b32 s45, 0, 147456\n"
"s_mul_i32 s46, s26, 4096\n"
"s_add_u32 s46, s46, s45\n"
"v_add_u32 v20, s46, v1\n"
"s_waitcnt lgkmcnt(6)\n"
"s_barrier\n"
"v_mfma_f32_16x16x32_bf16 v[48:51], v[32:35], v[240:243], v[48:51]\n"
"v_xor_b32 v6, 64, v4\n"
"ds_read_b64_tr_b16 v[24:25], v6\n"
"s_setprio 3\n"
"ds_read_b128 v[84:87], v20\n"
"v_mfma_f32_16x16x32_bf16 v[52:55], v[32:35], v[244:247], v[52:55]\n"
"ds_read_b64_tr_b16 v[26:27], v6 offset:2048\n"
"ds_read_b128 v[92:95], v20 offset:1024\n"
"s_waitcnt lgkmcnt(8)\n"
"v_mfma_f32_16x16x32_bf16 v[60:63], v[36:39], v[244:247], v[60:63]\n"
"v_xor_b32 v7, 96, v4\n"
"ds_read_b64_tr_b16 v[28:29], v7\n"
"ds_read_b128 v[100:103], v20 offset:2048\n"
"v_mfma_f32_16x16x32_bf16 v[56:59], v[36:39], v[240:243], v[56:59]\n"
"ds_read_b64_tr_b16 v[30:31], v7 offset:2048\n"
"ds_read_b128 v[108:111], v20 offset:3072\n"
"s_waitcnt lgkmcnt(10)\n"
"v_mfma_f32_16x16x32_bf16 v[48:51], v[40:43], v[248:251], v[48:51]\n"
"ds_read_b64_tr_b16 v[32:33], v6 offset:4096\n"
"v_mfma_f32_16x16x32_bf16 v[52:55], v[40:43], v[252:255], v[52:55]\n"
"ds_read_b64_tr_b16 v[34:35], v6 offset:6144\n"
"s_setprio 2\n"
"s_waitcnt lgkmcnt(8)\n"
"v_add_f32 v80, v80, v84\n"
"v_mfma_f32_16x16x32_bf16 v[60:63], v[44:47], v[252:255], v[60:63]\n"
"ds_read_b64_tr_b16 v[36:37], v7 offset:4096\n"
"v_add_f32 v81, v81, v85\n"
"v_mfma_f32_16x16x32_bf16 v[56:59], v[44:47], v[248:251], v[56:59]\n"
"ds_read_b64_tr_b16 v[38:39], v7 offset:6144\n"
"v_add_f32 v82, v82, v86\n"
"s_cmp_eq_u32 s25, 0\n"
"s_cselect_b32 s45, 0, 2048\n"
"s_mul_i32 s46, s26, 4096\n"
"s_add_u32 s46, s46, s45\n"
"s_add_u32 s46, s46, 24576\n"
"v_add_u32 v20, s46, v1\n"
"s_waitcnt lgkmcnt(9)\n"
"v_mfma_f32_16x16x32_bf16 v[64:67], v[24:27], v[208:211], v[64:67]\n"
"ds_read_b64_tr_b16 v[40:41], v6 offset:8192\n"
"v_add_f32 v83, v83, v87\n"
"v_mfma_f32_16x16x32_bf16 v[68:71], v[24:27], v[212:215], v[68:71]\n"
"ds_read_b64_tr_b16 v[42:43], v6 offset:10240\n"
"v_lshlrev_b32 v22, 3, v0\n"
"v_add_u32 v22, s78, v22\n"
"s_waitcnt lgkmcnt(10)\n"
"v_add_f32 v88, v88, v92\n"
"ds_write_b128 v20, v[52:55]\n"
"s_waitcnt lgkmcnt(8)\n"
"v_mfma_f32_16x16x32_bf16 v[76:79], v[28:31], v[212:215], v[76:79]\n"
"ds_read_b64_tr_b16 v[44:45], v7 offset:8192\n"
"s_setprio 1\n"
"v_add_f32 v89, v89, v93\n"
"v_mfma_f32_16x16x32_bf16 v[72:75], v[28:31], v[208:211], v[72:75]\n"
"ds_read_b64_tr_b16 v[46:47], v7 offset:10240\n"
"v_add_f32 v90, v90, v94\n"
"s_waitcnt lgkmcnt(7)\n"
"v_mfma_f32_16x16x32_bf16 v[64:67], v[32:35], v[216:219], v[64:67]\n"
"ds_read_b64_tr_b16 v[24:25], v6 offset:24576\n"
"v_add_f32 v91, v91, v95\n"
"v_mfma_f32_16x16x32_bf16 v[68:71], v[32:35], v[220:223], v[68:71]\n"
"ds_read_b64_tr_b16 v[26:27], v6 offset:26624\n"
"v_cvt_pk_bf16_f32 v12, v80, v81\n"
"ds_write_b128 v20, v[60:63] offset:1024\n"
"s_waitcnt lgkmcnt(8)\n"
"v_mfma_f32_16x16x32_bf16 v[76:79], v[36:39], v[220:223], v[76:79]\n"
"ds_read_b64_tr_b16 v[28:29], v7 offset:24576\n"
"v_cvt_pk_bf16_f32 v13, v82, v83\n"
"v_mfma_f32_16x16x32_bf16 v[72:75], v[36:39], v[216:219], v[72:75]\n"
"ds_read_b64_tr_b16 v[30:31], v7 offset:26624\n"
"s_setprio 0\n"
"v_cvt_pk_bf16_f32 v14, v88, v89\n"
"s_waitcnt lgkmcnt(8)\n"
"v_mfma_f32_16x16x32_bf16 v[64:67], v[40:43], v[224:227], v[64:67]\n"
"ds_read_b64_tr_b16 v[32:33], v6 offset:28672\n"
"v_cvt_pk_bf16_f32 v15, v90, v91\n"
"v_mfma_f32_16x16x32_bf16 v[68:71], v[40:43], v[228:231], v[68:71]\n"
"ds_read_b64_tr_b16 v[34:35], v6 offset:30720\n"
"v_permlane16_swap_b32 v12, v14\n"
"s_waitcnt lgkmcnt(7)\n"
"v_mfma_f32_16x16x32_bf16 v[76:79], v[44:47], v[228:231], v[76:79]\n"
"ds_read_b64_tr_b16 v[36:37], v7 offset:28672\n"
"v_permlane16_swap_b32 v13, v15\n"
"v_mfma_f32_16x16x32_bf16 v[72:75], v[44:47], v[224:227], v[72:75]\n"
"ds_read_b64_tr_b16 v[38:39], v7 offset:30720\n"
"buffer_store_dwordx4 v[12:15], v21, s[48:51], s80 offen sc1\n"
"s_cmp_eq_u32 s25, 1\n"
"s_cselect_b32 s45, 0, 2048\n"
"s_mul_i32 s46, s26, 4096\n"
"s_add_u32 s46, s46, s45\n"
"s_add_u32 s46, s46, 24576\n"
"v_add_u32 v20, s46, v1\n"
"s_waitcnt vmcnt(3) lgkmcnt(6)\n"
"s_barrier\n"
"v_mfma_f32_16x16x32_bf16 v[64:67], v[24:27], v[232:235], v[64:67]\n"
"ds_read_b64_tr_b16 v[40:41], v6 offset:32768\n"
"s_setprio 3\n"
"v_add_f32 v96, v96, v100\n"
"ds_read_b128 v[52:55], v20\n"
"v_mfma_f32_16x16x32_bf16 v[68:71], v[24:27], v[236:239], v[68:71]\n"
"ds_read_b64_tr_b16 v[42:43], v6 offset:34816\n"
"v_add_f32 v97, v97, v101\n"
"ds_read_b128 v[60:63], v20 offset:1024\n"
"s_waitcnt lgkmcnt(8)\n"
"v_mfma_f32_16x16x32_bf16 v[76:79], v[28:31], v[236:239], v[76:79]\n"
"ds_read_b64_tr_b16 v[44:45], v7 offset:32768\n"
"v_add_f32 v98, v98, v102\n"
"v_mfma_f32_16x16x32_bf16 v[72:75], v[28:31], v[232:235], v[72:75]\n"
"ds_read_b64_tr_b16 v[46:47], v7 offset:34816\n"
"v_add_f32 v99, v99, v103\n"
"s_waitcnt lgkmcnt(8)\n"
"v_mfma_f32_16x16x32_bf16 v[64:67], v[32:35], v[240:243], v[64:67]\n"
"v_add_u32 v4, 49152, v22\n"
"ds_read_b64_tr_b16 v[24:25], v4\n"
"v_add_f32 v104, v104, v108\n"
"v_mfma_f32_16x16x32_bf16 v[68:71], v[32:35], v[244:247], v[68:71]\n"
"ds_read_b64_tr_b16 v[26:27], v4 offset:512\n"
"v_add_f32 v105, v105, v109\n"
"s_waitcnt lgkmcnt(8)\n"
"v_mfma_f32_16x16x32_bf16 v[76:79], v[36:39], v[244:247], v[76:79]\n"
"ds_read_b64_tr_b16 v[28:29], v4 offset:1024\n"
"s_setprio 2\n"
"v_add_f32 v106, v106, v110\n"
"v_mfma_f32_16x16x32_bf16 v[72:75], v[36:39], v[240:243], v[72:75]\n"
"ds_read_b64_tr_b16 v[30:31], v4 offset:1536\n"
"v_add_f32 v107, v107, v111\n"
"s_waitcnt lgkmcnt(7)\n"
"v_mfma_f32_16x16x32_bf16 v[64:67], v[40:43], v[248:251], v[64:67]\n"
"ds_read_b64_tr_b16 v[32:33], v4 offset:2048\n"
"v_cvt_pk_bf16_f32 v12, v96, v97\n"
"v_mfma_f32_16x16x32_bf16 v[68:71], v[40:43], v[252:255], v[68:71]\n"
"ds_read_b64_tr_b16 v[34:35], v4 offset:2560\n"
"v_cvt_pk_bf16_f32 v13, v98, v99\n"
"s_waitcnt lgkmcnt(6)\n"
"v_mfma_f32_16x16x32_bf16 v[76:79], v[44:47], v[252:255], v[76:79]\n"
"ds_read_b64_tr_b16 v[36:37], v4 offset:3072\n"
"v_cvt_pk_bf16_f32 v14, v104, v105\n"
"v_mfma_f32_16x16x32_bf16 v[72:75], v[44:47], v[248:251], v[72:75]\n"
"ds_read_b64_tr_b16 v[38:39], v4 offset:3584\n"
"v_cvt_pk_bf16_f32 v15, v106, v107\n"
"s_cmp_eq_u32 s25, 0\n"
"s_cselect_b32 s45, 0, 2048\n"
"s_mul_i32 s46, s26, 4096\n"
"s_add_u32 s46, s46, s45\n"
"v_add_u32 v20, s46, v1\n"
"s_waitcnt lgkmcnt(6)\n"
"v_mfma_f32_16x16x32_bf16 v[80:83], v[24:27], v[112:115], 0\n"
"ds_read_b64_tr_b16 v[40:41], v4 offset:4096\n"
"s_setprio 1\n"
"v_permlane16_swap_b32 v12, v14\n"
"v_mfma_f32_16x16x32_bf16 v[84:87], v[24:27], v[116:119], 0\n"
"ds_read_b64_tr_b16 v[42:43], v4 offset:4608\n"
"v_permlane16_swap_b32 v13, v15\n"
"ds_write_b128 v20, v[68:71]\n"
"s_waitcnt lgkmcnt(7)\n"
"v_mfma_f32_16x16x32_bf16 v[80:83], v[28:31], v[120:123], v[80:83]\n"
"ds_read_b64_tr_b16 v[44:45], v4 offset:5120\n"
"buffer_store_dwordx4 v[12:15], v21, s[48:51], s80 offen offset:512 sc1\n"
"v_mfma_f32_16x16x32_bf16 v[84:87], v[28:31], v[124:127], v[84:87]\n"
"ds_read_b64_tr_b16 v[46:47], v4 offset:5632\n"
"v_add_f32 v48, v48, v52\n"
"s_waitcnt lgkmcnt(7)\n"
"v_mfma_f32_16x16x32_bf16 v[80:83], v[32:35], v[128:131], v[80:83]\n"
"ds_read_b64_tr_b16 v[24:25], v4 offset:6144\n"
"v_add_f32 v49, v49, v53\n"
"v_mfma_f32_16x16x32_bf16 v[84:87], v[32:35], v[132:135], v[84:87]\n"
"ds_read_b64_tr_b16 v[26:27], v4 offset:6656\n"
"v_add_f32 v50, v50, v54\n"
"ds_write_b128 v20, v[76:79] offset:1024\n"
"s_waitcnt lgkmcnt(8)\n"
"v_mfma_f32_16x16x32_bf16 v[80:83], v[36:39], v[136:139], v[80:83]\n"
"ds_read_b64_tr_b16 v[28:29], v4 offset:7168\n"
"s_setprio 0\n"
"v_add_f32 v51, v51, v55\n"
"v_mfma_f32_16x16x32_bf16 v[84:87], v[36:39], v[140:143], v[84:87]\n"
"ds_read_b64_tr_b16 v[30:31], v4 offset:7680\n"
"v_add_f32 v56, v56, v60\n"
"s_waitcnt lgkmcnt(8)\n"
"v_mfma_f32_16x16x32_bf16 v[80:83], v[40:43], v[144:147], v[80:83]\n"
"ds_read_b64_tr_b16 v[32:33], v4 offset:8192\n"
"v_add_f32 v57, v57, v61\n"
"v_mfma_f32_16x16x32_bf16 v[84:87], v[40:43], v[148:151], v[84:87]\n"
"ds_read_b64_tr_b16 v[34:35], v4 offset:8704\n"
"v_add_f32 v58, v58, v62\n"
"s_waitcnt lgkmcnt(7)\n"
"v_mfma_f32_16x16x32_bf16 v[80:83], v[44:47], v[152:155], v[80:83]\n"
"ds_read_b64_tr_b16 v[36:37], v4 offset:9216\n"
"v_add_f32 v59, v59, v63\n"
"v_mfma_f32_16x16x32_bf16 v[84:87], v[44:47], v[156:159], v[84:87]\n"
"ds_read_b64_tr_b16 v[38:39], v4 offset:9728\n"
"v_cvt_pk_bf16_f32 v12, v48, v49\n"
"s_cmp_eq_u32 s25, 1\n"
"s_cselect_b32 s45, 0, 2048\n"
"s_mul_i32 s46, s26, 4096\n"
"s_add_u32 s46, s46, s45\n"
"v_add_u32 v20, s46, v1\n"
"s_waitcnt vmcnt(2) lgkmcnt(6)\n"
"s_barrier\n"
"v_mfma_f32_16x16x32_bf16 v[80:83], v[24:27], v[160:163], v[80:83]\n"
"ds_read_b64_tr_b16 v[40:41], v4 offset:24576\n"
"s_setprio 3\n"
"v_cvt_pk_bf16_f32 v13, v50, v51\n"
"ds_read_b128 v[68:71], v20\n"
"v_mfma_f32_16x16x32_bf16 v[84:87], v[24:27], v[164:167], v[84:87]\n"
"ds_read_b64_tr_b16 v[42:43], v4 offset:25088\n"
"v_cvt_pk_bf16_f32 v14, v56, v57\n"
"ds_read_b128 v[76:79], v20 offset:1024\n"
"s_waitcnt lgkmcnt(8)\n"
"v_mfma_f32_16x16x32_bf16 v[80:83], v[28:31], v[168:171], v[80:83]\n"
"ds_read_b64_tr_b16 v[44:45], v4 offset:25600\n"
"v_cvt_pk_bf16_f32 v15, v58, v59\n"
"v_mfma_f32_16x16x32_bf16 v[84:87], v[28:31], v[172:175], v[84:87]\n"
"ds_read_b64_tr_b16 v[46:47], v4 offset:26112\n"
"v_permlane16_swap_b32 v12, v14\n"
"s_waitcnt lgkmcnt(8)\n"
"v_mfma_f32_16x16x32_bf16 v[80:83], v[32:35], v[176:179], v[80:83]\n"
"ds_read_b64_tr_b16 v[24:25], v4 offset:26624\n"
"v_permlane16_swap_b32 v13, v15\n"
"v_mfma_f32_16x16x32_bf16 v[84:87], v[32:35], v[180:183], v[84:87]\n"
"ds_read_b64_tr_b16 v[26:27], v4 offset:27136\n"
"buffer_store_dwordx4 v[12:15], v21, s[48:51], s80 offen offset:1024 sc1\n"
"s_waitcnt lgkmcnt(8)\n"
"v_mfma_f32_16x16x32_bf16 v[80:83], v[36:39], v[184:187], v[80:83]\n"
"ds_read_b64_tr_b16 v[28:29], v4 offset:27648\n"
"s_setprio 2\n"
"s_waitcnt lgkmcnt(7)\n"
"v_add_f32 v64, v64, v68\n"
"v_mfma_f32_16x16x32_bf16 v[84:87], v[36:39], v[188:191], v[84:87]\n"
"ds_read_b64_tr_b16 v[30:31], v4 offset:28160\n"
"v_add_f32 v65, v65, v69\n"
"s_waitcnt lgkmcnt(7)\n"
"v_mfma_f32_16x16x32_bf16 v[80:83], v[40:43], v[192:195], v[80:83]\n"
"ds_read_b64_tr_b16 v[32:33], v4 offset:28672\n"
"v_add_f32 v66, v66, v70\n"
"v_mfma_f32_16x16x32_bf16 v[84:87], v[40:43], v[196:199], v[84:87]\n"
"ds_read_b64_tr_b16 v[34:35], v4 offset:29184\n"
"v_add_f32 v67, v67, v71\n"
"s_waitcnt lgkmcnt(6)\n"
"v_mfma_f32_16x16x32_bf16 v[80:83], v[44:47], v[200:203], v[80:83]\n"
"ds_read_b64_tr_b16 v[36:37], v4 offset:29696\n"
"v_add_f32 v72, v72, v76\n"
"v_mfma_f32_16x16x32_bf16 v[84:87], v[44:47], v[204:207], v[84:87]\n"
"ds_read_b64_tr_b16 v[38:39], v4 offset:30208\n"
"v_add_f32 v73, v73, v77\n"
"s_waitcnt lgkmcnt(6)\n"
"v_mfma_f32_16x16x32_bf16 v[80:83], v[24:27], v[208:211], v[80:83]\n"
"ds_read_b64_tr_b16 v[40:41], v4 offset:30720\n"
"s_setprio 1\n"
"v_add_f32 v74, v74, v78\n"
"v_mfma_f32_16x16x32_bf16 v[84:87], v[24:27], v[212:215], v[84:87]\n"
"ds_read_b64_tr_b16 v[42:43], v4 offset:31232\n"
"v_add_f32 v75, v75, v79\n"
"s_waitcnt lgkmcnt(6)\n"
"v_mfma_f32_16x16x32_bf16 v[80:83], v[28:31], v[216:219], v[80:83]\n"
"ds_read_b64_tr_b16 v[44:45], v4 offset:31744\n"
"v_cvt_pk_bf16_f32 v12, v64, v65\n"
"v_mfma_f32_16x16x32_bf16 v[84:87], v[28:31], v[220:223], v[84:87]\n"
"ds_read_b64_tr_b16 v[46:47], v4 offset:32256\n"
"v_cvt_pk_bf16_f32 v13, v66, v67\n"
"s_waitcnt lgkmcnt(6)\n"
"v_mfma_f32_16x16x32_bf16 v[80:83], v[32:35], v[224:227], v[80:83]\n"
"v_cvt_pk_bf16_f32 v14, v72, v73\n"
"v_mfma_f32_16x16x32_bf16 v[84:87], v[32:35], v[228:231], v[84:87]\n"
"v_cvt_pk_bf16_f32 v15, v74, v75\n"
"s_waitcnt lgkmcnt(4)\n"
"v_mfma_f32_16x16x32_bf16 v[80:83], v[36:39], v[232:235], v[80:83]\n"
"s_setprio 0\n"
"v_permlane16_swap_b32 v12, v14\n"
"v_mfma_f32_16x16x32_bf16 v[84:87], v[36:39], v[236:239], v[84:87]\n"
"v_permlane16_swap_b32 v13, v15\n"
"s_waitcnt lgkmcnt(2)\n"
"v_mfma_f32_16x16x32_bf16 v[80:83], v[40:43], v[240:243], v[80:83]\n"
"buffer_store_dwordx4 v[12:15], v21, s[48:51], s80 offen offset:1536 sc1\n"
"v_mfma_f32_16x16x32_bf16 v[84:87], v[40:43], v[244:247], v[84:87]\n"
"s_waitcnt lgkmcnt(0)\n"
"v_mfma_f32_16x16x32_bf16 v[80:83], v[44:47], v[248:251], v[80:83]\n"
"v_mfma_f32_16x16x32_bf16 v[84:87], v[44:47], v[252:255], v[84:87]\n"
"s_nop 7\n"
"ds_write_b128 v20, v[84:87]\n"
"s_cmp_eq_u32 s25, 0\n"
"s_cselect_b32 s45, 0, 2048\n"
"s_mul_i32 s46, s26, 4096\n"
"s_add_u32 s46, s46, s45\n"
"v_add_u32 v20, s46, v1\n"
"v_bfe_u32 v3, v0, 4, 1\n"
"v_mul_u32_u24 v3, 248, v3\n"
"v_sub_u32 v21, v21, v3\n"
"s_or_b32 s45, s36, 1\n"
"v_mov_b32 v16, s45\n"
"v_mov_b32 v17, s37\n"
"v_lshrrev_b32 v18, 4, v0\n"
"v_lshlrev_b32 v18, 28, v18\n"
"v_lshl_add_u32 v18, v0, 3, v18\n"
"s_waitcnt lgkmcnt(0)\n"
"s_barrier\n"
"ds_read_b128 v[84:87], v20\n"
"s_waitcnt lgkmcnt(0)\n"
"v_add_f32 v80, v80, v84\n"
"v_add_f32 v81, v81, v85\n"
"v_add_f32 v82, v82, v86\n"
"v_add_f32 v83, v83, v87\n"
"v_cvt_pk_bf16_f32 v12, v80, v81\n"
"v_cvt_pk_bf16_f32 v13, v82, v83\n"
"buffer_store_dwordx2 v[12:13], v21, s[48:51], s80 offen offset:2048 sc1\n"
"s_and_b32 s45, s27, 28\n"
"s_lshr_b32 s46, s24, 1\n"
"s_lshr_b32 s47, s32, 1\n"
"s_mul_i32 s3, s45, 8192\n"
"s_mul_i32 s42, s46, 8192\n"
"s_add_u32 s3, s3, s42\n"
"s_mul_i32 s42, s29, 1024\n"
"s_add_u32 s3, s3, s42\n"
"s_mul_i32 s42, s47, 512\n"
"s_add_u32 s3, s3, s42\n"
"s_mul_i32 s42, s30, 128\n"
"s_add_u32 s3, s3, s42\n"
"s_add_u32 s3, s3, 1314816\n"
"v_and_b32 v19, 3, v0\n"
"v_lshlrev_b32 v19, 11, v19\n"
"v_add_u32 v19, s3, v19\n"
"v_lshrrev_b32 v3, 3, v0\n"
"v_and_b32 v4, 0x70, v1\n"
"v_lshl_add_u32 v4, v3, 1, v4\n"
"s_and_b32 s45, s24, 1\n"
"s_mul_i32 s84, s35, 36864\n"
"s_mul_i32 s42, s46, 2304\n"
"s_add_u32 s84, s84, s42\n"
"s_mul_i32 s42, s45, 1024\n"
"s_add_u32 s84, s84, s42\n"
"s_add_u32 s84, s84, 25169920\n"
"s_mul_i32 s85, s35, 36864\n"
"s_mul_i32 s42, s46, 2304\n"
"s_add_u32 s85, s85, s42\n"
"s_mul_i32 s42, s45, 128\n"
"s_add_u32 s85, s85, s42\n"
"s_add_u32 s85, s85, 25171968\n"
"v_bfe_u32 v3, v0, 3, 2\n"
"v_mul_u32_u24 v20, 9216, v3\n"
"v_lshrrev_b32 v3, 5, v0\n"
"v_lshl_add_u32 v20, v3, 3, v20\n"
"v_and_b32 v3, 0x70, v1\n"
"v_add_u32 v20, v20, v3\n"
"v_and_b32 v3, 8, v0\n"
"v_cmp_ne_u32 s[0:1], 0, v3\n"
"s_mul_i32 s46, s35, 9216\n"
"s_mul_i32 s42, s24, 1024\n"
"s_add_u32 s46, s46, s42\n"
"s_add_u32 s46, s46, 3936256\n"
"buffer_load_dwordx4 v[112:115], v1, s[48:51], s46 offen sc1\n"
"v_mov_b32 v116, 0\n"
"s_mul_i32 s46, s35, 9216\n"
"s_mul_i32 s42, s24, 128\n"
"s_add_u32 s46, s46, s42\n"
"s_add_u32 s46, s46, 3944448\n"
"buffer_load_short_d16_hi v116, v4, s[48:51], s46 offen sc1\n"
"v_mov_b32 v136, 0\n"
"v_mov_b32 v137, 0\n"
"v_mov_b32 v138, 0\n"
"v_mov_b32 v139, 0\n"
"v_and_b32 v3, 7, v0\n"
"v_mul_u32_u24 v7, 1152, v3\n"
"v_lshrrev_b32 v3, 3, v0\n"
"v_lshl_add_u32 v6, v3, 4, v7\n"
"v_lshl_add_u32 v7, v3, 1, v7\n"
"s_add_u32 s45, s79, 1310720\n"
"s_waitcnt vmcnt(2)\n"
"buffer_store_dwordx2 v[16:17], v18, s[48:51], s45 offen sc1\n"
"s_sleep 16\n"
"s_mov_b64 exec, 0xf\n"
"buffer_load_dwordx2 v[10:11], v19, s[48:51], 0 offen sc1\n"
"s_mov_b64 exec, -1\n"
"s_mul_i32 s87, s35, 9216\n"
"s_mul_i32 s42, s24, 128\n"
"s_add_u32 s87, s87, s42\n"
"s_mul_i32 s88, s35, 9216\n"
"s_mul_i32 s42, s24, 16\n"
"s_add_u32 s88, s88, s42\n"
"s_waitcnt vmcnt(3)\n"
"v_lshlrev_b32 v160, 16, v112\n"
"v_and_b32 v161, 0xffff0000, v112\n"
"v_lshlrev_b32 v162, 16, v113\n"
"v_and_b32 v163, 0xffff0000, v113\n"
"v_lshlrev_b32 v164, 16, v114\n"
"v_and_b32 v165, 0xffff0000, v114\n"
"s_branch .Lsk11_0\n"
".Lst11_0:\n"
"s_branch .Lbk11\n"
".Lsk11_0:\n"
"v_lshlrev_b32 v166, 16, v115\n"
"v_and_b32 v167, 0xffff0000, v115\n"
"v_mov_b32 v112, 0x3f317218\n"
"v_mov_b32 v16, s36\n"
"s_add_u32 s45, s2, s24\n"
"v_add_u32 v18, s45, v0\n"
"v_min_u32 v18, 1, v18\n"
"v_lshlrev_b32 v18, 31, v18\n"
"s_or_b32 s40, s36, 1\n"
"s_waitcnt vmcnt(0)\n"
"s_mov_b32 s44, 0x40000\n"
"s_mov_b64 exec, 0xf\n"
".Lp8:\n"
"v_cmp_le_u64 vcc, s[40:41], v[10:11]\n"
"v_cmp_ge_u64 s[42:43], s[38:39], v[10:11]\n"
"s_nop 1\n"
"s_and_b64 s[42:43], s[42:43], vcc\n"
"s_cmp_eq_u64 s[42:43], exec\n"
"s_cbranch_scc1 .Ld8\n"
"s_sub_u32 s44, s44, 1\n"
"s_cbranch_scc1 .Ld8\n"
"s_sleep 1\n"
"buffer_load_dwordx2 v[10:11], v19, s[48:51], 0 offen sc1\n"
"s_waitcnt vmcnt(0)\n"
"s_branch .Lp8\n"
".Ld8:\n"
"s_mov_b64 exec, -1\n"
"buffer_load_dwordx4 v[120:123], v1, s[48:51], s84 offen sc1\n"
"s_add_u32 s84, s84, 9216\n"
"buffer_load_dwordx4 v[124:127], v1, s[48:51], s84 offen sc1\n"
"s_add_u32 s84, s84, 9216\n"
"buffer_load_dwordx4 v[128:131], v1, s[48:51], s84 offen sc1\n"
"s_add_u32 s84, s84, 9216\n"
"buffer_load_dwordx4 v[132:135], v1, s[48:51], s84 offen sc1\n"
"buffer_load_dwordx2 v[140:141], v20, s[48:51], s85 offen sc1\n"
"buffer_store_dwordx2 v[16:17], v18, s[48:51], 0 offen sc1\n"
"s_waitcnt vmcnt(5)\n"
"v_lshlrev_b32 v152, 16, v120\n"
"v_and_b32 v153, 0xffff0000, v120\n"
"v_lshlrev_b32 v154, 16, v121\n"
"v_and_b32 v155, 0xffff0000, v121\n"
"v_lshlrev_b32 v156, 16, v122\n"
"v_and_b32 v157, 0xffff0000, v122\n"
"v_lshlrev_b32 v158, 16, v123\n"
"v_and_b32 v159, 0xffff0000, v123\n"
"s_waitcnt vmcnt(4)\n"
"v_lshlrev_b32 v8, 16, v124\n"
"v_and_b32 v9, 0xffff0000, v124\n"
"v_pk_add_f32 v[152:153], v[152:153], v[8:9]\n"
"v_lshlrev_b32 v12, 16, v125\n"
"v_and_b32 v13, 0xffff0000, v125\n"
"v_pk_add_f32 v[154:155], v[154:155], v[12:13]\n"
"v_lshlrev_b32 v8, 16, v126\n"
"v_and_b32 v9, 0xffff0000, v126\n"
"v_pk_add_f32 v[156:157], v[156:157], v[8:9]\n"
"v_lshlrev_b32 v12, 16, v127\n"
"v_and_b32 v13, 0xffff0000, v127\n"
"v_pk_add_f32 v[158:159], v[158:159], v[12:13]\n"
"s_waitcnt vmcnt(3)\n"
"v_lshlrev_b32 v8, 16, v128\n"
"v_and_b32 v9, 0xffff0000, v128\n"
"v_pk_add_f32 v[152:153], v[152:153], v[8:9]\n"
"v_lshlrev_b32 v12, 16, v129\n"
"v_and_b32 v13, 0xffff0000, v129\n"
"v_pk_add_f32 v[154:155], v[154:155], v[12:13]\n"
"v_lshlrev_b32 v8, 16, v130\n"
"v_and_b32 v9, 0xffff0000, v130\n"
"v_pk_add_f32 v[156:157], v[156:157], v[8:9]\n"
"v_lshlrev_b32 v12, 16, v131\n"
"v_and_b32 v13, 0xffff0000, v131\n"
"v_pk_add_f32 v[158:159], v[158:159], v[12:13]\n"
"s_waitcnt vmcnt(2)\n"
"v_lshlrev_b32 v8, 16, v132\n"
"v_and_b32 v9, 0xffff0000, v132\n"
"v_pk_add_f32 v[152:153], v[152:153], v[8:9]\n"
"v_lshlrev_b32 v12, 16, v133\n"
"v_and_b32 v13, 0xffff0000, v133\n"
"v_pk_add_f32 v[154:155], v[154:155], v[12:13]\n"
"v_lshlrev_b32 v8, 16, v134\n"
"v_and_b32 v9, 0xffff0000, v134\n"
"v_pk_add_f32 v[156:157], v[156:157], v[8:9]\n"
"v_lshlrev_b32 v12, 16, v135\n"
"v_and_b32 v13, 0xffff0000, v135\n"
"v_pk_add_f32 v[158:159], v[158:159], v[12:13]\n"
"v_pk_fma_f32 v[152:153], v[152:153], v[112:113], v[160:161] op_sel_hi:[1,0,1]\n"
"v_pk_fma_f32 v[154:155], v[154:155], v[112:113], v[162:163] op_sel_hi:[1,0,1]\n"
"v_pk_fma_f32 v[156:157], v[156:157], v[112:113], v[164:165] op_sel_hi:[1,0,1]\n"
"v_pk_fma_f32 v[158:159], v[158:159], v[112:113], v[166:167] op_sel_hi:[1,0,1]\n"
"v_cvt_pk_bf16_f32 v120, v152, v153\n"
"v_cvt_pk_bf16_f32 v121, v154, v155\n"
"v_cvt_pk_bf16_f32 v122, v156, v157\n"
"v_cvt_pk_bf16_f32 v123, v158, v159\n"
"buffer_store_dwordx4 v[120:123], v6, s[60:63], s87 offen\n"
"s_waitcnt vmcnt(2)\n"
"v_lshlrev_b32 v136, 16, v140\n"
"v_and_b32 v137, 0xffff0000, v140\n"
"v_lshlrev_b32 v138, 16, v141\n"
"v_and_b32 v139, 0xffff0000, v141\n"
"s_nop 0\n"
"v_permlane16_swap_b32 v136, v138\n"
"v_permlane16_swap_b32 v137, v139\n"
"v_pk_add_f32 v[136:137], v[136:137], v[138:139]\n"
"v_cndmask_b32 v138, v137, v136, s[0:1]\n"
"v_cndmask_b32 v136, v136, v137, s[0:1]\n"
"s_nop 1\n"
"v_add_f32 v136, v138, v136 row_ror:8 row_mask:0xf bank_mask:0xf\n"
"v_fmac_f32 v116, v136, v112\n"
"v_cvt_pk_bf16_f32 v116, v116, v116\n"
"buffer_store_short v116, v7, s[60:63], s88 offen offset:1024\n"
  :: "{s[0:1]}"(__builtin_amdgcn_kernarg_segment_ptr()), "{s2}"(blockIdx.x), "{v0}"(threadIdx.x), "{s100}"(lds)
  : "v1","v2","v3","v4","v5","v6","v7","v8","v9","v10","v11","v12","v13","v14","v15","v16","v17","v18","v19","v20","v21","v22","v23","v24","v25","v26","v27","v28","v29","v30","v31","v32","v33","v34","v35","v36","v37","v38","v39","v40","v41","v42","v43","v44","v45","v46","v47","v48","v49","v50","v51","v52","v53","v54","v55","v56","v57","v58","v59","v60","v61","v62","v63","v64","v65","v66","v67","v68","v69","v70","v71","v72","v73","v74","v75","v76","v77","v78","v79","v80","v81","v82","v83","v84","v85","v86","v87","v88","v89","v90","v91","v92","v93","v94","v95","v96","v97","v98","v99","v100","v101","v102","v103","v104","v105","v106","v107","v108","v109","v110","v111","v112","v113","v114","v115","v116","v117","v118","v119","v120","v121","v122","v123","v124","v125","v126","v127","v128","v129","v130","v131","v132","v133","v134","v135","v136","v137","v138","v139","v140","v141","v142","v143","v144","v145","v146","v147","v148","v149","v150","v151","v152","v153","v154","v155","v156","v157","v158","v159","v160","v161","v162","v163","v164","v165","v166","v167","v168","v169","v170","v171","v172","v173","v174","v175","v176","v177","v178","v179","v180","v181","v182","v183","v184","v185","v186","v187","v188","v189","v190","v191","v192","v193","v194","v195","v196","v197","v198","v199","v200","v201","v202","v203","v204","v205","v206","v207","v208","v209","v210","v211","v212","v213","v214","v215","v216","v217","v218","v219","v220","v221","v222","v223","v224","v225","v226","v227","v228","v229","v230","v231","v232","v233","v234","v235","v236","v237","v238","v239","v240","v241","v242","v243","v244","v245","v246","v247","v248","v249","v250","v251","v252","v253","v254","v255","s3","s4","s5","s6","s7","s8","s9","s10","s11","s12","s13","s14","s15","s16","s17","s18","s19","s20","s21","s22","s23","s24","s25","s26","s27","s28","s29","s30","s31","s32","s33","s34","s35","s36","s37","s38","s39","s40","s41","s42","s43","s44","s45","s46","s47","s48","s49","s50","s51","s52","s53","s54","s55","s56","s57","s58","s59","s60","s61","s62","s63","s64","s65","s66","s67","s68","s69","s70","s71","s72","s73","s74","s75","s76","s77","s78","s79","s80","s81","s82","s83","s84","s85","s86","s87","s88","s89","s90","s91","s92","s93","s94","s95","s96","s97","s98","s99","vcc","scc","memory");
}

extern "C" void kernel_launch(void* const* d_in, const int* in_sizes, int n_in, void* d_out, int out_size, void* d_ws,
                              size_t ws_size, hipStream_t stream) {
  fnet_glu_fused<<<256, 512, 0, stream>>>(d_in[0], d_in[1], d_in[2], d_in[3], d_in[4], d_in[6], d_in[7], d_in[8], d_out,
                                          d_ws);
}
```
